```python
import torch

def _softmax(x, axis=-1):
    return torch.softmax(x, dim=axis)

def _gelu(x):
    return torch.nn.functional.gelu(x, approximate='tanh')

def _silu(x):
    return torch.nn.functional.silu(x)

def _sigmoid(x):
    return torch.sigmoid(x)

def _layernorm(x, axis=-1, eps=1e-05):
    m = x.mean(dim=axis, keepdim=True)
    v = x.var(dim=axis, keepdim=True, unbiased=False)
    return (x - m) / torch.sqrt(v + eps)

def _scatter_add(tgt, idx, src, axis=0):
    return tgt.clone().index_add_(axis, idx, src)

def _segment_sum(src, seg, n):
    out = torch.zeros((n,) + src.shape[1:], dtype=src.dtype)
    return out.index_add_(0, seg, src)

def _pad(x, pw):
    flat = tuple((v for pair in reversed(pw) for v in pair))
    return torch.nn.functional.pad(x, flat)
kernel_name = 'adaLN_cond__ssm_conv_s4__dense_activation_relu__prompt'

def setup_inputs():

    _w = lambda a: (lambda t: t.to(torch.bfloat16) if t.is_floating_point() else t)(torch.as_tensor(a))

    def _A_setup():
        B, L, D, N = (1, 2048, 640, 128)
        A_re = -(lambda _x: torch.exp(torch.as_tensor(_x)))((lambda *s: torch.randn(*s, dtype=torch.bfloat16) * 3.2)(D, N))
        A_im = torch.pi * (lambda *s: torch.randn(*s, dtype=torch.bfloat16) * 0.1)(D, N)
        B_re = (lambda *s: torch.randn(*s, dtype=torch.bfloat16) * 0.2)(D, N)
        B_im = (lambda *s: torch.randn(*s, dtype=torch.bfloat16) * 0.2)(D, N)
        C_re = (lambda *s: torch.randn(*s, dtype=torch.bfloat16) * 0.2)(D, N)
        C_im = (lambda *s: torch.randn(*s, dtype=torch.bfloat16) * 0.2)(D, N)
        log_dt = (lambda *s: torch.randn(*s, dtype=torch.bfloat16) * 0.1)(D) * 0.5 - 4.0
        Dskip = (lambda *s: torch.randn(*s, dtype=torch.bfloat16) * 0.2)(D)
        return {'u': _w((lambda *s: torch.randn(*s, dtype=torch.bfloat16) * 0.05)(B, L, D)), 'A_re': _w(A_re), 'A_im': _w(A_im), 'B_re': _w(B_re), 'B_im': _w(B_im), 'C_re': _w(C_re), 'C_im': _w(C_im), 'log_dt': _w(log_dt), 'Dskip': _w(Dskip)}
    _A_in = _A_setup()

    def _B_setup():
        B, S, D, Dff = (1, 2048, 640, 1728)
        return {'x': _w((lambda *s: torch.randn(*s, dtype=torch.bfloat16) * 0.05)(B, S, D)), 'w1': _w((lambda *s: torch.randn(*s, dtype=torch.bfloat16) * 0.05)(D, Dff)), 'w2': _w((lambda *s: torch.randn(*s, dtype=torch.bfloat16) * 0.05)(Dff, D))}
    _B_in = _B_setup()
    return {'A_u': _A_in['u'], 'A_A_re': _A_in['A_re'], 'A_A_im': _A_in['A_im'], 'A_B_re': _A_in['B_re'], 'A_B_im': _A_in['B_im'], 'A_C_re': _A_in['C_re'], 'A_C_im': _A_in['C_im'], 'A_log_dt': _A_in['log_dt'], 'A_Dskip': _A_in['Dskip'], 'B_x': _B_in['x'], 'B_w1': _B_in['w1'], 'B_w2': _B_in['w2'], 'c': _w((lambda *s: torch.randn(*s, dtype=torch.bfloat16) * 0.1)(1, 640) * 8.0), 'w_mod': _w((lambda *s: torch.randn(*s, dtype=torch.bfloat16) * 0.1)(640, 6 * 640) * 640 ** (-0.5) * 8.0)}

def reference(A_u, A_A_re, A_A_im, A_B_re, A_B_im, A_C_re, A_C_im, A_log_dt, A_Dskip, B_x, B_w1, B_w2, c, w_mod):

    def _A_ref(u, A_re, A_im, B_re, B_im, C_re, C_im, log_dt, Dskip):
        Bsz, L, D = u.shape
        dt = torch.exp(log_dt)[:, None]
        A = A_re + 1j * A_im
        Bc = B_re + 1j * B_im
        Cc = C_re + 1j * C_im
        logdA = dt * A
        dB = dt * Bc
        l = (lambda x, dt: x.to(dt))(torch.arange(L), A_re.dtype)
        P = torch.exp(logdA[:, :, None] * l[None, None, :])
        K = torch.einsum('dn,dnl,dn->dl', Cc, P, dB).real
        L2 = 2 * L
        Kp = _pad(K, ((0, 0), (0, L)))
        up = (lambda x, *d: x.permute(*d))(u, 0, 2, 1)
        up = _pad(up, ((0, 0), (0, 0), (0, L)))
        Kf = (lambda x, axes=(-2, -1): torch.fft.rfft2(x.float(), dim=axes))(Kp[:, None, :], axes=(-2, -1))
        Uf = (lambda x, axes=(-2, -1): torch.fft.rfft2(x.float(), dim=axes))(up[:, :, None, :], axes=(-2, -1))
        Yf = Uf * Kf[None, :, :, :]
        y = (lambda x, s, axes=(-2, -1): torch.fft.irfft2(x, s=s, dim=axes).to(torch.bfloat16))(Yf, (1, L2), axes=(-2, -1))[:, :, 0, :L]
        y = (lambda x, *d: x.permute(*d))(y, 0, 2, 1) + u * Dskip[None, None, :]
        return y

    def _B_ref(x, w1, w2):
        h = torch.einsum('bsd,df->bsf', x, w1)
        zero = torch.zeros_like(h)
        h = torch.maximum(h, zero)
        return torch.einsum('bsf,fd->bsd', h, w2)
    x = A_u
    mod = (_silu(c) @ w_mod).reshape(c.shape[0], 6, 1, -1)
    sa, ca, ga, sb, cb, gb = (mod[:, 0].clone(), mod[:, 1].clone(), mod[:, 2].clone(), mod[:, 3].clone(), mod[:, 4].clone(), mod[:, 5].clone())
    h = _layernorm(x, axis=-1) * (1 + ca) + sa
    x = x + ga * _A_ref(h, A_A_re, A_A_im, A_B_re, A_B_im, A_C_re, A_C_im, A_log_dt, A_Dskip)
    h = _layernorm(x, axis=-1) * (1 + cb) + sb
    x = x + gb * _B_ref(h, B_w1, B_w2)
    return x
```

```cpp
#include <hip/hip_runtime.h>
#include <hip/hip_bf16.h>
#include <stdint.h>
#define ROUND_L 0
typedef unsigned short u16;
__device__ __forceinline__ float bf2f(u16 v){ return __uint_as_float(((unsigned)v)<<16); }
__device__ __forceinline__ u16 f2bf(float f){ unsigned u=__float_as_uint(f); u += 0x7FFFu + ((u>>16)&1u); return (u16)(u>>16); }
__device__ __forceinline__ float rb(float f){ return bf2f(f2bf(f)); }
#define L_ 2048
#define D_ 640
#define N_ 128
#define F_ 1728

__global__ void k_mod(const u16* c, const u16* w, float* mod){
  int j = blockIdx.x*blockDim.x+threadIdx.x; if(j>=6*D_) return;
  float acc=0;
  for(int k=0;k<D_;k++){ float cv=bf2f(c[k]); float s = rb(cv/(1.f+expf(-cv))); acc += s*bf2f(w[k*6*D_+j]); }
  mod[j]=rb(acc);
}
__global__ void k_ln(const float* x, const float* sc, const float* sh, float* h){
  int t = blockIdx.x; __shared__ float red[256];
  float s=0; for(int d=threadIdx.x; d<D_; d+=256) s+=x[t*D_+d];
  red[threadIdx.x]=s; __syncthreads();
  for(int o=128;o>0;o>>=1){ if(threadIdx.x<o) red[threadIdx.x]+=red[threadIdx.x+o]; __syncthreads(); }
  float mean=red[0]/D_; __syncthreads();
  float q=0; for(int d=threadIdx.x; d<D_; d+=256){ float v=x[t*D_+d]-mean; q+=v*v; }
  red[threadIdx.x]=q; __syncthreads();
  for(int o=128;o>0;o>>=1){ if(threadIdx.x<o) red[threadIdx.x]+=red[threadIdx.x+o]; __syncthreads(); }
  float var=rb(red[0]/D_); float mb=rb(mean);
  float sd=rb(sqrtf(rb(var+1e-5f)));
  for(int d=threadIdx.x; d<D_; d+=256){
    float n = rb(rb(x[t*D_+d]-mb)/sd);
    h[t*D_+d] = rb(rb(n*rb(1.f+sc[d]))+sh[d]);
  }
}
__global__ void k_cvt(const u16* a, float* b, int n){ int i=blockIdx.x*blockDim.x+threadIdx.x; if(i<n) b[i]=bf2f(a[i]); }
__global__ void k_kgen(const u16* Are,const u16* Aim,const u16* Bre,const u16* Bim,const u16* Cre,const u16* Cim,const u16* ldt, float* K){
  int d=blockIdx.y; int l=blockIdx.x*blockDim.x+threadIdx.x;
  float dt = rb(expf(bf2f(ldt[d])));
  float lf = ROUND_L ? rb((float)l) : (float)l;
  float acc=0;
  for(int n=0;n<N_;n++){
    float ar=dt*bf2f(Are[d*N_+n]), ai=dt*bf2f(Aim[d*N_+n]);
    float br=dt*bf2f(Bre[d*N_+n]), bi=dt*bf2f(Bim[d*N_+n]);
    float cr=bf2f(Cre[d*N_+n]), ci=bf2f(Cim[d*N_+n]);
    float e=expf(ar*lf); float sn,cs; sincosf(ai*lf,&sn,&cs);
    float pr=e*cs, pi=e*sn;
    float wr=cr*br-ci*bi, wi=cr*bi+ci*br;
    acc += wr*pr-wi*pi;
  }
  K[d*L_+l]=acc;
}
__global__ void k_conv(const float* K, const float* h, const float* x, const u16* Dsk, const float* ga, float* x1){
  int d=blockIdx.y; int l=blockIdx.x*blockDim.x+threadIdx.x;
  float acc=0;
  for(int m=0;m<=l;m++) acc += K[d*L_+m]*h[(l-m)*D_+d];
  float y=rb(acc);
  float hv=h[l*D_+d];
  y = rb(y + rb(hv*bf2f(Dsk[d])));
  x1[l*D_+d] = rb(x[l*D_+d] + rb(ga[d]*y));
}
__global__ void k_mlp1(const float* h, const u16* w1, float* hid){
  int t=blockIdx.y; int f=blockIdx.x*blockDim.x+threadIdx.x; if(f>=F_) return;
  float acc=0; for(int k=0;k<D_;k++) acc+=h[t*D_+k]*bf2f(w1[k*F_+f]);
  acc=rb(acc); hid[t*F_+f]=acc>0?acc:0;
}
__global__ void k_mlp2(const float* hid, const u16* w2, const float* x1, const float* gb, u16* out){
  int t=blockIdx.y; int d=blockIdx.x*blockDim.x+threadIdx.x; if(d>=D_) return;
  float acc=0; for(int k=0;k<F_;k++) acc+=hid[t*F_+k]*bf2f(w2[k*D_+d]);
  acc=rb(acc);
  out[t*D_+d]=f2bf(x1[t*D_+d]+rb(gb[d]*acc));
}
extern "C" void kernel_launch(void* const* d_in, const int* in_sizes, int n_in,
                              void* d_out, int out_size, void* d_ws, size_t ws_size,
                              hipStream_t stream) {
  const u16* u=(const u16*)d_in[0];
  float* ws=(float*)d_ws;
  float* mod=ws; float* x=ws+4096; float* h=x+L_*D_; float* K=h+L_*D_; float* x1=K+L_*D_; float* h2=x1+L_*D_; float* hid=h2+L_*D_;
  k_mod<<<15,256,0,stream>>>((const u16*)d_in[12],(const u16*)d_in[13],mod);
  k_cvt<<<L_*D_/256,256,0,stream>>>(u,x,L_*D_);
  k_ln<<<L_,256,0,stream>>>(x,mod+D_,mod,h);
  k_kgen<<<dim3(L_/256,D_),256,0,stream>>>((const u16*)d_in[1],(const u16*)d_in[2],(const u16*)d_in[3],(const u16*)d_in[4],(const u16*)d_in[5],(const u16*)d_in[6],(const u16*)d_in[7],K);
  k_conv<<<dim3(L_/256,D_),256,0,stream>>>(K,h,x,(const u16*)d_in[8],mod+2*D_,x1);
  k_ln<<<L_,256,0,stream>>>(x1,mod+4*D_,mod+3*D_,h2);
  k_mlp1<<<dim3(7,L_),256,0,stream>>>(h2,(const u16*)d_in[10],hid);
  k_mlp2<<<dim3(3,L_),256,0,stream>>>(hid,(const u16*)d_in[11],x1,mod+5*D_,(u16*)d_out);
}
```

```cpp
#include <hip/hip_runtime.h>
#include <hip/hip_bf16.h>
#include <stdint.h>
#define ROUND_L 1
typedef unsigned short u16;
__device__ __forceinline__ float bf2f(u16 v){ return __uint_as_float(((unsigned)v)<<16); }
__device__ __forceinline__ u16 f2bf(float f){ unsigned u=__float_as_uint(f); u += 0x7FFFu + ((u>>16)&1u); return (u16)(u>>16); }
__device__ __forceinline__ float rb(float f){ return f; }
__device__ __forceinline__ float rbl(float f){ return bf2f(f2bf(f)); }
#define L_ 2048
#define D_ 640
#define N_ 128
#define F_ 1728

__global__ void k_mod(const u16* c, const u16* w, float* mod){
  int j = blockIdx.x*blockDim.x+threadIdx.x; if(j>=6*D_) return;
  float acc=0;
  for(int k=0;k<D_;k++){ float cv=bf2f(c[k]); float s = rb(cv/(1.f+expf(-cv))); acc += s*bf2f(w[k*6*D_+j]); }
  mod[j]=rb(acc);
}
__global__ void k_ln(const float* x, const float* sc, const float* sh, float* h){
  int t = blockIdx.x; __shared__ float red[256];
  float s=0; for(int d=threadIdx.x; d<D_; d+=256) s+=x[t*D_+d];
  red[threadIdx.x]=s; __syncthreads();
  for(int o=128;o>0;o>>=1){ if(threadIdx.x<o) red[threadIdx.x]+=red[threadIdx.x+o]; __syncthreads(); }
  float mean=red[0]/D_; __syncthreads();
  float q=0; for(int d=threadIdx.x; d<D_; d+=256){ float v=x[t*D_+d]-mean; q+=v*v; }
  red[threadIdx.x]=q; __syncthreads();
  for(int o=128;o>0;o>>=1){ if(threadIdx.x<o) red[threadIdx.x]+=red[threadIdx.x+o]; __syncthreads(); }
  float var=rb(red[0]/D_); float mb=rb(mean);
  float sd=rb(sqrtf(rb(var+1e-5f)));
  for(int d=threadIdx.x; d<D_; d+=256){
    float n = rb(rb(x[t*D_+d]-mb)/sd);
    h[t*D_+d] = rb(rb(n*rb(1.f+sc[d]))+sh[d]);
  }
}
__global__ void k_cvt(const u16* a, float* b, int n){ int i=blockIdx.x*blockDim.x+threadIdx.x; if(i<n) b[i]=bf2f(a[i]); }
__global__ void k_kgen(const u16* Are,const u16* Aim,const u16* Bre,const u16* Bim,const u16* Cre,const u16* Cim,const u16* ldt, float* K){
  int d=blockIdx.y; int l=blockIdx.x*blockDim.x+threadIdx.x;
  float dt = rb(expf(bf2f(ldt[d])));
  float lf = ROUND_L ? rbl((float)l) : (float)l;
  float acc=0;
  for(int n=0;n<N_;n++){
    float ar=dt*bf2f(Are[d*N_+n]), ai=dt*bf2f(Aim[d*N_+n]);
    float br=dt*bf2f(Bre[d*N_+n]), bi=dt*bf2f(Bim[d*N_+n]);
    float cr=bf2f(Cre[d*N_+n]), ci=bf2f(Cim[d*N_+n]);
    float e=expf(ar*lf); float sn,cs; sincosf(ai*lf,&sn,&cs);
    float pr=e*cs, pi=e*sn;
    float wr=cr*br-ci*bi, wi=cr*bi+ci*br;
    acc += wr*pr-wi*pi;
  }
  K[d*L_+l]=acc;
}
__global__ void k_conv(const float* K, const float* h, const float* x, const u16* Dsk, const float* ga, float* x1){
  int d=blockIdx.y; int l=blockIdx.x*blockDim.x+threadIdx.x;
  float acc=0;
  for(int m=0;m<=l;m++) acc += K[d*L_+m]*h[(l-m)*D_+d];
  float y=rb(acc);
  float hv=h[l*D_+d];
  y = rb(y + rb(hv*bf2f(Dsk[d])));
  x1[l*D_+d] = rb(x[l*D_+d] + rb(ga[d]*y));
}
__global__ void k_mlp1(const float* h, const u16* w1, float* hid){
  int t=blockIdx.y; int f=blockIdx.x*blockDim.x+threadIdx.x; if(f>=F_) return;
  float acc=0; for(int k=0;k<D_;k++) acc+=h[t*D_+k]*bf2f(w1[k*F_+f]);
  acc=rb(acc); hid[t*F_+f]=acc>0?acc:0;
}
__global__ void k_mlp2(const float* hid, const u16* w2, const float* x1, const float* gb, u16* out){
  int t=blockIdx.y; int d=blockIdx.x*blockDim.x+threadIdx.x; if(d>=D_) return;
  float acc=0; for(int k=0;k<F_;k++) acc+=hid[t*F_+k]*bf2f(w2[k*D_+d]);
  acc=rb(acc);
  out[t*D_+d]=f2bf(x1[t*D_+d]+rb(gb[d]*acc));
}
extern "C" void kernel_launch(void* const* d_in, const int* in_sizes, int n_in,
                              void* d_out, int out_size, void* d_ws, size_t ws_size,
                              hipStream_t stream) {
  const u16* u=(const u16*)d_in[0];
  float* ws=(float*)d_ws;
  float* mod=ws; float* x=ws+4096; float* h=x+L_*D_; float* K=h+L_*D_; float* x1=K+L_*D_; float* h2=x1+L_*D_; float* hid=h2+L_*D_;
  k_mod<<<15,256,0,stream>>>((const u16*)d_in[12],(const u16*)d_in[13],mod);
  k_cvt<<<L_*D_/256,256,0,stream>>>(u,x,L_*D_);
  k_ln<<<L_,256,0,stream>>>(x,mod+D_,mod,h);
  k_kgen<<<dim3(L_/256,D_),256,0,stream>>>((const u16*)d_in[1],(const u16*)d_in[2],(const u16*)d_in[3],(const u16*)d_in[4],(const u16*)d_in[5],(const u16*)d_in[6],(const u16*)d_in[7],K);
  k_conv<<<dim3(L_/256,D_),256,0,stream>>>(K,h,x,(const u16*)d_in[8],mod+2*D_,x1);
  k_ln<<<L_,256,0,stream>>>(x1,mod+4*D_,mod+3*D_,h2);
  k_mlp1<<<dim3(7,L_),256,0,stream>>>(h2,(const u16*)d_in[10],hid);
  k_mlp2<<<dim3(3,L_),256,0,stream>>>(hid,(const u16*)d_in[11],x1,mod+5*D_,(u16*)d_out);
}
```

```cpp
#include <hip/hip_runtime.h>
#include <hip/hip_bf16.h>
#include <stdint.h>

typedef unsigned short u16;
typedef unsigned int u32;
typedef __attribute__((ext_vector_type(8))) short bf16x8;
typedef __attribute__((ext_vector_type(4))) short s16x4;
typedef __attribute__((ext_vector_type(4))) float f32x4;
typedef __attribute__((ext_vector_type(2))) float f32x2;
typedef __attribute__((ext_vector_type(4))) unsigned int u32x4;
typedef __attribute__((ext_vector_type(2))) unsigned int u32x2;
typedef __bf16 bf16x2_t __attribute__((ext_vector_type(2)));
#define LDS_AS __attribute__((address_space(3)))
#define DI __device__ __forceinline__

constexpr int L_ = 2048, D_ = 640, N_ = 128, F_ = 1728;
constexpr int KSM = 8;

constexpr size_t WS_MODP = 0;
constexpr size_t WS_MODF = 131072;
constexpr size_t WS_NT = 262144;
constexpr size_t WS_XT = WS_NT + (size_t)D_ * L_ * 2;
constexpr size_t WS_X1T = WS_XT + (size_t)D_ * L_ * 2;
constexpr int KTAB = 1296;
constexpr size_t WS_KT = WS_X1T + (size_t)D_ * L_ * 2;
constexpr size_t WS_SB = WS_KT + (size_t)D_ * KTAB;
constexpr size_t WS_W1F = WS_SB + 2 * 4 * 1728 * 4;
constexpr size_t WS_W2F = WS_W1F + (size_t)108 * 20 * 1024;
constexpr size_t WS_HID = WS_W2F + (size_t)40 * 54 * 1024;

#define TS(kern, slot) do {} while (0)

DI u32 cvtpk(float lo, float hi) {
  f32x2 v = {lo, hi};
  bf16x2_t r = __builtin_convertvector(v, bf16x2_t);
  return __builtin_bit_cast(u32, r);
}
DI float lo2f(u32 v) { return __uint_as_float(v << 16); }
DI float hi2f(u32 v) { return __uint_as_float(v & 0xffff0000u); }
DI float bf2f(u16 v) { return __uint_as_float(((u32)v) << 16); }
DI float rb(float x) { return lo2f(cvtpk(x, x)); }
DI f32x4 mfma16(bf16x8 a, bf16x8 b, f32x4 c) { return __builtin_amdgcn_mfma_f32_16x16x32_bf16(a, b, c, 0, 0, 0); }
DI s16x4 tr_read(const char* p) {
  return __builtin_amdgcn_ds_read_tr16_b64_v4i16((LDS_AS s16x4*)p);
}
DI bf16x8 cat8(s16x4 a, s16x4 b) { return __builtin_shufflevector(a, b, 0, 1, 2, 3, 4, 5, 6, 7); }
DI void wave_sync() {
  __builtin_amdgcn_fence(__ATOMIC_RELEASE, "wavefront");
  __builtin_amdgcn_wave_barrier();
  __builtin_amdgcn_fence(__ATOMIC_ACQUIRE, "wavefront");
}

struct c32 { float r, i; };
DI c32 cmul(c32 a, c32 b) { return {a.r * b.r - a.i * b.i, a.r * b.i + a.i * b.r}; }
DI c32 cexp_scaled(float ar, float ai, float e) {
  const float mag = __expf(ar * e);
  const float x = ai * e;
  const float C_HI = 0.15915494f, C_LO = 6.4206382e-9f;
  const float r = x * C_HI;
  const float rl = fmaf(x, C_HI, -r) + x * C_LO;
  const float fr = (r - floorf(r)) + rl;
  return {mag * __builtin_amdgcn_cosf(fr), mag * __builtin_amdgcn_sinf(fr)};
}
DI int tap_index(int m) {
  u32 bits = __float_as_uint((float)m);
  bits += 0x7fffu + ((bits >> 16) & 1u);
  const int id2 = (int)(bits >> 16) - (0x4300 - 128);
  return m < 128 ? m : id2;
}

constexpr int NB_LN = 64, NB_KG = 320, NB_MOD = 30 * KSM;
constexpr int PRE_SMEM = 42240;

DI void role_ln1(int b, const u16* __restrict__ x, u16* __restrict__ nT, u16* __restrict__ xT, char* smem) {
  const int tid = threadIdx.x, u = tid >> 4, j = tid & 15;
  const int t0 = b * 32;
  const u32x4* xa = (const u32x4*)(x + (size_t)(t0 + 2 * u) * D_);
  u32x4 A[5], B[5];
#pragma unroll
  for (int q = 0; q < 5; ++q) { A[q] = xa[j + 16 * q]; B[q] = xa[80 + j + 16 * q]; }
  float sa = 0.f, sb = 0.f;
#pragma unroll
  for (int q = 0; q < 5; ++q)
#pragma unroll
    for (int k = 0; k < 4; ++k) { sa += lo2f(A[q][k]) + hi2f(A[q][k]); sb += lo2f(B[q][k]) + hi2f(B[q][k]); }
#pragma unroll
  for (int o = 1; o < 16; o <<= 1) { sa += __shfl_xor(sa, o, 16); sb += __shfl_xor(sb, o, 16); }
  const float ma = sa * (1.f / D_), mb = sb * (1.f / D_);
  float va = 0.f, vb = 0.f;
#pragma unroll
  for (int q = 0; q < 5; ++q)
#pragma unroll
    for (int k = 0; k < 4; ++k) {
      float d0 = lo2f(A[q][k]) - ma, d1 = hi2f(A[q][k]) - ma, d2 = lo2f(B[q][k]) - mb, d3 = hi2f(B[q][k]) - mb;
      va += d0 * d0 + d1 * d1; vb += d2 * d2 + d3 * d3;
    }
#pragma unroll
  for (int o = 1; o < 16; o <<= 1) { va += __shfl_xor(va, o, 16); vb += __shfl_xor(vb, o, 16); }
  const float mra = rb(ma), mrb = rb(mb);
  const float ra = 1.f / rb(sqrtf(rb(rb(va * (1.f / D_)) + 1e-5f)));
  const float rbb = 1.f / rb(sqrtf(rb(rb(vb * (1.f / D_)) + 1e-5f)));
  char* wbase = smem + (8 * j + (j >> 2)) * 64 + ((((u >> 2) ^ (j & 3)) << 2) + (u & 3)) * 4;
#pragma unroll
  for (int q = 0; q < 5; ++q)
#pragma unroll
    for (int k = 0; k < 4; ++k) {
      const u32 c0 = cvtpk(lo2f(A[q][k]) - mra, lo2f(B[q][k]) - mrb);
      const u32 c1 = cvtpk(hi2f(A[q][k]) - mra, hi2f(B[q][k]) - mrb);
      *(u32*)(wbase + (132 * q + 2 * k) * 64) = cvtpk(lo2f(c0) * ra, hi2f(c0) * rbb);
      *(u32*)(wbase + (132 * q + 2 * k + 1) * 64) = cvtpk(lo2f(c1) * ra, hi2f(c1) * rbb);
    }
  __syncthreads();
#pragma unroll
  for (int i = 0; i < 10; ++i) {
    const int id = tid + 256 * i, c = id >> 2, o = id & 3;
    const u32x4 v = *(const u32x4*)(smem + (c + (c >> 5)) * 64 + ((o ^ ((c >> 3) & 3)) << 4));
    *(u32x4*)(nT + (size_t)c * L_ + t0 + 8 * o) = v;
  }
  __syncthreads();
#pragma unroll
  for (int q = 0; q < 5; ++q)
#pragma unroll
    for (int k = 0; k < 4; ++k) {
      *(u32*)(wbase + (132 * q + 2 * k) * 64) = (A[q][k] & 0xffffu) | (B[q][k] << 16);
      *(u32*)(wbase + (132 * q + 2 * k + 1) * 64) = (A[q][k] >> 16) | (B[q][k] & 0xffff0000u);
    }
  __syncthreads();
#pragma unroll
  for (int i = 0; i < 10; ++i) {
    const int id = tid + 256 * i, c = id >> 2, o = id & 3;
    const u32x4 v = *(const u32x4*)(smem + (c + (c >> 5)) * 64 + ((o ^ ((c >> 3) & 3)) << 4));
    *(u32x4*)(xT + (size_t)c * L_ + t0 + 8 * o) = v;
  }
}

DI void role_kgen(int bk, const u16* __restrict__ Are, const u16* __restrict__ Aim, const u16* __restrict__ Bre,
                  const u16* __restrict__ Bim, const u16* __restrict__ Cre, const u16* __restrict__ Cim,
                  const u16* __restrict__ ldt, char* __restrict__ KT, char* smem) {
  const int tid = threadIdx.x, w = tid >> 6, lane = tid & 63, r16 = lane & 15, g = lane >> 4;
  const int d = 2 * bk + (w >> 1), n = (w & 1) * 64 + lane;
  const int gi = d * N_ + n;
  const float dt = rb(expf(bf2f(ldt[d])));
  const float ar = dt * bf2f(Are[gi]), ai = dt * bf2f(Aim[gi]);
  const c32 dB = {dt * bf2f(Bre[gi]), dt * bf2f(Bim[gi])};
  const c32 Cc = {bf2f(Cre[gi]), bf2f(Cim[gi])};
  const c32 W = cmul(Cc, dB);
  const c32 E1 = cexp_scaled(ar, ai, 1.f), E8 = cexp_scaled(ar, ai, 8.f), E128 = cexp_scaled(ar, ai, 128.f);

  char* buf = smem + w * 8704;
  char* wr = buf + lane * 4;
  const char* rd = buf + r16 * 272 + g * 16;
  auto put = [&](int row, float re, float im) {
    const u32 hi = cvtpk(re, im);
    const u32 lo = cvtpk(re - lo2f(hi), im - hi2f(hi));
    *(u32*)(wr + row * 272) = hi;
    *(u32*)(wr + 4352 + row * 272) = lo;
  };
  {
    c32 p = {1.f, 0.f};
#pragma unroll
    for (int jj = 0; jj < 16; ++jj) { put(jj, p.r, p.i); if (jj < 15) p = cmul(p, E8); }
  }
  wave_sync();
  bf16x8 bh[4], bl[4];
#pragma unroll
  for (int ks = 0; ks < 4; ++ks) { bh[ks] = *(const bf16x8*)(rd + ks * 64); bl[ks] = *(const bf16x8*)(rd + 4352 + ks * 64); }
  wave_sync();
  c32 sm[8];
  sm[0] = {1.f, 0.f}; sm[1] = E1;
#pragma unroll
  for (int i = 2; i < 8; ++i) sm[i] = cmul(sm[i - 1], E1);
  c32 WG[17];
  WG[0] = W;
#pragma unroll
  for (int i = 1; i < 17; ++i) WG[i] = cmul(WG[i - 1], E128);
  f32x4 acc[3];
  auto tile_mma = [&](int t) {
    wave_sync();
    f32x4 a = {0.f, 0.f, 0.f, 0.f};
#pragma unroll
    for (int ks = 0; ks < 4; ++ks) {
      const bf16x8 ah = *(const bf16x8*)(rd + ks * 64), al = *(const bf16x8*)(rd + 4352 + ks * 64);
      a = mfma16(al, bh[ks], a); a = mfma16(ah, bl[ks], a); a = mfma16(ah, bh[ks], a);
    }
    acc[t] = a;
    wave_sync();
  };
#pragma unroll
  for (int i = 0; i < 2; ++i)
#pragma unroll
    for (int rho = 0; rho < 8; ++rho) { const c32 v = rho ? cmul(WG[i], sm[rho]) : WG[i]; put(8 * i + rho, v.r, -v.i); }
  tile_mma(0);
#pragma unroll
  for (int i = 0; i < 2; ++i)
#pragma unroll
    for (int rho = 0; rho < 4; ++rho) { const c32 v = rho ? cmul(WG[2 + i], sm[2 * rho]) : WG[2 + i]; put(4 * i + rho, v.r, -v.i); }
#pragma unroll
  for (int i = 0; i < 4; ++i)
#pragma unroll
    for (int rho = 0; rho < 2; ++rho) { const c32 v = rho ? cmul(WG[4 + i], sm[4]) : WG[4 + i]; put(8 + 2 * i + rho, v.r, -v.i); }
  tile_mma(1);
#pragma unroll
  for (int i = 0; i < 9; ++i) put(i, WG[8 + i].r, -WG[8 + i].i);
  tile_mma(2);

  if (w & 1) {
#pragma unroll
    for (int t = 0; t < 3; ++t) *(f32x4*)(buf + t * 1024 + lane * 16) = acc[t];
  }
  __syncthreads();
  u16* T = (u16*)(smem + (w >> 1) * 2 * 8704);
  if (!(w & 1)) {
    const char* ob = buf + 8704;
#pragma unroll
    for (int t = 0; t < 3; ++t) acc[t] += *(const f32x4*)(ob + t * 1024 + lane * 16);
#pragma unroll
    for (int t = 0; t < 3; ++t)
#pragma unroll
      for (int rg = 0; rg < 4; ++rg) {
        const int row = 4 * g + rg;
        int base;
        if (t == 0) base = 128 * (row >> 3) + (row & 7);
        else if (t == 1) base = row < 8 ? 256 + 128 * (row >> 2) + 2 * (row & 3) : 512 + 128 * ((row - 8) >> 1) + 4 * (row & 1);
        else base = 1024 + 128 * row;
        const int e = base + 8 * r16;
        if (e <= 2048) T[tap_index(e)] = (u16)(cvtpk(acc[t][rg], 0.f) & 0xffffu);
      }
    if (lane < 7) T[641 + lane] = 0;
  }
  __syncthreads();
  const int t128 = tid & 127;
  if (t128 < 81) *(u32x4*)(KT + (size_t)d * KTAB + t128 * 16) = *(const u32x4*)((const char*)T + t128 * 16);
}

DI void role_mod(int bm, const u16* __restrict__ c, const u16* __restrict__ wm, float* __restrict__ modp, char* smem) {
  const int tid = threadIdx.x, cj = tid & 15, rg = tid >> 4, w = tid >> 6;
  const int cbk = bm % 30, ks = bm / 30;
  u32x4 v[5];
  float s[5];
#pragma unroll
  for (int i = 0; i < 5; ++i) {
    const int row = 80 * ks + rg + 16 * i;
    v[i] = *(const u32x4*)(wm + (size_t)row * (6 * D_) + 128 * cbk + 8 * cj);
    const float cv = bf2f(c[row]);
    s[i] = rb(cv / (1.f + expf(-cv)));
  }
  float a[8];
#pragma unroll
  for (int k = 0; k < 8; ++k) a[k] = 0.f;
#pragma unroll
  for (int i = 0; i < 5; ++i)
#pragma unroll
    for (int k = 0; k < 4; ++k) { a[2 * k] += s[i] * lo2f(v[i][k]); a[2 * k + 1] += s[i] * hi2f(v[i][k]); }
#pragma unroll
  for (int k = 0; k < 8; ++k) { a[k] += __shfl_xor(a[k], 16); a[k] += __shfl_xor(a[k], 32); }
  float* red = (float*)smem;
  if ((tid & 63) < 16) {
#pragma unroll
    for (int k = 0; k < 8; ++k) red[w * 128 + 8 * cj + k] = a[k];
  }
  __syncthreads();
  if (tid < 128) modp[ks * (6 * D_) + 128 * cbk + tid] = red[tid] + red[128 + tid] + red[256 + tid] + red[384 + tid];
}

DI int rl_swz(int row) { return (row & 3) | ((((row >> 2) ^ (row >> 3)) & 1) << 2); }

__global__ __launch_bounds__(256) void k_pre(const u16* __restrict__ x, const u16* __restrict__ Are, const u16* __restrict__ Aim,
                                             const u16* __restrict__ Bre, const u16* __restrict__ Bim, const u16* __restrict__ Cre,
                                             const u16* __restrict__ Cim, const u16* __restrict__ ldt,
                                             const u16* __restrict__ c, const u16* __restrict__ wm, char* __restrict__ ws) {
  __shared__ __attribute__((aligned(16))) char smem[PRE_SMEM];
  int b = blockIdx.x;
  TS(0, 0);
  if (b < NB_LN) { role_ln1(b, x, (u16*)(ws + WS_NT), (u16*)(ws + WS_XT), smem); TS(0, 7); return; }
  b -= NB_LN;
  if (b < NB_KG) { role_kgen(b, Are, Aim, Bre, Bim, Cre, Cim, ldt, ws + WS_KT, smem); TS(0, 7); return; }
  b -= NB_KG;
  role_mod(b, c, wm, (float*)(ws + WS_MODP), smem);
  TS(0, 7);
}

constexpr int CV_H0 = 0, CV_HS = 4160, CV_K = 12544, CV_T = 21760, CV_H3 = 24768 + 2816, CV_CH = CV_H3 + 4160;
constexpr int CV_SMEM = 2 * CV_CH + 40960;
static_assert(CV_CH % 256 == 0, "channel regions keep bank phase");
static_assert(CV_T + 4 * KTAB <= CV_H3, "tap tables fit");
static_assert(3 * CV_CH <= CV_SMEM, "lds");

DI float mod_final(const float* __restrict__ modp, int j) {
  float a = 0.f;
#pragma unroll
  for (int k = 0; k < KSM; ++k) a += modp[k * (6 * D_) + j];
  return rb(a);
}

DI void relayout_unit(int u, const u16* __restrict__ w1, const u16* __restrict__ w2, char* __restrict__ ws, char* buf, int lane) {
  const int g = lane >> 4, q = (lane & 15) >> 2, p = lane & 3;
  if (u < 216) {
    const int pr = u % 54, kq = u / 54;
    const int lr = lane >> 2, lc = lane & 3;
    const float* modp = (const float*)(ws + WS_MODP);
    u32x4 v[10];
    float cb1[10], sbv[10];
#pragma unroll
    for (int i = 0; i < 10; ++i) v[i] = *(const u32x4*)(w1 + (size_t)(160 * kq + 16 * i + lr) * F_ + 32 * pr + 8 * lc);
#pragma unroll
    for (int i = 0; i < 10; ++i) {
      const int row = 160 * kq + 16 * i + lr;
      float a = modp[(2 * lc) * (6 * D_) + 4 * D_ + row] + modp[(2 * lc + 1) * (6 * D_) + 4 * D_ + row];
      float bsum = modp[(2 * lc) * (6 * D_) + 3 * D_ + row] + modp[(2 * lc + 1) * (6 * D_) + 3 * D_ + row];
      cb1[i] = a; sbv[i] = bsum;
    }
    float S[8], Bi[8];
#pragma unroll
    for (int k = 0; k < 8; ++k) { S[k] = 0.f; Bi[k] = 0.f; }
#pragma unroll
    for (int i = 0; i < 10; ++i) {
      float a = cb1[i], bsum = sbv[i];
      a += __shfl_xor(a, 1); a += __shfl_xor(a, 2);
      bsum += __shfl_xor(bsum, 1); bsum += __shfl_xor(bsum, 2);
      const float c1 = rb(1.f + rb(a)), sb = rb(bsum);
      u32x4 o;
#pragma unroll
      for (int k = 0; k < 4; ++k) {
        const float w0 = lo2f(v[i][k]), w1v = hi2f(v[i][k]);
        o[k] = cvtpk(w0 * c1, w1v * c1);
        S[2 * k] += lo2f(o[k]); S[2 * k + 1] += hi2f(o[k]);
        Bi[2 * k] += sb * w0; Bi[2 * k + 1] += sb * w1v;
      }
      const int row = 16 * i + lr;
      *(u32x4*)(buf + row * 64 + ((lc ^ ((row >> 1) & 3)) << 4)) = o;
    }
#pragma unroll
    for (int k = 0; k < 8; ++k) {
#pragma unroll
      for (int o = 4; o < 64; o <<= 1) { S[k] += __shfl_xor(S[k], o); Bi[k] += __shfl_xor(Bi[k], o); }
    }
    if (lane < 4) {
      float* sp = (float*)(ws + WS_SB) + kq * F_ + 32 * pr + 8 * lane;
      *(f32x4*)(sp) = f32x4{S[0], S[1], S[2], S[3]}; *(f32x4*)(sp + 4) = f32x4{S[4], S[5], S[6], S[7]};
      *(f32x4*)(sp + 4 * F_) = f32x4{Bi[0], Bi[1], Bi[2], Bi[3]}; *(f32x4*)(sp + 4 * F_ + 4) = f32x4{Bi[4], Bi[5], Bi[6], Bi[7]};
    }
    __syncthreads();
#pragma unroll
    for (int ks = 0; ks < 5; ++ks)
#pragma unroll
      for (int cb = 0; cb < 2; ++cb) {
        const int r0 = 32 * ks + 8 * g + q, r1 = r0 + 4;
        const int ch = 2 * cb + (p >> 1);
        const s16x4 lo = tr_read(buf + r0 * 64 + ((ch ^ ((r0 >> 1) & 3)) << 4) + 8 * (p & 1));
        const s16x4 hi = tr_read(buf + r1 * 64 + ((ch ^ ((r1 >> 1) & 3)) << 4) + 8 * (p & 1));
        *(bf16x8*)(ws + WS_W1F + ((size_t)(2 * pr + cb) * 20 + 5 * kq + ks) * 1024 + lane * 16) = cat8(lo, hi);
      }
  } else if (u < 486) {
    const int t2 = u - 216, ks = t2 / 5, nb = t2 % 5;
    const int lr = lane >> 4, lc = lane & 15;
    u32x4 v[8];
#pragma unroll
    for (int i = 0; i < 8; ++i) v[i] = *(const u32x4*)(w2 + (size_t)(32 * ks + 4 * i + lr) * D_ + 128 * nb + 8 * lc);
#pragma unroll
    for (int i = 0; i < 8; ++i) {
      const int row = 4 * i + lr;
      *(u32x4*)(buf + row * 256 + (((lc >> 1) ^ rl_swz(row)) << 5) + ((lc & 1) << 4)) = v[i];
    }
    __syncthreads();
    const int row0 = 4 * g + q, row1 = row0 + 16;
#pragma unroll
    for (int cb = 0; cb < 8; ++cb) {
      const s16x4 lo = tr_read(buf + row0 * 256 + ((cb ^ rl_swz(row0)) << 5) + 8 * p);
      const s16x4 hi = tr_read(buf + row1 * 256 + ((cb ^ rl_swz(row1)) << 5) + 8 * p);
      *(bf16x8*)(ws + WS_W2F + ((size_t)(8 * nb + cb) * 54 + ks) * 1024 + lane * 16) = cat8(lo, hi);
    }
  } else {
    __syncthreads();
  }
  __syncthreads();
  __syncthreads();
}

__global__ __launch_bounds__(768) void k_conv(const u16* __restrict__ Dsk, const u16* __restrict__ w1, const u16* __restrict__ w2,
                                              char* __restrict__ ws) {
  __shared__ __attribute__((aligned(256))) char smem_all[CV_SMEM];
  const int wv = __builtin_amdgcn_readfirstlane(threadIdx.x >> 6), lane = threadIdx.x & 63, r16 = lane & 15, g = lane >> 4;
  const int b = blockIdx.x, cw = wv >> 2, w = wv & 3, tid = threadIdx.x & 255;
  TS(1, 0);
  if (b >= 128 && cw == 2) {
    relayout_unit((b - 128) * 4 + w, w1, w2, ws, smem_all + 2 * CV_CH + w * 10240, lane);
    return;
  }
  const int d = b < 128 ? 3 * b + cw : 384 + 2 * (b - 128) + cw;
  char* smem = smem_all + cw * CV_CH;
  const u16* nT = (const u16*)(ws + WS_NT) + (size_t)d * L_;
  const u16* xT = (const u16*)(ws + WS_XT) + (size_t)d * L_;
  u16* x1T = (u16*)(ws + WS_X1T) + (size_t)d * L_;
  const char* ktab = ws + WS_KT + (size_t)d * KTAB;
  const u32x4 tv0 = *(const u32x4*)(ktab + lane * 16);
  const u32x4 tv1 = *(const u32x4*)(ktab + (lane < 17 ? 64 + lane : 0) * 16);
  float mp = ((const float*)(ws + WS_MODP))[(lane & 7) * (6 * D_) + (lane < 48 ? lane >> 3 : 0) * D_ + d];
  const u32x4 nq0 = *(const u32x4*)(nT + 8 * (tid > 0 ? tid - 1 : 0));
  const u32x4 nq1 = *(const u32x4*)(nT + 8 * tid);
  u32 xv[2][2];
#pragma unroll
  for (int k = 0; k < 2; ++k) {
    const int i = k ? 7 - w : w;
    xv[k][0] = *(const u32*)(xT + 256 * i + 64 * g + 2 * r16);
    xv[k][1] = *(const u32*)(xT + 256 * i + 64 * g + 2 * r16 + 32);
  }
  const float dsk = bf2f(Dsk[d]);
  {
    char* tb = smem + CV_T + w * KTAB;
    *(u32x4*)(tb + lane * 16) = tv0;
    if (lane < 17) *(u32x4*)(tb + (64 + lane) * 16) = tv1;
    wave_sync();
    const u16* T = (const u16*)tb;
#pragma unroll
    for (int i = 0; i < 2; ++i) {
      const int cp = tid + 256 * i;
      if (cp < 284) {
        u32 e[9];
#pragma unroll
        for (int j = 0; j < 9; ++j) {
          const int m = 2047 - 8 * cp - j;
          e[j] = T[m < 0 ? 641 : tap_index(m)];
        }
        *(u32x4*)(smem + CV_K + 4576 + cp * 16) = u32x4{e[0] | (e[1] << 16), e[2] | (e[3] << 16), e[4] | (e[5] << 16), e[6] | (e[7] << 16)};
        *(u32x4*)(smem + CV_K + cp * 16) = u32x4{e[1] | (e[2] << 16), e[3] | (e[4] << 16), e[5] | (e[6] << 16), e[7] | (e[8] << 16)};
      }
    }
  }
  mp += __shfl_xor(mp, 1); mp += __shfl_xor(mp, 2); mp += __shfl_xor(mp, 4);
  mp = rb(mp);
  const float sa = __shfl(mp, 0), ca = __shfl(mp, 8), ga = __shfl(mp, 16);
  if (w == 0 && lane == 40) ((float*)(ws + WS_MODF))[2 * D_ + d] = mp;
  const float ca1 = rb(1.f + ca);
  {
    u32 h[8];
#pragma unroll
    for (int k = 0; k < 4; ++k) {
      const u32 p0 = cvtpk(lo2f(nq0[k]) * ca1, hi2f(nq0[k]) * ca1);
      const u32 p1 = cvtpk(lo2f(nq1[k]) * ca1, hi2f(nq1[k]) * ca1);
      const u32 h0 = cvtpk(lo2f(p0) + sa, hi2f(p0) + sa);
      h[k] = tid > 0 ? h0 : 0u;
      h[4 + k] = cvtpk(lo2f(p1) + sa, hi2f(p1) + sa);
    }
    const int yo = (tid + 3) * 16;
    *(u32x4*)(smem + CV_H0 + yo) = u32x4{h[1], h[2], h[3], h[4]};
    *(u32x4*)(smem + CV_H0 + CV_HS + yo) = u32x4{h[2], h[3], h[4], h[5]};
    *(u32x4*)(smem + CV_H0 + 2 * CV_HS + yo) = u32x4{h[3], h[4], h[5], h[6]};
    *(u32x4*)(smem + CV_H3 + yo) = u32x4{h[4], h[5], h[6], h[7]};
    if (tid < 12) {
      const int cc = tid / 3, yc = tid % 3;
      *(u32x4*)(smem + (cc == 3 ? CV_H3 : CV_H0 + cc * CV_HS) + yc * 16) = u32x4{0u, 0u, 0u, 0u};
    }
  }
  __syncthreads();
  TS(1, 1);
  const int cpy = r16 & 3;
  const char* hb = smem + (cpy == 3 ? CV_H3 : CV_H0 + cpy * CV_HS) + 16 * ((r16 >> 2) + 4 * w + g);
  bf16x8 Bf[16];
#pragma unroll
  for (int t = 0; t < 16; ++t) Bf[t] = *(const bf16x8*)(hb + 256 * t);
  const char* kb = smem + CV_K + (r16 & 1) * 4576 + 16 * (252 + 4 * w - 4 * (r16 >> 1) + g) - 256 * 14;
  f32x4 acc[8];
#pragma unroll
  for (int i = 0; i < 8; ++i) acc[i] = f32x4{0.f, 0.f, 0.f, 0.f};
#pragma unroll
  for (int sg = -1; sg <= 14; ++sg) {
    const bf16x8 Af = *(const bf16x8*)(kb + 256 * (14 - sg));
#pragma unroll
    for (int i = 0; i < 8; ++i) {
      const int tau = 2 * i - sg;
      if (tau >= 0 && tau <= 15) acc[i] = mfma16(Af, Bf[tau], acc[i]);
    }
  }
  __syncthreads();
  TS(1, 2);
#pragma unroll
  for (int i = 0; i < 8; ++i) {
    const int owner = i < 4 ? i : 7 - i;
    if (owner != w) {
      const int slot = w < owner ? w : w - 1;
      *(f32x4*)(smem + ((i * 3 + slot) * 64 + lane) * 16) = acc[i];
    }
  }
  __syncthreads();
#pragma unroll
  for (int k = 0; k < 2; ++k) {
    const int i = k ? 7 - w : w;
    f32x4 y;
#pragma unroll
    for (int ii = 0; ii < 4; ++ii) if (ii == w) y = k ? acc[7 - ii] : acc[ii];
#pragma unroll
    for (int s = 0; s < 3; ++s) y += *(const f32x4*)(smem + ((i * 3 + s) * 64 + lane) * 16);
    const int l0 = 256 * i + 64 * g + 2 * r16;
#pragma unroll
    for (int hh = 0; hh < 2; ++hh) {
      const u32 hv = *(const u32*)(smem + CV_H3 + 2 * (l0 + 32 * hh + 24));
      const u32 yr = cvtpk(y[2 * hh], y[2 * hh + 1]);
      const u32 hd = cvtpk(lo2f(hv) * dsk, hi2f(hv) * dsk);
      const u32 v = cvtpk(lo2f(yr) + lo2f(hd), hi2f(yr) + hi2f(hd));
      const u32 gv = cvtpk(lo2f(v) * ga, hi2f(v) * ga);
      *(u32*)(x1T + l0 + 32 * hh) = cvtpk(lo2f(xv[k][hh]) + lo2f(gv), hi2f(xv[k][hh]) + hi2f(gv));
    }
  }
  TS(1, 7);
}

constexpr int M1_A = 0, M1_ST = 81920, M1_SMEM = M1_ST + 512;

DI int h2_swz(int k) { return ((k >> 1) & 1) | (((k >> 3) & 1) << 1); }

__global__ __launch_bounds__(512, 2) void k_mlp1(char* __restrict__ ws) {
  __shared__ __attribute__((aligned(16))) char smem[M1_SMEM];
  const int tid = threadIdx.x, w = __builtin_amdgcn_readfirstlane(tid >> 6), lane = tid & 63;
  const int xcd = blockIdx.x & 7, idx = blockIdx.x >> 3;
  const int tt = (xcd >> 1) * 8 + (idx & 7), sl = (xcd & 1) * 4 + (idx >> 3);
  const int pbase = 7 * sl - (sl >> 2), pcnt = (sl & 3) == 3 ? 6 : 7;
  const bool active = w < pcnt;
  const int p0 = pbase + (active ? w : 0);
  const u16* x1T = (const u16*)(ws + WS_X1T);
  const int l8 = tid & 7, crow = tid >> 3;
  const int g = lane >> 4, r16 = lane & 15, q = r16 >> 2, p = lane & 3;
  TS(2, 0);
  u32x4 xv[10];
#pragma unroll
  for (int qq = 0; qq < 10; ++qq) xv[qq] = *(const u32x4*)(x1T + (size_t)(crow + 64 * qq) * L_ + tt * 64 + 8 * l8);
  __builtin_amdgcn_sched_barrier(0);
  const char* w1p = ws + WS_W1F + (size_t)(2 * p0) * 20 * 1024 + lane * 16;
  bf16x8 af[20][2];
  if (active) {
#pragma unroll
    for (int ks = 0; ks < 3; ++ks) {
#pragma unroll
      for (int fb = 0; fb < 2; ++fb) af[ks][fb] = *(const bf16x8*)(w1p + ((size_t)fb * 20 + ks) * 1024);
      __builtin_amdgcn_sched_barrier(0);
    }
  }
#pragma unroll
  for (int qq = 0; qq < 10; ++qq) {
    const int c = crow + 64 * qq;
    *(u32x4*)(smem + M1_A + c * 128 + (((l8 >> 1) ^ h2_swz(c)) << 5) + ((l8 & 1) << 4)) = xv[qq];
  }
  __builtin_amdgcn_sched_barrier(0);
  f32x4 sv[2], bv[2];
  if (active) {
#pragma unroll
    for (int ks = 3; ks < 20; ++ks) {
#pragma unroll
      for (int fb = 0; fb < 2; ++fb) af[ks][fb] = *(const bf16x8*)(w1p + ((size_t)fb * 20 + ks) * 1024);
      __builtin_amdgcn_sched_barrier(0);
    }
    const float* sp = (const float*)(ws + WS_SB) + 32 * p0 + 4 * g;
#pragma unroll
    for (int fb = 0; fb < 2; ++fb) {
      sv[fb] = *(const f32x4*)(sp + 16 * fb); bv[fb] = *(const f32x4*)(sp + 4 * F_ + 16 * fb);
#pragma unroll
      for (int kq = 1; kq < 4; ++kq) { sv[fb] += *(const f32x4*)(sp + kq * F_ + 16 * fb); bv[fb] += *(const f32x4*)(sp + (4 + kq) * F_ + 16 * fb); }
    }
  }
  TS(2, 1);
  __syncthreads();
  TS(2, 2);
  f32x4 acc[2][4];
#pragma unroll
  for (int fb = 0; fb < 2; ++fb)
#pragma unroll
    for (int tb = 0; tb < 4; ++tb) acc[fb][tb] = f32x4{0.f, 0.f, 0.f, 0.f};
  const int row0 = 8 * g + q, sw = h2_swz(row0);
  const char* rb0 = smem + M1_A + row0 * 128 + 8 * p;
  float* st = (float*)(smem + M1_ST);
  if (active) {
#pragma unroll
    for (int ks = 0; ks < 20; ++ks) {
      bf16x8 bfr[4];
#pragma unroll
      for (int tb = 0; tb < 4; ++tb) {
        const s16x4 lo = tr_read(rb0 + ks * 4096 + ((tb ^ sw) << 5));
        const s16x4 hi = tr_read(rb0 + ks * 4096 + 512 + ((tb ^ sw) << 5));
        bfr[tb] = cat8(lo, hi);
      }
#pragma unroll
      for (int fb = 0; fb < 2; ++fb)
#pragma unroll
        for (int tb = 0; tb < 4; ++tb) acc[fb][tb] = mfma16(af[ks][fb], bfr[tb], acc[fb][tb]);
    }
  } else if (w == 7) {
    const short one = 0x3f80;
    const bf16x8 ones = {one, one, one, one, one, one, one, one};
#pragma unroll
    for (int ks = 0; ks < 20; ++ks) {
#pragma unroll
      for (int tb = 0; tb < 4; ++tb) {
        const s16x4 lo = tr_read(rb0 + ks * 4096 + ((tb ^ sw) << 5));
        const s16x4 hi = tr_read(rb0 + ks * 4096 + 512 + ((tb ^ sw) << 5));
        const bf16x8 bb = cat8(lo, hi);
        acc[0][tb] = mfma16(ones, bb, acc[0][tb]);
        acc[1][tb] = mfma16(bb, bb, acc[1][tb]);
      }
    }
    if (g == q) {
#pragma unroll
      for (int tb = 0; tb < 4; ++tb) {
        const float sq = p == 0 ? acc[1][tb][0] : p == 1 ? acc[1][tb][1] : p == 2 ? acc[1][tb][2] : acc[1][tb][3];
        const float mean = acc[0][tb][0] * (1.f / D_);
        const float var = fmaxf(sq * (1.f / D_) - mean * mean, 0.f);
        st[tb * 16 + r16] = rb(mean);
        st[64 + tb * 16 + r16] = 1.f / rb(sqrtf(rb(rb(var) + 1e-5f)));
      }
    }
  }
  __syncthreads();
  if (!active) return;
  char* hid = ws + WS_HID + ((size_t)(tt * 4) * 54 + p0) * 1024 + lane * 16;
#pragma unroll
  for (int tb = 0; tb < 4; ++tb) {
    const float mb = st[tb * 16 + r16], rs = st[64 + tb * 16 + r16];
    u32x4 v;
#pragma unroll
    for (int hh = 0; hh < 2; ++hh) {
      const f32x4 a = (acc[hh][tb] - mb * sv[hh]) * rs + bv[hh];
      v[2 * hh] = cvtpk(fmaxf(a[0], 0.f), fmaxf(a[1], 0.f));
      v[2 * hh + 1] = cvtpk(fmaxf(a[2], 0.f), fmaxf(a[3], 0.f));
    }
    *(u32x4*)(hid + (size_t)tb * 54 * 1024) = v;
  }
  TS(2, 7);
}

constexpr int M2_RED = 0, M2_X = 61440, M2_XS = 176, M2_SMEM = M2_X + 64 * M2_XS;
constexpr int M2_PF = 9;

__global__ __launch_bounds__(256, 1) void k_mlp2(char* __restrict__ ws, u16* __restrict__ out) {
  __shared__ __attribute__((aligned(16))) char smem[M2_SMEM];
  const int tid = threadIdx.x, w = __builtin_amdgcn_readfirstlane(tid >> 6), lane = tid & 63, r16 = lane & 15, g = lane >> 4;
  const int xcd = blockIdx.x & 7, idx = blockIdx.x >> 3;
  const int tt = (xcd >> 1) * 8 + (idx & 7), cs = (xcd & 1) * 4 + (idx >> 3);
  const int k0 = w < 2 ? 14 * w : 28 + 13 * (w - 2), nk = w < 2 ? 14 : 13;
  const char* hp = ws + WS_HID + ((size_t)(tt * 4) * 54 + k0) * 1024 + lane * 16;
  const char* wp = ws + WS_W2F + ((size_t)(cs * 5) * 54 + k0) * 1024 + lane * 16;
  TS(3, 0);
  u32x4 xr0[3];
  {
    const u16* x1T = (const u16*)(ws + WS_X1T);
#pragma unroll
    for (int i = 0; i < 3; ++i) {
      const int id = (tid + 256 * i) < 640 ? tid + 256 * i : tid;
      xr0[i] = *(const u32x4*)(x1T + (size_t)(cs * 80 + (id >> 3)) * L_ + tt * 64 + 8 * (id & 7));
    }
  }
  f32x4 gbv[5];
#pragma unroll
  for (int cb = 0; cb < 5; ++cb) gbv[cb] = *(const f32x4*)((const float*)(ws + WS_MODF) + 2 * D_ + cs * 80 + 16 * cb + 4 * g);
  __builtin_amdgcn_sched_barrier(0);
  bf16x8 hf[M2_PF][4], wf[M2_PF][5];
#pragma unroll
  for (int i = 0; i < M2_PF; ++i) {
#pragma unroll
    for (int tb = 0; tb < 4; ++tb) hf[i][tb] = *(const bf16x8*)(hp + ((size_t)tb * 54 + i) * 1024);
#pragma unroll
    for (int cb = 0; cb < 5; ++cb) wf[i][cb] = *(const bf16x8*)(wp + ((size_t)cb * 54 + i) * 1024);
    __builtin_amdgcn_sched_barrier(0);
  }
#pragma unroll
  for (int i = 0; i < 3; ++i) {
    const int id = tid + 256 * i;
    if (id < 640) {
      const int c = id >> 3, o = id & 7;
#pragma unroll
      for (int k = 0; k < 4; ++k) {
        *(u16*)(smem + M2_X + (8 * o + 2 * k) * M2_XS + 2 * c) = (u16)(xr0[i][k] & 0xffffu);
        *(u16*)(smem + M2_X + (8 * o + 2 * k + 1) * M2_XS + 2 * c) = (u16)(xr0[i][k] >> 16);
      }
    }
  }
  __builtin_amdgcn_sched_barrier(0);
  f32x4 acc[5][4];
#pragma unroll
  for (int cb = 0; cb < 5; ++cb)
#pragma unroll
    for (int tb = 0; tb < 4; ++tb) acc[cb][tb] = f32x4{0.f, 0.f, 0.f, 0.f};
#pragma unroll
  for (int i = 0; i < 14; ++i) {
    if (i < 13 || nk == 14) {
#pragma unroll
      for (int cb = 0; cb < 5; ++cb)
#pragma unroll
        for (int tb = 0; tb < 4; ++tb) acc[cb][tb] = mfma16(wf[i % M2_PF][cb], hf[i % M2_PF][tb], acc[cb][tb]);
    }
    __builtin_amdgcn_sched_barrier(0);
    if (i + M2_PF < 14) {
      const int kk = (i + M2_PF < 13 || nk == 14) ? i + M2_PF : 12;
#pragma unroll
      for (int tb = 0; tb < 4; ++tb) hf[i % M2_PF][tb] = *(const bf16x8*)(hp + ((size_t)tb * 54 + kk) * 1024);
#pragma unroll
      for (int cb = 0; cb < 5; ++cb) wf[i % M2_PF][cb] = *(const bf16x8*)(wp + ((size_t)cb * 54 + kk) * 1024);
      __builtin_amdgcn_sched_barrier(0);
    }
  }
  TS(3, 1);
#pragma unroll
  for (int tb = 0; tb < 4; ++tb) {
    if (tb != w) {
      const int slot = w < tb ? w : w - 1;
#pragma unroll
      for (int cb = 0; cb < 5; ++cb) *(f32x4*)(smem + M2_RED + (((tb * 5 + cb) * 3 + slot) * 64 + lane) * 16) = acc[cb][tb];
    }
  }
  __syncthreads();
  const int tok = 16 * w + r16;
#pragma unroll
  for (int cb = 0; cb < 5; ++cb) {
    f32x4 y;
#pragma unroll
    for (int tb = 0; tb < 4; ++tb) if (tb == w) y = acc[cb][tb];
#pragma unroll
    for (int s = 0; s < 3; ++s) y += *(const f32x4*)(smem + M2_RED + (((w * 5 + cb) * 3 + s) * 64 + lane) * 16);
    const u32x2 xr = *(const u32x2*)(smem + M2_X + tok * M2_XS + 2 * (16 * cb + 4 * g));
    u32x2 o;
#pragma unroll
    for (int hh = 0; hh < 2; ++hh) {
      const u32 yr = cvtpk(y[2 * hh], y[2 * hh + 1]);
      const u32 gv = cvtpk(lo2f(yr) * gbv[cb][2 * hh], hi2f(yr) * gbv[cb][2 * hh + 1]);
      o[hh] = cvtpk(lo2f(xr[hh]) + lo2f(gv), hi2f(xr[hh]) + hi2f(gv));
    }
    *(u32x2*)(out + (size_t)(tt * 64 + tok) * D_ + cs * 80 + 16 * cb + 4 * g) = o;
  }
  TS(3, 7);
}

extern "C" void kernel_launch(void* const* d_in, const int* in_sizes, int n_in, void* d_out, int out_size, void* d_ws,
                              size_t ws_size, hipStream_t stream) {
  char* ws = (char*)d_ws;
  k_pre<<<NB_LN + NB_KG + NB_MOD, 256, 0, stream>>>(
      (const u16*)d_in[0], (const u16*)d_in[1], (const u16*)d_in[2], (const u16*)d_in[3], (const u16*)d_in[4],
      (const u16*)d_in[5], (const u16*)d_in[6], (const u16*)d_in[7], (const u16*)d_in[12], (const u16*)d_in[13], ws);
  k_conv<<<256, 768, 0, stream>>>((const u16*)d_in[8], (const u16*)d_in[10], (const u16*)d_in[11], ws);
  k_mlp1<<<256, 512, 0, stream>>>(ws);
  k_mlp2<<<256, 256, 0, stream>>>(ws, (u16*)d_out);
}
```

```cpp
#include <hip/hip_runtime.h>
#include <hip/hip_bf16.h>
#include <stdint.h>

typedef unsigned short u16;
typedef unsigned int u32;
typedef __attribute__((ext_vector_type(8))) short bf16x8;
typedef __attribute__((ext_vector_type(4))) short s16x4;
typedef __attribute__((ext_vector_type(4))) float f32x4;
typedef __attribute__((ext_vector_type(2))) float f32x2;
typedef __attribute__((ext_vector_type(4))) unsigned int u32x4;
typedef __attribute__((ext_vector_type(2))) unsigned int u32x2;
typedef __bf16 bf16x2_t __attribute__((ext_vector_type(2)));
#define LDS_AS __attribute__((address_space(3)))
#define DI __device__ __forceinline__

constexpr int L_ = 2048, D_ = 640, N_ = 128, F_ = 1728;
constexpr int KSM = 8;

constexpr size_t WS_MODP = 0;
constexpr size_t WS_CNT = 131072;
constexpr size_t WS_NT = 262144;
constexpr size_t WS_XT = WS_NT + (size_t)D_ * L_ * 2;
constexpr size_t WS_X1T = WS_XT + (size_t)D_ * L_ * 2;
constexpr int KTAB = 1296;
constexpr size_t WS_KT = WS_X1T + (size_t)D_ * L_ * 2;
constexpr size_t WS_SB = WS_KT + (size_t)D_ * KTAB;
constexpr size_t WS_W1F = WS_SB + 2 * 20 * 1728 * 4;
constexpr size_t WS_W2F = WS_W1F + (size_t)108 * 20 * 1024;
constexpr size_t WS_HID = WS_W2F + (size_t)40 * 54 * 1024;

#define TS(kern, slot) do {} while (0)
#define TC(kern, slot) do {} while (0)

DI u32 cvtpk(float lo, float hi) {
  f32x2 v = {lo, hi};
  bf16x2_t r = __builtin_convertvector(v, bf16x2_t);
  return __builtin_bit_cast(u32, r);
}
typedef _Float16 f16x2_t __attribute__((ext_vector_type(2)));
typedef _Float16 f16x8_t __attribute__((ext_vector_type(8)));
DI u32 cvtph(float lo, float hi) {
  f32x2 v = {lo, hi};
  f16x2_t r = __builtin_convertvector(v, f16x2_t);
  return __builtin_bit_cast(u32, r);
}
DI float dot2bf(u32 a, u32 b, float c) {
  return __builtin_amdgcn_fdot2_f32_bf16(__builtin_bit_cast(bf16x2_t, a), __builtin_bit_cast(bf16x2_t, b), c, false);
}
DI float lo2f(u32 v) { return __uint_as_float(v << 16); }
DI float hi2f(u32 v) { return __uint_as_float(v & 0xffff0000u); }
DI float bf2f(u16 v) { return __uint_as_float(((u32)v) << 16); }
DI float rb(float x) { return lo2f(cvtpk(x, x)); }
DI f32x4 mfma16(bf16x8 a, bf16x8 b, f32x4 c) { return __builtin_amdgcn_mfma_f32_16x16x32_bf16(a, b, c, 0, 0, 0); }
DI s16x4 tr_read(const char* p) {
  return __builtin_amdgcn_ds_read_tr16_b64_v4i16((LDS_AS s16x4*)p);
}
DI bf16x8 cat8(s16x4 a, s16x4 b) { return __builtin_shufflevector(a, b, 0, 1, 2, 3, 4, 5, 6, 7); }
DI void wave_sync() {
  __builtin_amdgcn_fence(__ATOMIC_RELEASE, "wavefront");
  __builtin_amdgcn_wave_barrier();
  __builtin_amdgcn_fence(__ATOMIC_ACQUIRE, "wavefront");
}

struct c32 { float r, i; };
DI c32 cmul(c32 a, c32 b) { return {a.r * b.r - a.i * b.i, a.r * b.i + a.i * b.r}; }
DI c32 cexp_scaled(float ar, float ai, float e) {
  const float mag = __expf(ar * e);
  const float x = ai * e;
  const float C_HI = 0.15915494f, C_LO = 6.4206382e-9f;
  const float r = x * C_HI;
  const float rl = fmaf(x, C_HI, -r) + x * C_LO;
  const float fr = (r - floorf(r)) + rl;
  return {mag * __builtin_amdgcn_cosf(fr), mag * __builtin_amdgcn_sinf(fr)};
}
DI int tap_index(int m) {
  u32 bits = __float_as_uint((float)m);
  bits += 0x7fffu + ((bits >> 16) & 1u);
  const int id2 = (int)(bits >> 16) - (0x4300 - 128);
  return m < 128 ? m : id2;
}

constexpr int NB_LN = 64, NB_KG = 320, NB_R1 = 120, NB_MOD = 20 * KSM;
constexpr int PRE_SMEM = 43008;

DI void role_ln1(int b, const u16* __restrict__ x, u16* __restrict__ nT, u16* __restrict__ xT, char* smem) {
  const int tid = threadIdx.x, u = tid >> 4, j = tid & 15;
  const int t0 = b * 32;
  const u32x4* xa = (const u32x4*)(x + (size_t)(t0 + 2 * u) * D_);
  u32x4 A[5], B[5];
#pragma unroll
  for (int q = 0; q < 5; ++q) { A[q] = xa[j + 16 * q]; B[q] = xa[80 + j + 16 * q]; }
  float sa = 0.f, sb = 0.f, qa = 0.f, qb = 0.f;
#pragma unroll
  for (int q = 0; q < 5; ++q)
#pragma unroll
    for (int k = 0; k < 4; ++k) {
      sa = dot2bf(A[q][k], 0x3f803f80u, sa); qa = dot2bf(A[q][k], A[q][k], qa);
      sb = dot2bf(B[q][k], 0x3f803f80u, sb); qb = dot2bf(B[q][k], B[q][k], qb);
    }
#pragma unroll
  for (int o = 1; o < 16; o <<= 1) {
    sa += __shfl_xor(sa, o, 16); sb += __shfl_xor(sb, o, 16); qa += __shfl_xor(qa, o, 16); qb += __shfl_xor(qb, o, 16);
  }
  const float ma = sa * (1.f / D_), mb = sb * (1.f / D_);
  const float va = fmaxf(qa - sa * ma, 0.f), vb = fmaxf(qb - sb * mb, 0.f);
  const float mra = rb(ma), mrb = rb(mb);
  const float ra = 1.f / rb(sqrtf(rb(rb(va * (1.f / D_)) + 1e-5f)));
  const float rbb = 1.f / rb(sqrtf(rb(rb(vb * (1.f / D_)) + 1e-5f)));
  char* wbase = smem + (8 * j + (j >> 2)) * 64 + ((((u >> 2) ^ (j & 3)) << 2) + (u & 3)) * 4;
#pragma unroll
  for (int q = 0; q < 5; ++q)
#pragma unroll
    for (int k = 0; k < 4; ++k) {
      const u32 c0 = cvtpk(lo2f(A[q][k]) - mra, lo2f(B[q][k]) - mrb);
      const u32 c1 = cvtpk(hi2f(A[q][k]) - mra, hi2f(B[q][k]) - mrb);
      *(u32*)(wbase + (132 * q + 2 * k) * 64) = cvtpk(lo2f(c0) * ra, hi2f(c0) * rbb);
      *(u32*)(wbase + (132 * q + 2 * k + 1) * 64) = cvtpk(lo2f(c1) * ra, hi2f(c1) * rbb);
    }
  __syncthreads();
#pragma unroll
  for (int i = 0; i < 10; ++i) {
    const int id = tid + 256 * i, c = id >> 2, o = id & 3;
    const u32x4 v = *(const u32x4*)(smem + (c + (c >> 5)) * 64 + ((o ^ ((c >> 3) & 3)) << 4));
    *(u32x4*)(nT + (size_t)c * L_ + t0 + 8 * o) = v;
  }
  __syncthreads();
#pragma unroll
  for (int q = 0; q < 5; ++q)
#pragma unroll
    for (int k = 0; k < 4; ++k) {
      *(u32*)(wbase + (132 * q + 2 * k) * 64) = (A[q][k] & 0xffffu) | (B[q][k] << 16);
      *(u32*)(wbase + (132 * q + 2 * k + 1) * 64) = (A[q][k] >> 16) | (B[q][k] & 0xffff0000u);
    }
  __syncthreads();
#pragma unroll
  for (int i = 0; i < 10; ++i) {
    const int id = tid + 256 * i, c = id >> 2, o = id & 3;
    const u32x4 v = *(const u32x4*)(smem + (c + (c >> 5)) * 64 + ((o ^ ((c >> 3) & 3)) << 4));
    *(u32x4*)(xT + (size_t)c * L_ + t0 + 8 * o) = v;
  }
}

DI void role_kgen(int bk, const u16* __restrict__ Are, const u16* __restrict__ Aim, const u16* __restrict__ Bre,
                  const u16* __restrict__ Bim, const u16* __restrict__ Cre, const u16* __restrict__ Cim,
                  const u16* __restrict__ ldt, char* __restrict__ KT, char* smem) {
  const int tid = threadIdx.x, w = tid >> 6, lane = tid & 63, r16 = lane & 15, g = lane >> 4;
  const int d = 2 * bk + (w >> 1), n = (w & 1) * 64 + lane;
  const int gi = d * N_ + n;
  const float dt = rb(expf(bf2f(ldt[d])));
  const float ar = dt * bf2f(Are[gi]), ai = dt * bf2f(Aim[gi]);
  const c32 dB = {dt * bf2f(Bre[gi]), dt * bf2f(Bim[gi])};
  const c32 Cc = {bf2f(Cre[gi]), bf2f(Cim[gi])};
  const c32 W = cmul(Cc, dB);
  const c32 E1 = cexp_scaled(ar, ai, 1.f), E8 = cexp_scaled(ar, ai, 8.f), E128 = cexp_scaled(ar, ai, 128.f);

  float wmax = fmaxf(fabsf(W.r), fabsf(W.i));
#pragma unroll
  for (int o = 1; o < 64; o <<= 1) wmax = fmaxf(wmax, __shfl_xor(wmax, o));
  int ew = (int)((__float_as_uint(wmax) >> 23) & 0xff);
  ew = ew < 20 ? 20 : (ew > 240 ? 240 : ew);
  const float wsc = __uint_as_float((u32)(127 + 10 + 127 - ew - 1) << 23);
  const float unsc = __uint_as_float((u32)(ew + 1 - 20) << 23);

  char* buf = smem + w * 8704;
  char* wr = buf + lane * 4;
  const char* rd = buf + r16 * 272 + g * 16;
  auto put = [&](int row, float re, float im) { *(u32*)(wr + row * 272) = cvtph(re, im); };
  {
    c32 p = {1024.f, 0.f};
#pragma unroll
    for (int jj = 0; jj < 16; ++jj) { put(jj, p.r, p.i); if (jj < 15) p = cmul(p, E8); }
  }
  wave_sync();
  f16x8_t bh[4];
#pragma unroll
  for (int ks = 0; ks < 4; ++ks) bh[ks] = *(const f16x8_t*)(rd + ks * 64);
  wave_sync();
  c32 sm[8];
  sm[0] = {1.f, 0.f}; sm[1] = E1;
#pragma unroll
  for (int i = 2; i < 8; ++i) sm[i] = cmul(sm[i - 1], E1);
  c32 WG[17];
  WG[0] = {W.r * wsc, W.i * wsc};
#pragma unroll
  for (int i = 1; i < 17; ++i) WG[i] = cmul(WG[i - 1], E128);
  f32x4 acc[3];
  auto tile_mma = [&](int t) {
    wave_sync();
    f32x4 a = {0.f, 0.f, 0.f, 0.f};
#pragma unroll
    for (int ks = 0; ks < 4; ++ks) a = __builtin_amdgcn_mfma_f32_16x16x32_f16(*(const f16x8_t*)(rd + ks * 64), bh[ks], a, 0, 0, 0);
    acc[t] = a * unsc;
    wave_sync();
  };
#pragma unroll
  for (int i = 0; i < 2; ++i)
#pragma unroll
    for (int rho = 0; rho < 8; ++rho) { const c32 v = rho ? cmul(WG[i], sm[rho]) : WG[i]; put(8 * i + rho, v.r, -v.i); }
  tile_mma(0);
#pragma unroll
  for (int i = 0; i < 2; ++i)
#pragma unroll
    for (int rho = 0; rho < 4; ++rho) { const c32 v = rho ? cmul(WG[2 + i], sm[2 * rho]) : WG[2 + i]; put(4 * i + rho, v.r, -v.i); }
#pragma unroll
  for (int i = 0; i < 4; ++i)
#pragma unroll
    for (int rho = 0; rho < 2; ++rho) { const c32 v = rho ? cmul(WG[4 + i], sm[4]) : WG[4 + i]; put(8 + 2 * i + rho, v.r, -v.i); }
  tile_mma(1);
#pragma unroll
  for (int i = 0; i < 9; ++i) put(i, WG[8 + i].r, -WG[8 + i].i);
  tile_mma(2);

  if (w & 1) {
#pragma unroll
    for (int t = 0; t < 3; ++t) *(f32x4*)(buf + t * 1024 + lane * 16) = acc[t];
  }
  __syncthreads();
  u16* T = (u16*)(smem + (w >> 1) * 2 * 8704);
  if (!(w & 1)) {
    const char* ob = buf + 8704;
#pragma unroll
    for (int t = 0; t < 3; ++t) acc[t] += *(const f32x4*)(ob + t * 1024 + lane * 16);
#pragma unroll
    for (int t = 0; t < 3; ++t)
#pragma unroll
      for (int rg = 0; rg < 4; ++rg) {
        const int row = 4 * g + rg;
        int base;
        if (t == 0) base = 128 * (row >> 3) + (row & 7);
        else if (t == 1) base = row < 8 ? 256 + 128 * (row >> 2) + 2 * (row & 3) : 512 + 128 * ((row - 8) >> 1) + 4 * (row & 1);
        else base = 1024 + 128 * row;
        const int e = base + 8 * r16;
        if (e <= 2048) T[tap_index(e)] = (u16)(cvtpk(acc[t][rg], 0.f) & 0xffffu);
      }
    if (lane < 7) T[641 + lane] = 0;
  }
  __syncthreads();
  const int t128 = tid & 127;
  if (t128 < 81) *(u32x4*)(KT + (size_t)d * KTAB + t128 * 16) = *(const u32x4*)((const char*)T + t128 * 16);
}

DI void role_mod(int bm, const u16* __restrict__ c, const u16* __restrict__ wm, float* __restrict__ modp, char* smem) {
  const int tid = threadIdx.x, cj = tid & 15, rg = tid >> 4, w = tid >> 6;
  const int cb0 = bm % 20, ks = bm / 20;
  const int cbk = cb0 < 15 ? cb0 : cb0 + 10;
  u32x4 v[5];
  float s[5];
#pragma unroll
  for (int i = 0; i < 5; ++i) {
    const int row = 80 * ks + rg + 16 * i;
    v[i] = *(const u32x4*)(wm + (size_t)row * (6 * D_) + 128 * cbk + 8 * cj);
    const float cv = bf2f(c[row]);
    s[i] = rb(cv / (1.f + expf(-cv)));
  }
  float a[8];
#pragma unroll
  for (int k = 0; k < 8; ++k) a[k] = 0.f;
#pragma unroll
  for (int i = 0; i < 5; ++i)
#pragma unroll
    for (int k = 0; k < 4; ++k) { a[2 * k] += s[i] * lo2f(v[i][k]); a[2 * k + 1] += s[i] * hi2f(v[i][k]); }
#pragma unroll
  for (int k = 0; k < 8; ++k) { a[k] += __shfl_xor(a[k], 16); a[k] += __shfl_xor(a[k], 32); }
  float* red = (float*)smem;
  if ((tid & 63) < 16) {
#pragma unroll
    for (int k = 0; k < 8; ++k) red[w * 128 + 8 * cj + k] = a[k];
  }
  __syncthreads();
  if (tid < 128) modp[ks * (6 * D_) + 128 * cbk + tid] = red[tid] + red[128 + tid] + red[256 + tid] + red[384 + tid];
}

DI int rl_swz(int row) { return (row & 3) | ((((row >> 2) ^ (row >> 3)) & 1) << 2); }

DI void role_r1(int rw, const u16* __restrict__ c, const u16* __restrict__ wm, const u16* __restrict__ w1,
                char* __restrict__ ws, char* smem) {
  const int tid = threadIdx.x, w = tid >> 6, lane = tid & 63, g = lane >> 4, q = (lane & 15) >> 2, p = lane & 3;
  const int ks = rw % 20, part = rw / 20;
  const int ch8 = tid & 7, kg = tid >> 3;
  u32x4 mv[20];
  const u16* mp = wm + (ch8 < 4 ? 4 * D_ : 3 * D_) + 32 * ks + 8 * (ch8 & 3);
#pragma unroll
  for (int i = 0; i < 20; ++i) mv[i] = *(const u32x4*)(mp + (size_t)(kg + 32 * i) * (6 * D_));
  const int col0 = 288 * part;
  u32x4 wv[5];
#pragma unroll
  for (int i = 0; i < 5; ++i) {
    const int ch = ch8 + 8 * i;
    wv[i] = *(const u32x4*)(w1 + (size_t)(32 * ks + kg) * F_ + col0 + 8 * (ch < 36 ? ch : 0));
  }
  float* sc = (float*)(smem + 19456);
  float* red = (float*)(smem + 19456 + 2560);
#pragma unroll
  for (int i = 0; i < 3; ++i) {
    const int k = tid + 256 * i;
    if (k < D_) { const float cv = bf2f(c[k]); sc[k] = rb(cv / (1.f + __expf(-cv))); }
  }
#pragma unroll
  for (int i = 0; i < 5; ++i) {
    const int ch = ch8 + 8 * i;
    if (ch < 36) *(u32x4*)(smem + kg * 608 + ch * 16) = wv[i];
  }
  __syncthreads();
  float a[8];
#pragma unroll
  for (int k = 0; k < 8; ++k) a[k] = 0.f;
#pragma unroll
  for (int i = 0; i < 20; ++i) {
    const float sv = sc[kg + 32 * i];
#pragma unroll
    for (int k = 0; k < 4; ++k) { a[2 * k] += sv * lo2f(mv[i][k]); a[2 * k + 1] += sv * hi2f(mv[i][k]); }
  }
#pragma unroll
  for (int k = 0; k < 8; ++k) { a[k] += __shfl_xor(a[k], 8); a[k] += __shfl_xor(a[k], 16); a[k] += __shfl_xor(a[k], 32); }
  if (lane < 8) {
    *(f32x4*)(red + w * 64 + 8 * lane) = f32x4{a[0], a[1], a[2], a[3]};
    *(f32x4*)(red + w * 64 + 8 * lane + 4) = f32x4{a[4], a[5], a[6], a[7]};
  }
  __syncthreads();
  float c1[8];
  u32 sbp[4];
  {
    f32x4 t[4];
#pragma unroll
    for (int j = 0; j < 4; ++j) {
      const float* rp = red + (j >> 1) * 32 + 8 * g + 4 * (j & 1);
      t[j] = (*(const f32x4*)(rp) + *(const f32x4*)(rp + 64)) + (*(const f32x4*)(rp + 128) + *(const f32x4*)(rp + 192));
    }
#pragma unroll
    for (int e = 0; e < 8; ++e) c1[e] = rb(1.f + rb(t[e >> 2][e & 3]));
#pragma unroll
    for (int k = 0; k < 4; ++k) sbp[k] = cvtpk(t[2 + (k >> 1)][2 * (k & 1)], t[2 + (k >> 1)][2 * (k & 1) + 1]);
  }
  float* sp = (float*)(ws + WS_SB) + ks * F_ + col0;
#pragma unroll
  for (int i = 0; i < 5; ++i) {
    const int blk = w + 4 * i, bl = blk < 18 ? blk : 0;
    const s16x4 lo = tr_read(smem + (8 * g + q) * 608 + bl * 32 + 8 * p);
    const s16x4 hi = tr_read(smem + (8 * g + q + 4) * 608 + bl * 32 + 8 * p);
    const u32x4 r = __builtin_bit_cast(u32x4, cat8(lo, hi));
    u32x4 o;
    float S = 0.f, B = 0.f;
#pragma unroll
    for (int k = 0; k < 4; ++k) {
      o[k] = cvtpk(lo2f(r[k]) * c1[2 * k], hi2f(r[k]) * c1[2 * k + 1]);
      S = dot2bf(o[k], 0x3f803f80u, S);
      B = dot2bf(r[k], sbp[k], B);
    }
    S += __shfl_xor(S, 16); S += __shfl_xor(S, 32);
    B += __shfl_xor(B, 16); B += __shfl_xor(B, 32);
    if (blk < 18) {
      *(u32x4*)(ws + WS_W1F + ((size_t)(18 * part + blk) * 20 + ks) * 1024 + lane * 16) = o;
      if (lane < 16) { sp[16 * blk + lane] = S; sp[20 * F_ + 16 * blk + lane] = B; }
    }
  }
}

__global__ __launch_bounds__(256) void k_pre(const u16* __restrict__ x, const u16* __restrict__ Are, const u16* __restrict__ Aim,
                                             const u16* __restrict__ Bre, const u16* __restrict__ Bim, const u16* __restrict__ Cre,
                                             const u16* __restrict__ Cim, const u16* __restrict__ ldt, const u16* __restrict__ w1,
                                             const u16* __restrict__ c, const u16* __restrict__ wm,
                                             char* __restrict__ ws) {
  __shared__ __attribute__((aligned(16))) char smem[PRE_SMEM];
  int b = blockIdx.x;
  TS(0, 0);
  if (b < NB_LN) {
    if (b == 0 && threadIdx.x < 48) ((u32*)(ws + WS_CNT))[threadIdx.x * 64] = 0u;
    role_ln1(b, x, (u16*)(ws + WS_NT), (u16*)(ws + WS_XT), smem); TS(0, 7); return;
  }
  b -= NB_LN;
  if (b < NB_R1) { role_r1(b, c, wm, w1, ws, smem); TS(0, 7); return; }
  b -= NB_R1;
  if (b < NB_KG) { role_kgen(b, Are, Aim, Bre, Bim, Cre, Cim, ldt, ws + WS_KT, smem); TS(0, 7); return; }
  b -= NB_KG;
  role_mod(b, c, wm, (float*)(ws + WS_MODP), smem);
  TS(0, 7);
}

constexpr int CV_H0 = 0, CV_HS = 4160, CV_K = 12544, CV_T = 21760, CV_H3 = 32768 + 192, CV_CH = 37376;
static_assert(CV_CH % 256 == 0 && CV_H3 + 4160 <= CV_CH && CV_T + 4 * KTAB <= 32768, "conv lds");
constexpr int M1_A = 0, M1_ST = 81920;
constexpr int MN_SB = 3 * CV_CH;
constexpr int M2_RED = 0, M2_X = 143360, M2_XS = 176;
constexpr int MN_GB = M2_X + 64 * M2_XS;
constexpr int MN_SMEM = MN_GB + 320;
static_assert(MN_SB + 2048 <= M2_X && M1_ST + 512 <= MN_SB, "mlp lds");

typedef __amdgpu_buffer_rsrc_t rsrc_t;
DI u32x4 ld_sc1(rsrc_t r, u32 voff, u32 soff) { return __builtin_amdgcn_raw_buffer_load_b128(r, voff, soff, 16); }
DI void st_sc1(rsrc_t r, u32 voff, u32 soff, u32x4 v) { __builtin_amdgcn_raw_buffer_store_b128(v, r, voff, soff, 16); }
DI bf16x8 ld_frag1(rsrc_t r, u32 voff, u32 soff) { return __builtin_bit_cast(bf16x8, __builtin_amdgcn_raw_buffer_load_b128(r, voff, soff, 16)); }
DI bf16x8 ld_frag(rsrc_t r, u32 voff, u32 soff) { return __builtin_bit_cast(bf16x8, __builtin_amdgcn_raw_buffer_load_b128(r, voff, soff, 0)); }

DI int h2_swz(int k) { return ((k >> 1) & 1) | (((k >> 3) & 1) << 1); }

DI void conv_stage_k(char* smem, u32x4 tv0, u32x4 tv1, int w, int lane, int tid) {
  char* tb = smem + CV_T + w * KTAB;
  *(u32x4*)(tb + lane * 16) = tv0;
  if (lane < 17) *(u32x4*)(tb + (64 + lane) * 16) = tv1;
  wave_sync();
  const u16* T = (const u16*)tb;
#pragma unroll
  for (int i = 0; i < 2; ++i) {
    const int cp = tid + 256 * i;
    if (cp < 284) {
      u32 e[9];
#pragma unroll
      for (int j = 0; j < 9; ++j) {
        const int m = 2047 - 8 * cp - j;
        e[j] = T[m < 0 ? 641 : tap_index(m)];
      }
      *(u32x4*)(smem + CV_K + 4576 + cp * 16) = u32x4{e[0] | (e[1] << 16), e[2] | (e[3] << 16), e[4] | (e[5] << 16), e[6] | (e[7] << 16)};
      *(u32x4*)(smem + CV_K + cp * 16) = u32x4{e[1] | (e[2] << 16), e[3] | (e[4] << 16), e[5] | (e[6] << 16), e[7] | (e[8] << 16)};
    }
  }
}

DI void conv_stage_h(char* smem, u32x4 nq0, u32x4 nq1, float ca1, float sa, int tid) {
  u32 h[8];
#pragma unroll
  for (int k = 0; k < 4; ++k) {
    const u32 p0 = cvtpk(lo2f(nq0[k]) * ca1, hi2f(nq0[k]) * ca1);
    const u32 p1 = cvtpk(lo2f(nq1[k]) * ca1, hi2f(nq1[k]) * ca1);
    const u32 h0 = cvtpk(lo2f(p0) + sa, hi2f(p0) + sa);
    h[k] = tid > 0 ? h0 : 0u;
    h[4 + k] = cvtpk(lo2f(p1) + sa, hi2f(p1) + sa);
  }
  const int yo = (tid + 3) * 16;
  *(u32x4*)(smem + CV_H0 + yo) = u32x4{h[1], h[2], h[3], h[4]};
  *(u32x4*)(smem + CV_H0 + CV_HS + yo) = u32x4{h[2], h[3], h[4], h[5]};
  *(u32x4*)(smem + CV_H0 + 2 * CV_HS + yo) = u32x4{h[3], h[4], h[5], h[6]};
  *(u32x4*)(smem + CV_H3 + yo) = u32x4{h[4], h[5], h[6], h[7]};
  if (tid < 12) {
    const int cc = tid / 3, yc = tid % 3;
    *(u32x4*)(smem + (cc == 3 ? CV_H3 : CV_H0 + cc * CV_HS) + yc * 16) = u32x4{0u, 0u, 0u, 0u};
  }
}

DI void conv_mma(const char* smem, int w, int lane, f32x4 (&acc)[8]) {
  const int r16 = lane & 15, g = lane >> 4, cpy = r16 & 3;
  const char* hb = smem + (cpy == 3 ? CV_H3 : CV_H0 + cpy * CV_HS) + 16 * ((r16 >> 2) + 4 * w + g);
  const char* kb = smem + CV_K + (r16 & 1) * 4576 + 16 * (252 + 4 * w - 4 * (r16 >> 1) + g) - 256 * 14;
#pragma unroll
  for (int i = 0; i < 8; ++i) acc[i] = f32x4{0.f, 0.f, 0.f, 0.f};
#pragma unroll
  for (int par = 1; par >= 0; --par) {
    bf16x8 Bf[8];
#pragma unroll
    for (int t = 0; t < 8; ++t) Bf[t] = *(const bf16x8*)(hb + 256 * (2 * t + par));
#pragma unroll
    for (int sg = -par; sg <= 14; sg += 2) {
      const bf16x8 Af = *(const bf16x8*)(kb + 256 * (14 - sg));
#pragma unroll
      for (int i = 0; i < 8; ++i) {
        const int tau = 2 * i - sg;
        if (tau >= 0 && tau <= 15) acc[i] = mfma16(Af, Bf[tau >> 1], acc[i]);
      }
    }
  }
}

DI void conv_put(char* smem, int w, int lane, const f32x4 (&acc)[8]) {
  char* pb = smem + w * 8192 + (64 * (lane >> 4) + 2 * (lane & 15)) * 4;
#pragma unroll
  for (int i = 0; i < 8; ++i) {
    *(f32x2*)(pb + i * 1024) = f32x2{acc[i][0], acc[i][1]};
    *(f32x2*)(pb + i * 1024 + 128) = f32x2{acc[i][2], acc[i][3]};
  }
}

DI u32x4 conv_fin(const char* smem, int t, u32x4 xv, float dsk, float ga) {
  f32x4 y0 = *(const f32x4*)(smem + t * 32), y1 = *(const f32x4*)(smem + t * 32 + 16);
#pragma unroll
  for (int w = 1; w < 4; ++w) { y0 += *(const f32x4*)(smem + w * 8192 + t * 32); y1 += *(const f32x4*)(smem + w * 8192 + t * 32 + 16); }
  const u32x4 hv = *(const u32x4*)(smem + CV_H3 + 48 + t * 16);
  u32x4 o;
#pragma unroll
  for (int k = 0; k < 4; ++k) {
    const u32 yr = k < 2 ? cvtpk(y0[2 * k], y0[2 * k + 1]) : cvtpk(y1[2 * k - 4], y1[2 * k - 3]);
    const u32 hd = cvtpk(lo2f(hv[k]) * dsk, hi2f(hv[k]) * dsk);
    const u32 v = cvtpk(lo2f(yr) + lo2f(hd), hi2f(yr) + hi2f(hd));
    const u32 gv = cvtpk(lo2f(v) * ga, hi2f(v) * ga);
    o[k] = cvtpk(lo2f(xv[k]) + lo2f(gv), hi2f(xv[k]) + hi2f(gv));
  }
  return o;
}

template <int NW>
DI void poll_words(const u32* cnt, u32 target, int lane) {
  for (int spins = 0; spins < (1 << 20); ++spins) {
    u32 v = target;
    if (lane < NW) v = __hip_atomic_load(cnt + lane * 64, __ATOMIC_RELAXED, __HIP_MEMORY_SCOPE_AGENT);
    if (__all(v >= target)) break;
    __builtin_amdgcn_s_sleep(1);
  }
}

constexpr int W1_RING = 10;
constexpr int W2_MID = 4;

__global__ __launch_bounds__(512, 2) void k_main(const u16* __restrict__ Dsk, const u16* __restrict__ w2,
                                                    char* __restrict__ ws, u16* __restrict__ out) {
  __shared__ __attribute__((aligned(256))) char smem[MN_SMEM];
  const int tid = threadIdx.x, wv = __builtin_amdgcn_readfirstlane(tid >> 6), lane = tid & 63;
  const int r16 = lane & 15, g = lane >> 4, q = r16 >> 2, p = lane & 3;
  const int b = blockIdx.x, hw = wv >> 2, w4 = wv & 3, t256 = tid & 255;
  const rsrc_t rs = __builtin_amdgcn_make_buffer_rsrc((void*)ws, 0, 0x7fffffff, 0x00020000);
  u32* cnt = (u32*)(ws + WS_CNT);
  const int idx = b >> 3, tt = (b & 7) * 4 + (idx & 3), sl = idx >> 2;
  const int pbase = 7 * sl - (sl >> 2), pcnt = (sl & 3) == 3 ? 6 : 7;
  const bool active = wv < pcnt;
  const int p0 = pbase + (active ? wv : 0);
  TS(1, 0);

  const bool three = b < 128;
  const int d0 = three ? 3 * b : 384 + 2 * (b - 128);
  const int dA = d0 + hw, dC = three ? d0 + 2 : d0;
  bf16x8 af[W1_RING][2];
  const u32 l16 = lane * 16;
  const u32 w1o = (u32)WS_W1F + (u32)(2 * p0) * 20 * 1024;
  {
    const u16* nT = (const u16*)(ws + WS_NT);
    const u16* xT = (const u16*)(ws + WS_XT);
    const float* modp = (const float*)(ws + WS_MODP);
    const char* ktA = ws + WS_KT + (size_t)dA * KTAB;
    const u32x4 tA0 = *(const u32x4*)(ktA + lane * 16), tA1 = *(const u32x4*)(ktA + (lane < 17 ? 64 + lane : 0) * 16);
    const int mj = (lane & 31) >> 3;
    float mp = modp[(lane & 7) * (6 * D_) + (mj < 3 ? mj : 0) * D_ + (lane < 32 ? dA : dC)];
    const u32x4 nA0 = *(const u32x4*)(nT + (size_t)dA * L_ + 8 * (t256 > 0 ? t256 - 1 : 0));
    const u32x4 nA1 = *(const u32x4*)(nT + (size_t)dA * L_ + 8 * t256);
    u32x4 c0, c1;
    if (hw == 0) {
      const char* ktC = ws + WS_KT + (size_t)dC * KTAB;
      c0 = *(const u32x4*)(ktC + lane * 16); c1 = *(const u32x4*)(ktC + (lane < 17 ? 64 + lane : 0) * 16);
    } else {
      c0 = *(const u32x4*)(nT + (size_t)dC * L_ + 8 * (t256 > 0 ? t256 - 1 : 0));
      c1 = *(const u32x4*)(nT + (size_t)dC * L_ + 8 * t256);
    }
    const u32x4 xA = *(const u32x4*)(xT + (size_t)dA * L_ + 8 * t256);
    const u32x4 xC = *(const u32x4*)(xT + (size_t)dC * L_ + 8 * t256);
    const float dskA = bf2f(Dsk[dA]), dskC = bf2f(Dsk[dC]);
    float sbs = 0.f;
    {
      const float* sp = (const float*)(ws + WS_SB) + (lane >> 5) * 20 * F_ + 32 * p0 + (lane & 31);
#pragma unroll
      for (int k = 0; k < 20; ++k) sbs += sp[k * F_];
    }
    float gbs = 0.f;
    if (tid < 80) {
#pragma unroll
      for (int k = 0; k < KSM; ++k) gbs += modp[k * (6 * D_) + 5 * D_ + sl * 80 + tid];
    }
    char* smA = smem + hw * CV_CH;
    char* smC = smem + 2 * CV_CH;
    conv_stage_k(smA, tA0, tA1, w4, lane, t256);
    if (three && hw == 0) conv_stage_k(smC, c0, c1, w4, lane, t256);
    mp += __shfl_xor(mp, 1); mp += __shfl_xor(mp, 2); mp += __shfl_xor(mp, 4);
    mp = rb(mp);
    const float saA = __shfl(mp, 0), caA = __shfl(mp, 8), gaA = __shfl(mp, 16);
    const float saC = __shfl(mp, 32), caC = __shfl(mp, 40), gaC = __shfl(mp, 48);
    conv_stage_h(smA, nA0, nA1, rb(1.f + caA), saA, t256);
    if (three && hw == 1) conv_stage_h(smC, c0, c1, rb(1.f + caC), saC, t256);
    ((float*)(smem + MN_SB))[wv * 64 + lane] = sbs;
    if (tid < 80) ((float*)(smem + MN_GB))[tid] = rb(gbs);
    __builtin_amdgcn_sched_barrier(0);
#pragma unroll
    for (int ks = 0; ks < W1_RING; ++ks)
#pragma unroll
      for (int fb = 0; fb < 2; ++fb) af[ks][fb] = ld_frag(rs, l16, w1o + (fb * 20 + ks) * 1024);
    __builtin_amdgcn_sched_barrier(0);
    __syncthreads();
    TS(1, 1);
    f32x4 accA[8], accC[8];
    conv_mma(smA, w4, lane, accA);
    if (three && hw == 0) conv_mma(smC, w4, lane, accC);
    if (!three && wv < 3) {
      const int t2 = (b - 128) * 3 + wv;
      if (t2 < 270) {
        const int ks = t2 / 5, nb = t2 % 5;
        char* buf = smC + wv * 8192;
        u32x4 v[8];
#pragma unroll
        for (int i = 0; i < 8; ++i) v[i] = *(const u32x4*)(w2 + (size_t)(32 * ks + 4 * i + g) * D_ + 128 * nb + 8 * r16);
#pragma unroll
        for (int i = 0; i < 8; ++i) {
          const int row = 4 * i + g;
          *(u32x4*)(buf + row * 256 + (((r16 >> 1) ^ rl_swz(row)) << 5) + ((r16 & 1) << 4)) = v[i];
        }
        wave_sync();
        const int row0 = 4 * g + q, row1 = row0 + 16;
#pragma unroll
        for (int cb = 0; cb < 8; ++cb) {
          const s16x4 lo = tr_read(buf + row0 * 256 + ((cb ^ rl_swz(row0)) << 5) + 8 * p);
          const s16x4 hi = tr_read(buf + row1 * 256 + ((cb ^ rl_swz(row1)) << 5) + 8 * p);
          st_sc1(rs, l16, (u32)WS_W2F + ((8 * nb + cb) * 54 + ks) * 1024, __builtin_bit_cast(u32x4, cat8(lo, hi)));
        }
      }
    }
    __syncthreads();
    TS(1, 2);
    conv_put(smA, w4, lane, accA);
    if (three && hw == 0) conv_put(smC, w4, lane, accC);
    __syncthreads();
    st_sc1(rs, t256 * 16, (u32)WS_X1T + dA * (L_ * 2), conv_fin(smA, t256, xA, dskA, gaA));
    if (three && hw == 1) st_sc1(rs, t256 * 16, (u32)WS_X1T + dC * (L_ * 2), conv_fin(smC, t256, xC, dskC, gaC));
  }
  asm volatile("s_waitcnt vmcnt(0)" ::: "memory");
  __syncthreads();
  TS(1, 3);
  if (tid == 448) __hip_atomic_fetch_add(cnt + (b & 15) * 64, 1u, __ATOMIC_RELAXED, __HIP_MEMORY_SCOPE_AGENT);
  if (wv == 7) poll_words<16>(cnt, 16u, lane);
  __syncthreads();
  TS(1, 4);

  const int k0 = wv < 6 ? 7 * wv : 42 + 6 * (wv - 6), nk = wv < 6 ? 7 : 6;
  const u32 w2o = (u32)WS_W2F + (u32)((sl * 5) * 54 + k0) * 1024;
  bf16x8 wf[7][5];
  {
    const int l8 = tid & 7, crow = tid >> 3;
    u32x4 xv[10];
#pragma unroll
    for (int qq = 0; qq < 10; ++qq) xv[qq] = ld_sc1(rs, crow * (L_ * 2) + l8 * 16, (u32)WS_X1T + qq * (64 * L_ * 2) + tt * 128);
#pragma unroll
    for (int qq = 0; qq < 10; ++qq) {
      const int c = crow + 64 * qq;
      *(u32x4*)(smem + M1_A + c * 128 + (((l8 >> 1) ^ h2_swz(c)) << 5) + ((l8 & 1) << 4)) = xv[qq];
    }
    __syncthreads();
    TS(1, 5);
    TC(3, 0);
    f32x4 acc[2][4];
#pragma unroll
    for (int fb = 0; fb < 2; ++fb)
#pragma unroll
      for (int tb = 0; tb < 4; ++tb) acc[fb][tb] = f32x4{0.f, 0.f, 0.f, 0.f};
    const int row0 = 8 * g + q, sw = h2_swz(row0);
    const char* rb0 = smem + M1_A + row0 * 128 + 8 * p;
    float* st = (float*)(smem + M1_ST);
    auto ldb = [&](int ks, bf16x8 (&bfr)[4]) {
#pragma unroll
      for (int tb = 0; tb < 4; ++tb) {
        const s16x4 lo = tr_read(rb0 + ks * 4096 + ((tb ^ sw) << 5));
        const s16x4 hi = tr_read(rb0 + ks * 4096 + 512 + ((tb ^ sw) << 5));
        bfr[tb] = cat8(lo, hi);
      }
    };
    bf16x8 bb[2][4];
    ldb(0, bb[0]);
    const short one = 0x3f80;
    const bf16x8 ones = {one, one, one, one, one, one, one, one};
#pragma unroll
    for (int ks = 0; ks < 20; ++ks) {
      if (ks < 19) ldb(ks + 1, bb[(ks + 1) & 1]);
      if (active) {
#pragma unroll
        for (int fb = 0; fb < 2; ++fb)
#pragma unroll
          for (int tb = 0; tb < 4; ++tb) acc[fb][tb] = mfma16(af[ks % W1_RING][fb], bb[ks & 1][tb], acc[fb][tb]);
      } else {
#pragma unroll
        for (int tb = 0; tb < 4; ++tb) {
          acc[0][tb] = mfma16(ones, bb[ks & 1][tb], acc[0][tb]);
          acc[1][tb] = mfma16(bb[ks & 1][tb], bb[ks & 1][tb], acc[1][tb]);
        }
      }
      if (ks + W1_RING < 20) {
#pragma unroll
        for (int fb = 0; fb < 2; ++fb) af[ks % W1_RING][fb] = ld_frag(rs, l16, w1o + (fb * 20 + ks + W1_RING) * 1024);
      } else if (ks + W1_RING >= 20) {
#pragma unroll
        for (int j = 0; j < 2; ++j) {
          const int f = 2 * (ks + W1_RING - 20) + j;
          wf[f / 5][f % 5] = ld_frag1(rs, l16, w2o + ((f % 5) * 54 + f / 5) * 1024);
        }
      }
      __builtin_amdgcn_sched_barrier(0);
    }
    TS(2, 1);
    TC(3, 1);
    if (wv == 7 && g == q) {
#pragma unroll
      for (int tb = 0; tb < 4; ++tb) {
        const float sq = p == 0 ? acc[1][tb][0] : p == 1 ? acc[1][tb][1] : p == 2 ? acc[1][tb][2] : acc[1][tb][3];
        const float mean = acc[0][tb][0] * (1.f / D_);
        const float var = fmaxf(sq * (1.f / D_) - mean * mean, 0.f);
        st[tb * 16 + r16] = rb(mean);
        st[64 + tb * 16 + r16] = 1.f / rb(sqrtf(rb(rb(var) + 1e-5f)));
      }
    }
#pragma unroll
    for (int i = 0; i < 2; ++i) {
      const int id = tid + 512 * i;
      if (id < 640) {
        const int c = id >> 3, o = id & 7, cg = sl * 80 + c;
        const u32x4 v = *(const u32x4*)(smem + M1_A + cg * 128 + (((o >> 1) ^ h2_swz(cg)) << 5) + ((o & 1) << 4));
#pragma unroll
        for (int k = 0; k < 4; ++k) {
          *(u16*)(smem + M2_X + (8 * o + 2 * k) * M2_XS + 2 * c) = (u16)(v[k] & 0xffffu);
          *(u16*)(smem + M2_X + (8 * o + 2 * k + 1) * M2_XS + 2 * c) = (u16)(v[k] >> 16);
        }
      }
    }
    __syncthreads();
    TS(2, 2);
    if (active) {
      const float* sbp = (const float*)(smem + MN_SB) + wv * 64 + 4 * g;
      const u32 hoff = (u32)WS_HID + (u32)((tt * 4) * 54 + p0) * 1024;
#pragma unroll
      for (int tb = 0; tb < 4; ++tb) {
        const float mb = st[tb * 16 + r16], rsd = st[64 + tb * 16 + r16];
        u32x4 v;
#pragma unroll
        for (int hh = 0; hh < 2; ++hh) {
          const f32x4 sv = *(const f32x4*)(sbp + 16 * hh), bv = *(const f32x4*)(sbp + 32 + 16 * hh);
          const f32x4 a = (acc[hh][tb] - mb * sv) * rsd + bv;
          v[2 * hh] = cvtpk(fmaxf(a[0], 0.f), fmaxf(a[1], 0.f));
          v[2 * hh + 1] = cvtpk(fmaxf(a[2], 0.f), fmaxf(a[3], 0.f));
        }
        st_sc1(rs, l16, hoff + tb * 54 * 1024, v);
      }
    }
  }
  TS(2, 3);
  asm volatile("s_waitcnt vmcnt(0)" ::: "memory");
  __syncthreads();
  TS(2, 4);
  if (tid == 448) __hip_atomic_fetch_add(cnt + (16 + tt) * 64, 1u, __ATOMIC_RELAXED, __HIP_MEMORY_SCOPE_AGENT);
  __builtin_amdgcn_sched_barrier(0);
#pragma unroll
  for (int i = W2_MID; i < 7; ++i)
#pragma unroll
    for (int cb = 0; cb < 5; ++cb) wf[i][cb] = ld_frag1(rs, l16, w2o + (cb * 54 + (i < 6 || nk == 7 ? i : 5)) * 1024);
  __builtin_amdgcn_sched_barrier(0);
  if (wv == 7) poll_words<1>(cnt + (16 + tt) * 64, 8u, lane);
  __syncthreads();
  TS(1, 6);

  {
    const u32 hoff = (u32)WS_HID + (u32)((tt * 4) * 54 + k0) * 1024;
    u32x4 hf[2][4];
#pragma unroll
    for (int i = 0; i < 2; ++i)
#pragma unroll
      for (int tb = 0; tb < 4; ++tb) hf[i][tb] = ld_sc1(rs, l16, hoff + (tb * 54 + i) * 1024);
    f32x4 acc[5][4];
#pragma unroll
    for (int cb = 0; cb < 5; ++cb)
#pragma unroll
      for (int tb = 0; tb < 4; ++tb) acc[cb][tb] = f32x4{0.f, 0.f, 0.f, 0.f};
#pragma unroll
    for (int i = 0; i < 7; ++i) {
      if (i < 6 || nk == 7) {
#pragma unroll
        for (int tb = 0; tb < 4; ++tb)
#pragma unroll
          for (int cb = 0; cb < 5; ++cb) acc[cb][tb] = mfma16(wf[i][cb], __builtin_bit_cast(bf16x8, hf[i & 1][tb]), acc[cb][tb]);
      }
      __builtin_amdgcn_sched_barrier(0);
      if (i + 2 < 7) {
        const int kk = (i + 2 < 6 || nk == 7) ? i + 2 : 5;
#pragma unroll
        for (int tb = 0; tb < 4; ++tb) hf[i & 1][tb] = ld_sc1(rs, l16, hoff + (tb * 54 + kk) * 1024);
        __builtin_amdgcn_sched_barrier(0);
      }
    }
    TS(2, 5);
#pragma unroll
    for (int tb = 0; tb < 4; ++tb)
#pragma unroll
      for (int cb = 0; cb < 5; ++cb) {
        const int owner = tb + (cb < 3 ? 0 : 4);
        if (owner != wv) {
          const int slot = wv < owner ? wv : wv - 1;
          *(f32x4*)(smem + M2_RED + (((tb * 5 + cb) * 7 + slot) * 64 + lane) * 16) = acc[cb][tb];
        }
      }
    __syncthreads();
    TS(2, 6);
    const int tok = 16 * w4 + r16;
#pragma unroll
    for (int j = 0; j < 3; ++j) {
      if (j < 2 || wv < 4) {
        const int cb = wv < 4 ? j : 3 + j;
        f32x4 y;
#pragma unroll
        for (int tb = 0; tb < 4; ++tb)
#pragma unroll
          for (int c2 = 0; c2 < 5; ++c2) if (tb == w4 && c2 == cb) y = acc[c2][tb];
#pragma unroll
        for (int s7 = 0; s7 < 7; ++s7) y += *(const f32x4*)(smem + M2_RED + (((w4 * 5 + cb) * 7 + s7) * 64 + lane) * 16);
        const f32x4 gb4 = *(const f32x4*)(smem + MN_GB + (16 * cb + 4 * g) * 4);
        const float g0 = gb4[0], g1 = gb4[1], g2 = gb4[2], g3 = gb4[3];
        const u32x2 xr = *(const u32x2*)(smem + M2_X + tok * M2_XS + 2 * (16 * cb + 4 * g));
        const u32 y0 = cvtpk(y[0], y[1]), y1 = cvtpk(y[2], y[3]);
        const u32 v0 = cvtpk(lo2f(y0) * g0, hi2f(y0) * g1), v1 = cvtpk(lo2f(y1) * g2, hi2f(y1) * g3);
        u32x2 o;
        o[0] = cvtpk(lo2f(xr[0]) + lo2f(v0), hi2f(xr[0]) + hi2f(v0));
        o[1] = cvtpk(lo2f(xr[1]) + lo2f(v1), hi2f(xr[1]) + hi2f(v1));
        *(u32x2*)(out + (size_t)(tt * 64 + tok) * D_ + sl * 80 + 16 * cb + 4 * g) = o;
      }
    }
  }
  TS(1, 7);
}

extern "C" void kernel_launch(void* const* d_in, const int* in_sizes, int n_in, void* d_out, int out_size, void* d_ws,
                              size_t ws_size, hipStream_t stream) {
  char* ws = (char*)d_ws;
  k_pre<<<NB_LN + NB_R1 + NB_KG + NB_MOD, 256, 0, stream>>>(
      (const u16*)d_in[0], (const u16*)d_in[1], (const u16*)d_in[2], (const u16*)d_in[3], (const u16*)d_in[4],
      (const u16*)d_in[5], (const u16*)d_in[6], (const u16*)d_in[7], (const u16*)d_in[10],
      (const u16*)d_in[12], (const u16*)d_in[13], ws);
  k_main<<<256, 512, 0, stream>>>((const u16*)d_in[8], (const u16*)d_in[11], ws, (u16*)d_out);
}
```

```cpp
#include <hip/hip_runtime.h>
#include <hip/hip_bf16.h>
#include <stdint.h>

typedef unsigned short u16;
typedef unsigned int u32;
typedef __attribute__((ext_vector_type(8))) short bf16x8;
typedef __attribute__((ext_vector_type(4))) short s16x4;
typedef __attribute__((ext_vector_type(4))) float f32x4;
typedef __attribute__((ext_vector_type(2))) float f32x2;
typedef __attribute__((ext_vector_type(4))) unsigned int u32x4;
typedef __attribute__((ext_vector_type(2))) unsigned int u32x2;
typedef __bf16 bf16x2_t __attribute__((ext_vector_type(2)));
#define LDS_AS __attribute__((address_space(3)))
#define DI __device__ __forceinline__

constexpr int L_ = 2048, D_ = 640, N_ = 128, F_ = 1728;
constexpr int KSM = 8;

constexpr size_t WS_MODP = 0;
constexpr size_t WS_CNT = 126976;
constexpr size_t WS_NT = 262144;
constexpr size_t WS_XT = WS_NT + (size_t)D_ * L_ * 2;
constexpr size_t WS_X1T = WS_XT + (size_t)D_ * L_ * 2;
constexpr size_t WS_X1S = WS_X1T + (size_t)D_ * L_ * 2;
constexpr int KTAB = 1296;
constexpr size_t WS_KT = WS_X1S + (size_t)D_ * L_ * 2;
constexpr size_t WS_SB = WS_KT + (size_t)D_ * KTAB;
constexpr size_t WS_W1F = WS_SB + 2 * 20 * 1728 * 4;
constexpr size_t WS_W2F = WS_W1F + (size_t)108 * 20 * 1024;
constexpr size_t WS_HID = WS_W2F + (size_t)40 * 54 * 1024;
constexpr size_t WS_ST = WS_HID + (size_t)128 * 54 * 1024;

#define TS(kern, slot) do {} while (0)
#define TC(kern, slot) do {} while (0)

DI u32 cvtpk(float lo, float hi) {
  f32x2 v = {lo, hi};
  bf16x2_t r = __builtin_convertvector(v, bf16x2_t);
  return __builtin_bit_cast(u32, r);
}
typedef _Float16 f16x2_t __attribute__((ext_vector_type(2)));
typedef _Float16 f16x8_t __attribute__((ext_vector_type(8)));
DI u32 cvtph(float lo, float hi) {
  f32x2 v = {lo, hi};
  f16x2_t r = __builtin_convertvector(v, f16x2_t);
  return __builtin_bit_cast(u32, r);
}
DI float dot2bf(u32 a, u32 b, float c) {
  return __builtin_amdgcn_fdot2_f32_bf16(__builtin_bit_cast(bf16x2_t, a), __builtin_bit_cast(bf16x2_t, b), c, false);
}
DI float lo2f(u32 v) { return __uint_as_float(v << 16); }
DI float hi2f(u32 v) { return __uint_as_float(v & 0xffff0000u); }
DI float bf2f(u16 v) { return __uint_as_float(((u32)v) << 16); }
DI float rb(float x) { return lo2f(cvtpk(x, x)); }
DI f32x4 mfma16(bf16x8 a, bf16x8 b, f32x4 c) { return __builtin_amdgcn_mfma_f32_16x16x32_bf16(a, b, c, 0, 0, 0); }
DI s16x4 tr_read(const char* p) {
  return __builtin_amdgcn_ds_read_tr16_b64_v4i16((LDS_AS s16x4*)p);
}
DI bf16x8 cat8(s16x4 a, s16x4 b) { return __builtin_shufflevector(a, b, 0, 1, 2, 3, 4, 5, 6, 7); }
DI void wave_sync() {
  __builtin_amdgcn_fence(__ATOMIC_RELEASE, "wavefront");
  __builtin_amdgcn_wave_barrier();
  __builtin_amdgcn_fence(__ATOMIC_ACQUIRE, "wavefront");
}

struct c32 { float r, i; };
DI c32 cmul(c32 a, c32 b) { return {a.r * b.r - a.i * b.i, a.r * b.i + a.i * b.r}; }
DI c32 cexp_scaled(float ar, float ai, float e) {
  const float mag = __expf(ar * e);
  const float x = ai * e;
  const float C_HI = 0.15915494f, C_LO = 6.4206382e-9f;
  const float r = x * C_HI;
  const float rl = fmaf(x, C_HI, -r) + x * C_LO;
  const float fr = (r - floorf(r)) + rl;
  return {mag * __builtin_amdgcn_cosf(fr), mag * __builtin_amdgcn_sinf(fr)};
}
DI int tap_index(int m) {
  u32 bits = __float_as_uint((float)m);
  bits += 0x7fffu + ((bits >> 16) & 1u);
  const int id2 = (int)(bits >> 16) - (0x4300 - 128);
  return m < 128 ? m : id2;
}

constexpr int NB_LN = 64, NB_KG = 320, NB_MOD = 30 * KSM, NB_RL = 138;
constexpr int PRE_SMEM = 43008;

DI void role_ln1(int b, const u16* __restrict__ x, u16* __restrict__ nT, u16* __restrict__ xT, char* smem) {
  const int tid = threadIdx.x, u = tid >> 4, j = tid & 15;
  const int t0 = b * 32;
  const u32x4* xa = (const u32x4*)(x + (size_t)(t0 + 2 * u) * D_);
  u32x4 A[5], B[5];
#pragma unroll
  for (int q = 0; q < 5; ++q) { A[q] = xa[j + 16 * q]; B[q] = xa[80 + j + 16 * q]; }
  float sa = 0.f, sb = 0.f, qa = 0.f, qb = 0.f;
#pragma unroll
  for (int q = 0; q < 5; ++q)
#pragma unroll
    for (int k = 0; k < 4; ++k) {
      sa = dot2bf(A[q][k], 0x3f803f80u, sa); qa = dot2bf(A[q][k], A[q][k], qa);
      sb = dot2bf(B[q][k], 0x3f803f80u, sb); qb = dot2bf(B[q][k], B[q][k], qb);
    }
#pragma unroll
  for (int o = 1; o < 16; o <<= 1) {
    sa += __shfl_xor(sa, o, 16); sb += __shfl_xor(sb, o, 16); qa += __shfl_xor(qa, o, 16); qb += __shfl_xor(qb, o, 16);
  }
  const float ma = sa * (1.f / D_), mb = sb * (1.f / D_);
  const float va = fmaxf(qa - sa * ma, 0.f), vb = fmaxf(qb - sb * mb, 0.f);
  const float mra = rb(ma), mrb = rb(mb);
  const float ra = 1.f / rb(sqrtf(rb(rb(va * (1.f / D_)) + 1e-5f)));
  const float rbb = 1.f / rb(sqrtf(rb(rb(vb * (1.f / D_)) + 1e-5f)));
  char* wbase = smem + (8 * j + (j >> 2)) * 64 + ((((u >> 2) ^ (j & 3)) << 2) + (u & 3)) * 4;
#pragma unroll
  for (int q = 0; q < 5; ++q)
#pragma unroll
    for (int k = 0; k < 4; ++k) {
      const u32 c0 = cvtpk(lo2f(A[q][k]) - mra, lo2f(B[q][k]) - mrb);
      const u32 c1 = cvtpk(hi2f(A[q][k]) - mra, hi2f(B[q][k]) - mrb);
      *(u32*)(wbase + (132 * q + 2 * k) * 64) = cvtpk(lo2f(c0) * ra, hi2f(c0) * rbb);
      *(u32*)(wbase + (132 * q + 2 * k + 1) * 64) = cvtpk(lo2f(c1) * ra, hi2f(c1) * rbb);
    }
  __syncthreads();
#pragma unroll
  for (int i = 0; i < 10; ++i) {
    const int id = tid + 256 * i, c = id >> 2, o = id & 3;
    const u32x4 v = *(const u32x4*)(smem + (c + (c >> 5)) * 64 + ((o ^ ((c >> 3) & 3)) << 4));
    *(u32x4*)(nT + (size_t)c * L_ + t0 + 8 * o) = v;
  }
  __syncthreads();
#pragma unroll
  for (int q = 0; q < 5; ++q)
#pragma unroll
    for (int k = 0; k < 4; ++k) {
      *(u32*)(wbase + (132 * q + 2 * k) * 64) = (A[q][k] & 0xffffu) | (B[q][k] << 16);
      *(u32*)(wbase + (132 * q + 2 * k + 1) * 64) = (A[q][k] >> 16) | (B[q][k] & 0xffff0000u);
    }
  __syncthreads();
#pragma unroll
  for (int i = 0; i < 10; ++i) {
    const int id = tid + 256 * i, c = id >> 2, o = id & 3;
    const u32x4 v = *(const u32x4*)(smem + (c + (c >> 5)) * 64 + ((o ^ ((c >> 3) & 3)) << 4));
    *(u32x4*)(xT + (size_t)c * L_ + t0 + 8 * o) = v;
  }
}

DI void role_kgen(int bk, const u16* __restrict__ Are, const u16* __restrict__ Aim, const u16* __restrict__ Bre,
                  const u16* __restrict__ Bim, const u16* __restrict__ Cre, const u16* __restrict__ Cim,
                  const u16* __restrict__ ldt, char* __restrict__ KT, char* smem) {
  const int tid = threadIdx.x, w = tid >> 6, lane = tid & 63, r16 = lane & 15, g = lane >> 4;
  const int d = 2 * bk + (w >> 1), n = (w & 1) * 64 + lane;
  const int gi = d * N_ + n;
  const float dt = rb(expf(bf2f(ldt[d])));
  const float ar = dt * bf2f(Are[gi]), ai = dt * bf2f(Aim[gi]);
  const c32 dB = {dt * bf2f(Bre[gi]), dt * bf2f(Bim[gi])};
  const c32 Cc = {bf2f(Cre[gi]), bf2f(Cim[gi])};
  const c32 W = cmul(Cc, dB);
  const c32 E1 = cexp_scaled(ar, ai, 1.f), E8 = cexp_scaled(ar, ai, 8.f), E128 = cexp_scaled(ar, ai, 128.f);

  float wmax = fmaxf(fabsf(W.r), fabsf(W.i));
#pragma unroll
  for (int o = 1; o < 64; o <<= 1) wmax = fmaxf(wmax, __shfl_xor(wmax, o));
  int ew = (int)((__float_as_uint(wmax) >> 23) & 0xff);
  ew = ew < 20 ? 20 : (ew > 240 ? 240 : ew);
  const float wsc = __uint_as_float((u32)(127 + 10 + 127 - ew - 1) << 23);
  const float unsc = __uint_as_float((u32)(ew + 1 - 20) << 23);

  char* buf = smem + w * 8704;
  char* wr = buf + lane * 4;
  const char* rd = buf + r16 * 272 + g * 16;
  auto put = [&](int row, float re, float im) { *(u32*)(wr + row * 272) = cvtph(re, im); };
  {
    c32 p = {1024.f, 0.f};
#pragma unroll
    for (int jj = 0; jj < 16; ++jj) { put(jj, p.r, p.i); if (jj < 15) p = cmul(p, E8); }
  }
  wave_sync();
  f16x8_t bh[4];
#pragma unroll
  for (int ks = 0; ks < 4; ++ks) bh[ks] = *(const f16x8_t*)(rd + ks * 64);
  wave_sync();
  c32 sm[8];
  sm[0] = {1.f, 0.f}; sm[1] = E1;
#pragma unroll
  for (int i = 2; i < 8; ++i) sm[i] = cmul(sm[i - 1], E1);
  c32 WG[17];
  WG[0] = {W.r * wsc, W.i * wsc};
#pragma unroll
  for (int i = 1; i < 17; ++i) WG[i] = cmul(WG[i - 1], E128);
  f32x4 acc[3];
  auto tile_mma = [&](int t) {
    wave_sync();
    f32x4 a = {0.f, 0.f, 0.f, 0.f};
#pragma unroll
    for (int ks = 0; ks < 4; ++ks) a = __builtin_amdgcn_mfma_f32_16x16x32_f16(*(const f16x8_t*)(rd + ks * 64), bh[ks], a, 0, 0, 0);
    acc[t] = a * unsc;
    wave_sync();
  };
#pragma unroll
  for (int i = 0; i < 2; ++i)
#pragma unroll
    for (int rho = 0; rho < 8; ++rho) { const c32 v = rho ? cmul(WG[i], sm[rho]) : WG[i]; put(8 * i + rho, v.r, -v.i); }
  tile_mma(0);
#pragma unroll
  for (int i = 0; i < 2; ++i)
#pragma unroll
    for (int rho = 0; rho < 4; ++rho) { const c32 v = rho ? cmul(WG[2 + i], sm[2 * rho]) : WG[2 + i]; put(4 * i + rho, v.r, -v.i); }
#pragma unroll
  for (int i = 0; i < 4; ++i)
#pragma unroll
    for (int rho = 0; rho < 2; ++rho) { const c32 v = rho ? cmul(WG[4 + i], sm[4]) : WG[4 + i]; put(8 + 2 * i + rho, v.r, -v.i); }
  tile_mma(1);
#pragma unroll
  for (int i = 0; i < 9; ++i) put(i, WG[8 + i].r, -WG[8 + i].i);
  tile_mma(2);

#pragma unroll
  for (int t = 0; t < 3; ++t) *(f32x4*)(buf + t * 1024 + lane * 16) = acc[t];
  __syncthreads();
  u16* T = (u16*)(smem + (w >> 1) * 2 * 8704 + 4352);
  {
    const char* ob = smem + (w ^ 1) * 8704 + lane * 16;
    if (!(w & 1)) {
      const f32x4 a0 = acc[0] + *(const f32x4*)(ob), a2 = acc[2] + *(const f32x4*)(ob + 2048);
      u16* t0 = T + 128 * (g >> 1) + 4 * (g & 1) + 8 * r16;
      *(u32x2*)t0 = u32x2{cvtpk(a0[0], a0[1]), cvtpk(a0[2], a0[3])};
      if (g < 2) {
#pragma unroll
        for (int rg = 0; rg < 4; ++rg) T[512 + 16 * (4 * g + rg) + r16] = (u16)cvtpk(a2[rg], 0.f);
      } else if (lane == 32) {
        T[640] = (u16)cvtpk(a2[0], 0.f);
      } else if (lane >= 48 && lane < 55) {
        T[641 + lane - 48] = 0;
      }
    } else {
      const f32x4 a1 = acc[1] + *(const f32x4*)(ob + 1024);
      if (g < 2) {
        *(u32x2*)(T + 256 + 64 * g + 4 * r16) = u32x2{cvtpk(a1[0], a1[1]), cvtpk(a1[2], a1[3])};
      } else {
        *(u32*)(T + 384 + 64 * (g - 2) + 2 * r16) = cvtpk(a1[0], a1[1]);
        *(u32*)(T + 384 + 64 * (g - 2) + 32 + 2 * r16) = cvtpk(a1[2], a1[3]);
      }
    }
  }
  __syncthreads();
  const int t128 = tid & 127;
  if (t128 < 81) *(u32x4*)(KT + (size_t)d * KTAB + t128 * 16) = *(const u32x4*)((const char*)T + t128 * 16);
}

DI void role_mod(int bm, const u16* __restrict__ c, const u16* __restrict__ wm, float* __restrict__ modp, char* smem) {
  const int tid = threadIdx.x, cj = tid & 15, rg = tid >> 4, w = tid >> 6;
  const int cbk = bm % 30, ks = bm / 30;
  u32x4 v[5];
  float s[5];
#pragma unroll
  for (int i = 0; i < 5; ++i) {
    const int row = 80 * ks + rg + 16 * i;
    v[i] = *(const u32x4*)(wm + (size_t)row * (6 * D_) + 128 * cbk + 8 * cj);
    const float cv = bf2f(c[row]);
    s[i] = rb(cv / (1.f + expf(-cv)));
  }
  float a[8];
#pragma unroll
  for (int k = 0; k < 8; ++k) a[k] = 0.f;
#pragma unroll
  for (int i = 0; i < 5; ++i)
#pragma unroll
    for (int k = 0; k < 4; ++k) { a[2 * k] += s[i] * lo2f(v[i][k]); a[2 * k + 1] += s[i] * hi2f(v[i][k]); }
#pragma unroll
  for (int k = 0; k < 8; ++k) { a[k] += __shfl_xor(a[k], 16); a[k] += __shfl_xor(a[k], 32); }
  float* red = (float*)smem;
  if ((tid & 63) < 16) {
#pragma unroll
    for (int k = 0; k < 8; ++k) red[w * 128 + 8 * cj + k] = a[k];
  }
  __syncthreads();
  if (tid < 128) modp[ks * (6 * D_) + 128 * cbk + tid] = red[tid] + red[128 + tid] + red[256 + tid] + red[384 + tid];
}

DI int rl_swz(int row) { return (row & 3) | ((((row >> 2) ^ (row >> 3)) & 1) << 2); }

DI void role_relayout(int br, const u16* __restrict__ w1, const u16* __restrict__ w2, char* __restrict__ ws, char* smem) {
  const int tid = threadIdx.x, w = tid >> 6, lane = tid & 63;
  const int tile = br * 4 + w;
  if (tile >= 550) return;
  const bool is1 = tile < 280;
  int ks, nb, ld, nblk;
  const u16* src;
  if (is1) { ks = tile / 14; nb = tile % 14; ld = F_; src = w1; nblk = nb == 13 ? 4 : 8; }
  else { const int t2 = tile - 280; ks = t2 / 5; nb = t2 % 5; ld = D_; src = w2; nblk = 8; }
  char* buf = smem + w * 8192;
  const int lr = lane >> 4, lc = lane & 15;
  u32x4 v[8];
#pragma unroll
  for (int i = 0; i < 8; ++i) v[i] = *(const u32x4*)(src + (size_t)(32 * ks + 4 * i + lr) * ld + 128 * nb + 8 * (lc < 2 * nblk ? lc : 0));
#pragma unroll
  for (int i = 0; i < 8; ++i) {
    const int row = 4 * i + lr;
    *(u32x4*)(buf + row * 256 + (((lc >> 1) ^ rl_swz(row)) << 5) + ((lc & 1) << 4)) = v[i];
  }
  wave_sync();
  const int g = lane >> 4, q = (lane & 15) >> 2, p = lane & 3;
  const int row0 = is1 ? 8 * g + q : 4 * g + q, row1 = is1 ? row0 + 4 : row0 + 16;
  char* dst = is1 ? ws + WS_W1F + ((size_t)(8 * nb) * 20 + ks) * 1024 : ws + WS_W2F + ((size_t)(8 * nb) * 54 + ks) * 1024;
  const int dstride = is1 ? 20 * 1024 : 54 * 1024;
#pragma unroll
  for (int cb = 0; cb < 8; ++cb) {
    const s16x4 lo = tr_read(buf + row0 * 256 + ((cb ^ rl_swz(row0)) << 5) + 8 * p);
    const s16x4 hi = tr_read(buf + row1 * 256 + ((cb ^ rl_swz(row1)) << 5) + 8 * p);
    if (cb < nblk) *(bf16x8*)(dst + (size_t)cb * dstride + lane * 16) = cat8(lo, hi);
  }
}

__global__ __launch_bounds__(256) void k_pre(const u16* __restrict__ x, const u16* __restrict__ Are, const u16* __restrict__ Aim,
                                             const u16* __restrict__ Bre, const u16* __restrict__ Bim, const u16* __restrict__ Cre,
                                             const u16* __restrict__ Cim, const u16* __restrict__ ldt, const u16* __restrict__ w1,
                                             const u16* __restrict__ w2, const u16* __restrict__ c, const u16* __restrict__ wm,
                                             char* __restrict__ ws) {
  __shared__ __attribute__((aligned(16))) char smem[PRE_SMEM];
  int b = blockIdx.x;
  TS(0, 0);
  if (b < NB_LN) {
    if (b < 34 && threadIdx.x < 64) ((u32*)(ws + WS_CNT))[(b * 8 + (threadIdx.x >> 3)) * 64 + (threadIdx.x & 7)] = 0u;
    *(u32x2*)(ws + WS_ST + (b * 256 + threadIdx.x) * 8) = u32x2{0u, 0u};
    role_ln1(b, x, (u16*)(ws + WS_NT), (u16*)(ws + WS_XT), smem); TS(0, 7); return;
  }
  b -= NB_LN;
  if (b < NB_KG) { role_kgen(b, Are, Aim, Bre, Bim, Cre, Cim, ldt, ws + WS_KT, smem); TS(0, 7); return; }
  b -= NB_KG;
  if (b < NB_MOD) { role_mod(b, c, wm, (float*)(ws + WS_MODP), smem); TS(0, 7); return; }
  b -= NB_MOD;
  role_relayout(b, w1, w2, ws, smem);
  TS(0, 7);
}

constexpr int CV_H0 = 0, CV_HS = 4160, CV_K = 12544, CV_T = 21760, CV_H3 = 32768 + 192, CV_CH = 37376;
static_assert(CV_CH % 256 == 0 && CV_H3 + 4160 <= CV_CH && CV_T + 4 * KTAB <= 32768, "conv lds");
constexpr int M1_A = 0, M1_ST = 81920;
constexpr int MN_SB = 3 * CV_CH;
constexpr int MN_R1 = MN_SB + 2048;
constexpr int M2_RED = 0, M2_X = 143360, M2_XS = 176;
constexpr int MN_GB = M2_X + 64 * M2_XS;
constexpr int MN_SMEM = MN_GB + 320;
static_assert(MN_R1 + 5 * 4864 <= M2_X && M1_ST + 512 <= MN_SB, "mlp lds");

typedef __amdgpu_buffer_rsrc_t rsrc_t;
DI u32x4 ld_sc1(rsrc_t r, u32 voff, u32 soff) { return __builtin_amdgcn_raw_buffer_load_b128(r, voff, soff, 16); }
DI void st_sc1(rsrc_t r, u32 voff, u32 soff, u32x4 v) { __builtin_amdgcn_raw_buffer_store_b128(v, r, voff + soff, 0, 16); }
DI bf16x8 ld_frag1(rsrc_t r, u32 voff, u32 soff) { return __builtin_bit_cast(bf16x8, __builtin_amdgcn_raw_buffer_load_b128(r, voff, soff, 16)); }
DI bf16x8 ld_frag(rsrc_t r, u32 voff, u32 soff) { return __builtin_bit_cast(bf16x8, __builtin_amdgcn_raw_buffer_load_b128(r, voff, soff, 0)); }

DI int h2_swz(int k) { return ((k >> 1) & 1) | (((k >> 3) & 1) << 1); }

DI void conv_stage_k(char* smem, u32x4 tv0, u32x4 tv1, int w, int lane, int tid) {
  char* tb = smem + CV_T + w * KTAB;
  *(u32x4*)(tb + lane * 16) = tv0;
  if (lane < 17) *(u32x4*)(tb + (64 + lane) * 16) = tv1;
  wave_sync();
  const u16* T = (const u16*)tb;
#pragma unroll
  for (int i = 0; i < 2; ++i) {
    const int cp = tid + 256 * i;
    if (cp < 284) {
      u32 e[9];
#pragma unroll
      for (int j = 0; j < 9; ++j) {
        const int m = 2047 - 8 * cp - j;
        e[j] = T[m < 0 ? 641 : tap_index(m)];
      }
      *(u32x4*)(smem + CV_K + 4576 + cp * 16) = u32x4{e[0] | (e[1] << 16), e[2] | (e[3] << 16), e[4] | (e[5] << 16), e[6] | (e[7] << 16)};
      *(u32x4*)(smem + CV_K + cp * 16) = u32x4{e[1] | (e[2] << 16), e[3] | (e[4] << 16), e[5] | (e[6] << 16), e[7] | (e[8] << 16)};
    }
  }
}

DI void conv_stage_h(char* smem, u32x4 nq0, u32x4 nq1, float ca1, float sa, int tid) {
  u32 h[8];
#pragma unroll
  for (int k = 0; k < 4; ++k) {
    const u32 p0 = cvtpk(lo2f(nq0[k]) * ca1, hi2f(nq0[k]) * ca1);
    const u32 p1 = cvtpk(lo2f(nq1[k]) * ca1, hi2f(nq1[k]) * ca1);
    const u32 h0 = cvtpk(lo2f(p0) + sa, hi2f(p0) + sa);
    h[k] = tid > 0 ? h0 : 0u;
    h[4 + k] = cvtpk(lo2f(p1) + sa, hi2f(p1) + sa);
  }
  const int yo = (tid + 3) * 16;
  *(u32x4*)(smem + CV_H0 + yo) = u32x4{h[1], h[2], h[3], h[4]};
  *(u32x4*)(smem + CV_H0 + CV_HS + yo) = u32x4{h[2], h[3], h[4], h[5]};
  *(u32x4*)(smem + CV_H0 + 2 * CV_HS + yo) = u32x4{h[3], h[4], h[5], h[6]};
  *(u32x4*)(smem + CV_H3 + yo) = u32x4{h[4], h[5], h[6], h[7]};
  if (tid < 12) {
    const int cc = tid / 3, yc = tid % 3;
    *(u32x4*)(smem + (cc == 3 ? CV_H3 : CV_H0 + cc * CV_HS) + yc * 16) = u32x4{0u, 0u, 0u, 0u};
  }
}

DI void conv_mma(const char* smem, int w, int lane, f32x4 (&acc)[8]) {
  const int r16 = lane & 15, g = lane >> 4, cpy = r16 & 3;
  const char* hb = smem + (cpy == 3 ? CV_H3 : CV_H0 + cpy * CV_HS) + 16 * ((r16 >> 2) + 4 * w + g);
  const char* kb = smem + CV_K + (r16 & 1) * 4576 + 16 * (252 + 4 * w - 4 * (r16 >> 1) + g) - 256 * 14;
#pragma unroll
  for (int i = 0; i < 8; ++i) acc[i] = f32x4{0.f, 0.f, 0.f, 0.f};
#pragma unroll
  for (int par = 1; par >= 0; --par) {
    bf16x8 Bf[8];
#pragma unroll
    for (int t = 0; t < 8; ++t) Bf[t] = *(const bf16x8*)(hb + 256 * (2 * t + par));
#pragma unroll
    for (int sg = -par; sg <= 14; sg += 2) {
      const bf16x8 Af = *(const bf16x8*)(kb + 256 * (14 - sg));
#pragma unroll
      for (int i = 0; i < 8; ++i) {
        const int tau = 2 * i - sg;
        if (tau >= 0 && tau <= 15) acc[i] = mfma16(Af, Bf[tau >> 1], acc[i]);
      }
    }
  }
}

DI void conv_put(char* smem, int w, int lane, const f32x4 (&acc)[8]) {
  char* pb = smem + w * 8192 + (64 * (lane >> 4) + 2 * (lane & 15)) * 4;
#pragma unroll
  for (int i = 0; i < 8; ++i) {
    *(f32x2*)(pb + i * 1024) = f32x2{acc[i][0], acc[i][1]};
    *(f32x2*)(pb + i * 1024 + 128) = f32x2{acc[i][2], acc[i][3]};
  }
}

DI u32x4 conv_fin(const char* smem, int t, u32x4 xv, float dsk, float ga, float c1, u32x4& os) {
  f32x4 y0 = *(const f32x4*)(smem + t * 32), y1 = *(const f32x4*)(smem + t * 32 + 16);
#pragma unroll
  for (int w = 1; w < 4; ++w) { y0 += *(const f32x4*)(smem + w * 8192 + t * 32); y1 += *(const f32x4*)(smem + w * 8192 + t * 32 + 16); }
  const u32x4 hv = *(const u32x4*)(smem + CV_H3 + 48 + t * 16);
  u32x4 o;
#pragma unroll
  for (int k = 0; k < 4; ++k) {
    const u32 yr = k < 2 ? cvtpk(y0[2 * k], y0[2 * k + 1]) : cvtpk(y1[2 * k - 4], y1[2 * k - 3]);
    const u32 hd = cvtpk(lo2f(hv[k]) * dsk, hi2f(hv[k]) * dsk);
    const u32 v = cvtpk(lo2f(yr) + lo2f(hd), hi2f(yr) + hi2f(hd));
    const u32 gv = cvtpk(lo2f(v) * ga, hi2f(v) * ga);
    o[k] = cvtpk(lo2f(xv[k]) + lo2f(gv), hi2f(xv[k]) + hi2f(gv));
    os[k] = cvtpk(lo2f(o[k]) * c1, hi2f(o[k]) * c1);
  }
  return o;
}

template <int NW>
DI void poll_words(const u32* cnt, u32 target, int lane) {
  for (int spins = 0; spins < (1 << 20); ++spins) {
    u32 v = target;
    if (lane < NW) v = __hip_atomic_load(cnt + lane * 64, __ATOMIC_RELAXED, __HIP_MEMORY_SCOPE_AGENT);
    if (__all(v >= target)) break;
    __builtin_amdgcn_s_sleep(1);
  }
}

constexpr int W1_RING = 10;
constexpr int W2_MID = 4;

__global__ __launch_bounds__(512, 2) void k_main(const u16* __restrict__ Dsk, char* __restrict__ ws, u16* __restrict__ out) {
  __shared__ __attribute__((aligned(256))) char smem[MN_SMEM];
  const int tid = threadIdx.x, wv = __builtin_amdgcn_readfirstlane(tid >> 6), lane = tid & 63;
  const int r16 = lane & 15, g = lane >> 4, q = r16 >> 2, p = lane & 3;
  const int b = blockIdx.x, hw = wv >> 2, w4 = wv & 3, t256 = tid & 255;
  const rsrc_t rs = __builtin_amdgcn_make_buffer_rsrc((void*)ws, 0, 0x7fffffff, 0x00020000);
  u32* cnt = (u32*)(ws + WS_CNT);
  const int idx = b >> 3, tt = ((b & 7) >> 1) * 8 + (idx & 7), sl = (b & 1) * 4 + (idx >> 3);
  const int pbase = 7 * sl - (sl >> 2), pcnt = (sl & 3) == 3 ? 6 : 7;
  const bool active = wv < pcnt;
  const int p0 = pbase + (active ? wv : 0);
  TS(1, 0);

  const bool three = b < 128;
  const int d0 = three ? 3 * b : 384 + 2 * (b - 128);
  const int dA = d0 + hw, dC = three ? d0 + 2 : d0;
  bf16x8 af[W1_RING][2];
  const u32 l16 = lane * 16;
  const u32 w1o = (u32)WS_W1F + (u32)(2 * p0) * 20 * 1024;
  {
    const u16* nT = (const u16*)(ws + WS_NT);
    const u16* xT = (const u16*)(ws + WS_XT);
    const float* modp = (const float*)(ws + WS_MODP);
    const char* ktA = ws + WS_KT + (size_t)dA * KTAB;
    const u32x4 tA0 = *(const u32x4*)(ktA + lane * 16), tA1 = *(const u32x4*)(ktA + (lane < 17 ? 64 + lane : 0) * 16);
    const int mj = (lane & 31) >> 3;
    float mp = modp[(lane & 7) * (6 * D_) + (mj < 3 ? mj : 4) * D_ + (lane < 32 ? dA : dC)];
    const u32x4 nA0 = *(const u32x4*)(nT + (size_t)dA * L_ + 8 * (t256 > 0 ? t256 - 1 : 0));
    const u32x4 nA1 = *(const u32x4*)(nT + (size_t)dA * L_ + 8 * t256);
    u32x4 c0, c1;
    if (hw == 0) {
      const char* ktC = ws + WS_KT + (size_t)dC * KTAB;
      c0 = *(const u32x4*)(ktC + lane * 16); c1 = *(const u32x4*)(ktC + (lane < 17 ? 64 + lane : 0) * 16);
    } else {
      c0 = *(const u32x4*)(nT + (size_t)dC * L_ + 8 * (t256 > 0 ? t256 - 1 : 0));
      c1 = *(const u32x4*)(nT + (size_t)dC * L_ + 8 * t256);
    }
    const u32x4 xA = *(const u32x4*)(xT + (size_t)dA * L_ + 8 * t256);
    const u32x4 xC = *(const u32x4*)(xT + (size_t)dC * L_ + 8 * t256);
    const float dskA = bf2f(Dsk[dA]), dskC = bf2f(Dsk[dC]);
    const int t1 = (b - 128) * 4 + wv;
    const bool do1 = !three && wv < 4 && t1 < 432;
    u32x4 rv[5];
    f32x4 mc = {0.f, 0.f, 0.f, 0.f}, ms = mc;
    if (do1) {
#pragma unroll
      for (int i = 0; i < 5; ++i) rv[i] = __builtin_bit_cast(u32x4, ld_frag(rs, l16, (u32)WS_W1F + (t1 * 5 + i) * 1024));
      const float* mq = modp + 160 * (t1 & 3) + 4 * (lane < 40 ? lane : 0);
#pragma unroll
      for (int k = 0; k < KSM; ++k) { mc += *(const f32x4*)(mq + k * (6 * D_) + 4 * D_); ms += *(const f32x4*)(mq + k * (6 * D_) + 3 * D_); }
    }
    float gbs = 0.f;
    if (tid < 80) {
#pragma unroll
      for (int k = 0; k < KSM; ++k) gbs += modp[k * (6 * D_) + 5 * D_ + sl * 80 + tid];
    }
    char* smA = smem + hw * CV_CH;
    char* smC = smem + 2 * CV_CH;
    conv_stage_k(smA, tA0, tA1, w4, lane, t256);
    if (three && hw == 0) conv_stage_k(smC, c0, c1, w4, lane, t256);
    mp += __shfl_xor(mp, 1); mp += __shfl_xor(mp, 2); mp += __shfl_xor(mp, 4);
    mp = rb(mp);
    const float saA = __shfl(mp, 0), caA = __shfl(mp, 8), gaA = __shfl(mp, 16);
    const float saC = __shfl(mp, 32), caC = __shfl(mp, 40), gaC = __shfl(mp, 48);
    const float c1A = rb(1.f + __shfl(mp, 24)), c1C = rb(1.f + __shfl(mp, 56));
    conv_stage_h(smA, nA0, nA1, rb(1.f + caA), saA, t256);
    if (three && hw == 1) conv_stage_h(smC, c0, c1, rb(1.f + caC), saC, t256);
    if (tid < 80) ((float*)(smem + MN_GB))[tid] = rb(gbs);
    __builtin_amdgcn_sched_barrier(0);
    if (active) {
#pragma unroll
      for (int ks = 0; ks < W1_RING; ++ks)
#pragma unroll
        for (int fb = 0; fb < 2; ++fb) af[ks][fb] = ld_frag(rs, l16, w1o + (fb * 20 + ks) * 1024);
    }
    __builtin_amdgcn_sched_barrier(0);
    __syncthreads();
    TS(1, 1);
    f32x4 accA[8], accC[8];
    conv_mma(smA, w4, lane, accA);
    if (three && hw == 0) conv_mma(smC, w4, lane, accC);
    if (do1) {
      char* buf = smC + wv * 1024;
      if (lane < 40) {
        *(u32x2*)(buf + lane * 8) = u32x2{cvtpk(1.f + rb(mc[0]), 1.f + rb(mc[1])), cvtpk(1.f + rb(mc[2]), 1.f + rb(mc[3]))};
        *(u32x2*)(buf + 512 + lane * 8) = u32x2{cvtpk(ms[0], ms[1]), cvtpk(ms[2], ms[3])};
      }
      wave_sync();
      float S = 0.f, B = 0.f;
#pragma unroll
      for (int i = 0; i < 5; ++i) {
        const u32x4 c1p = *(const u32x4*)(buf + i * 64 + g * 16), sbp = *(const u32x4*)(buf + 512 + i * 64 + g * 16);
#pragma unroll
        for (int k = 0; k < 4; ++k) { S = dot2bf(rv[i][k], c1p[k], S); B = dot2bf(rv[i][k], sbp[k], B); }
      }
      S += __shfl_xor(S, 16); S += __shfl_xor(S, 32);
      B += __shfl_xor(B, 16); B += __shfl_xor(B, 32);
      if (lane < 32)
        __builtin_amdgcn_raw_buffer_store_b32(__float_as_uint(lane < 16 ? S : B), rs, (g * 4 * F_ + r16) * 4, (u32)WS_SB + ((t1 & 3) * F_ + 16 * (t1 >> 2)) * 4, 16);
    }
    __syncthreads();
    TS(1, 2);
    conv_put(smA, w4, lane, accA);
    if (three && hw == 0) conv_put(smC, w4, lane, accC);
    __syncthreads();
    u32x4 os;
    st_sc1(rs, t256 * 16, (u32)WS_X1T + dA * (L_ * 2), conv_fin(smA, t256, xA, dskA, gaA, c1A, os));
    st_sc1(rs, t256 * 16, (u32)WS_X1S + dA * (L_ * 2), os);
    if (three && hw == 1) {
      st_sc1(rs, t256 * 16, (u32)WS_X1T + dC * (L_ * 2), conv_fin(smC, t256, xC, dskC, gaC, c1C, os));
      st_sc1(rs, t256 * 16, (u32)WS_X1S + dC * (L_ * 2), os);
    }
  }
  asm volatile("s_waitcnt vmcnt(0)" ::: "memory");
  __syncthreads();
  TS(1, 3);
  if (wv == 7) {
    u32 o = 0;
    if (lane == 0) o = __hip_atomic_fetch_add(cnt + (b & 7) * 64, 1u, __ATOMIC_RELAXED, __HIP_MEMORY_SCOPE_AGENT);
    o = __builtin_amdgcn_readfirstlane(o);
    if (o == 31u && lane < 8) __hip_atomic_store(cnt + (8 + lane) * 64 + (b & 7), 1u, __ATOMIC_RELAXED, __HIP_MEMORY_SCOPE_AGENT);
    for (int spins = 0; spins < (1 << 20); ++spins) {
      u32 v = 1u;
      if (lane < 8) v = __hip_atomic_load(cnt + (8 + (b & 7)) * 64 + lane, __ATOMIC_RELAXED, __HIP_MEMORY_SCOPE_AGENT);
      if (__all(v != 0u)) break;
      __builtin_amdgcn_s_sleep(1);
    }
  }
  __syncthreads();
  TS(1, 4);

  const int k0 = 7 * wv - (wv >> 2), nk = (wv & 3) == 3 ? 6 : 7;
  const u32 w2o = (u32)WS_W2F + (u32)((sl * 5) * 54 + k0) * 1024;
  bf16x8 wf[7][5];
  {
    const int l8 = tid & 7, crow = tid >> 3;
    u32x4 xv[12];
    f32x4 sb4;
    f32x4 acc[2][4];
#pragma unroll
    for (int fb = 0; fb < 2; ++fb)
#pragma unroll
      for (int tb = 0; tb < 4; ++tb) acc[fb][tb] = f32x4{0.f, 0.f, 0.f, 0.f};
    if (wv < 7) {
#pragma unroll
      for (int qq = 0; qq < 12; ++qq)
        xv[qq] = ld_sc1(rs, (qq < 11 || crow < 24 ? crow : 0) * (L_ * 2) + l8 * 16, (u32)WS_X1S + qq * (56 * L_ * 2) + tt * 128);
      __builtin_amdgcn_sched_barrier(0);
#pragma unroll
      for (int qq = 0; qq < 12; ++qq) {
        const int c = crow + 56 * qq;
        if (qq < 11 || crow < 24) *(u32x4*)(smem + M1_A + c * 128 + (((l8 >> 1) ^ h2_swz(c)) << 5) + ((l8 & 1) << 4)) = xv[qq];
      }
    } else {
      __builtin_amdgcn_s_setprio(3);
#pragma unroll
      for (int i = 0; i < 10; ++i) af[i >> 1][i & 1] = ld_frag1(rs, (lane >> 3) * (L_ * 2) + (lane & 7) * 16, (u32)WS_X1T + (sl * 80 + 8 * i) * (L_ * 2) + tt * 128);
    }
    __syncthreads();
    TS(1, 5);
    TC(3, 0);
    const int row0 = 8 * g + q, sw = h2_swz(row0);
    const char* rb0 = smem + M1_A + row0 * 128 + 8 * p;
    float* st = (float*)(smem + M1_ST);
    auto ldb = [&](int ks, bf16x8 (&bfr)[4]) {
#pragma unroll
      for (int tb = 0; tb < 4; ++tb) {
        const s16x4 lo = tr_read(rb0 + ks * 4096 + ((tb ^ sw) << 5));
        const s16x4 hi = tr_read(rb0 + ks * 4096 + 512 + ((tb ^ sw) << 5));
        bfr[tb] = cat8(lo, hi);
      }
    };
    bf16x8 bb[2][4];
    ldb(0, bb[0]);
#pragma unroll
    for (int ks = 0; ks < 20; ++ks) {
      if (active) {
        if (ks < 19) ldb(ks + 1, bb[(ks + 1) & 1]);
#pragma unroll
        for (int fb = 0; fb < 2; ++fb)
#pragma unroll
          for (int tb = 0; tb < 4; ++tb) acc[fb][tb] = mfma16(af[ks % W1_RING][fb], bb[ks & 1][tb], acc[fb][tb]);
        if (ks + W1_RING < 20) {
#pragma unroll
          for (int fb = 0; fb < 2; ++fb) af[ks % W1_RING][fb] = ld_frag(rs, l16, w1o + (fb * 20 + ks + W1_RING) * 1024);
        }
      } else if (ks == 0 && wv == 7) {
#pragma unroll
        for (int i = 0; i < 10; ++i) {
          const u32x4 v = __builtin_bit_cast(u32x4, af[i >> 1][i & 1]);
          const f32x4 a0 = {lo2f(v[0]), hi2f(v[0]), lo2f(v[1]), hi2f(v[1])}, a1 = {lo2f(v[2]), hi2f(v[2]), lo2f(v[3]), hi2f(v[3])};
          acc[0][0] += a0; acc[0][1] += a1; acc[0][2] += a0 * a0; acc[0][3] += a1 * a1;
        }
        const bool b5 = lane & 32, b4 = lane & 16, b3 = lane & 8;
        float v4[2][4], v2[2][2], v1[2];
#pragma unroll
        for (int h = 0; h < 2; ++h) {
#pragma unroll
          for (int j = 0; j < 4; ++j) {
            const float lo = acc[0][2 * h][j], hi = acc[0][2 * h + 1][j];
            v4[h][j] = (b5 ? hi : lo) + __shfl_xor(b5 ? lo : hi, 32);
          }
#pragma unroll
          for (int j = 0; j < 2; ++j) v2[h][j] = (b4 ? v4[h][2 + j] : v4[h][j]) + __shfl_xor(b4 ? v4[h][j] : v4[h][2 + j], 16);
          v1[h] = (b3 ? v2[h][1] : v2[h][0]) + __shfl_xor(b3 ? v2[h][0] : v2[h][1], 8);
        }
        const u32 toff = (8 * (lane & 7) + (lane >> 3)) * 8;
        __builtin_amdgcn_raw_buffer_store_b64(u32x2{__float_as_uint(v1[0]), __float_as_uint(v1[1]) | 0x80000000u}, rs, toff,
                                              (u32)WS_ST + (tt * 8 + sl) * 512, 16);
#pragma unroll
        for (int i = 0; i < 10; ++i) {
          const u32x4 v = __builtin_bit_cast(u32x4, af[i >> 1][i & 1]);
          char* dp = smem + M2_X + 8 * (lane & 7) * M2_XS + 2 * (8 * i + (lane >> 3));
#pragma unroll
          for (int k = 0; k < 4; ++k) {
            *(u16*)(dp + (2 * k) * M2_XS) = (u16)(v[k] & 0xffffu);
            *(u16*)(dp + (2 * k + 1) * M2_XS) = (u16)(v[k] >> 16);
          }
        }
        u32x2 gv[8];
        for (int spins = 0; spins < (1 << 20); ++spins) {
          u32 ok = 0x80000000u;
#pragma unroll
          for (int j = 0; j < 8; ++j) { gv[j] = __builtin_amdgcn_raw_buffer_load_b64(rs, lane * 8, (u32)WS_ST + (tt * 8 + j) * 512, 16); ok &= gv[j][1]; }
          if (__all(ok != 0u)) break;
          __builtin_amdgcn_s_sleep(2);
        }
        float sm = 0.f, sq = 0.f;
#pragma unroll
        for (int j = 0; j < 8; ++j) { sm += __uint_as_float(gv[j][0]); sq += __uint_as_float(gv[j][1] & 0x7fffffffu); }
        const float mean = sm * (1.f / D_);
        const float var = fmaxf(sq * (1.f / D_) - mean * mean, 0.f);
        st[lane] = rb(mean);
        st[64 + lane] = 1.f / rb(sqrtf(rb(rb(var) + 1e-5f)));
        __builtin_amdgcn_s_setprio(0);
      }
      if (ks >= 10) {
#pragma unroll
        for (int j = 0; j < 2; ++j) {
          const int f = 2 * (ks - 10) + j;
          wf[f / 5][f % 5] = ld_frag(rs, l16, w2o + ((f % 5) * 54 + f / 5) * 1024);
        }
      }
      if (ks == 9) {
        sb4 = __builtin_bit_cast(f32x4, ld_sc1(rs, ((lane >> 3) * F_ + 4 * (lane & 7)) * 4, (u32)WS_SB + 32 * p0 * 4));
      }
      if (ks == 17) {
#pragma unroll
        for (int e = 0; e < 4; ++e) { sb4[e] += __shfl_xor(sb4[e], 8); sb4[e] += __shfl_xor(sb4[e], 16); }
        if ((lane & 24) == 0) *(f32x4*)(smem + MN_SB + wv * 256 + (lane >> 5) * 128 + (lane & 7) * 16) = sb4;
      }
      __builtin_amdgcn_sched_barrier(0);
    }
    TS(2, 1);
    TC(3, 1);
    __syncthreads();
    TS(2, 2);
    if (active) {
      const float* sbp = (const float*)(smem + MN_SB) + wv * 64 + 4 * g;
      const u32 hoff = (u32)WS_HID + (u32)((tt * 4) * 54 + p0) * 1024;
#pragma unroll
      for (int tb = 0; tb < 4; ++tb) {
        const float mb = st[tb * 16 + r16], rsd = st[64 + tb * 16 + r16];
        u32x4 v;
#pragma unroll
        for (int hh = 0; hh < 2; ++hh) {
          const f32x4 sv = *(const f32x4*)(sbp + 16 * hh), bv = *(const f32x4*)(sbp + 32 + 16 * hh);
          const f32x4 a = (acc[hh][tb] - mb * sv) * rsd + bv;
          v[2 * hh] = cvtpk(fmaxf(a[0], 0.f), fmaxf(a[1], 0.f));
          v[2 * hh + 1] = cvtpk(fmaxf(a[2], 0.f), fmaxf(a[3], 0.f));
        }
        st_sc1(rs, l16, hoff + tb * 54 * 1024, v);
      }
    }
  }
  TS(2, 3);
  asm volatile("s_waitcnt vmcnt(0)" ::: "memory");
  __syncthreads();
  TS(2, 4);
  if (tid == 448) __hip_atomic_store(cnt + (16 + tt * 8 + sl) * 64, 1u, __ATOMIC_RELAXED, __HIP_MEMORY_SCOPE_AGENT);
  __builtin_amdgcn_sched_barrier(0);
#pragma unroll
  for (int i = W2_MID; i < 7; ++i)
#pragma unroll
    for (int cb = 0; cb < 5; ++cb) wf[i][cb] = ld_frag(rs, l16, w2o + (cb * 54 + (i < 6 || nk == 7 ? i : 5)) * 1024);
  __builtin_amdgcn_sched_barrier(0);
  poll_words<1>(cnt + (16 + tt * 8 + wv) * 64, 1u, lane);
  TS(1, 6);

  {
    const u32 hoff = (u32)WS_HID + (u32)((tt * 4) * 54 + k0) * 1024;
    u32x4 hf[2][4];
#pragma unroll
    for (int i = 0; i < 2; ++i)
#pragma unroll
      for (int tb = 0; tb < 4; ++tb) hf[i][tb] = ld_sc1(rs, l16, hoff + (tb * 54 + i) * 1024);
    f32x4 acc[5][4];
#pragma unroll
    for (int cb = 0; cb < 5; ++cb)
#pragma unroll
      for (int tb = 0; tb < 4; ++tb) acc[cb][tb] = f32x4{0.f, 0.f, 0.f, 0.f};
#pragma unroll
    for (int i = 0; i < 7; ++i) {
      if (i < 6 || nk == 7) {
#pragma unroll
        for (int tb = 0; tb < 4; ++tb)
#pragma unroll
          for (int cb = 0; cb < 5; ++cb) acc[cb][tb] = mfma16(wf[i][cb], __builtin_bit_cast(bf16x8, hf[i & 1][tb]), acc[cb][tb]);
      }
      __builtin_amdgcn_sched_barrier(0);
      if (i + 2 < 7) {
        const int kk = (i + 2 < 6 || nk == 7) ? i + 2 : 5;
#pragma unroll
        for (int tb = 0; tb < 4; ++tb) hf[i & 1][tb] = ld_sc1(rs, l16, hoff + (tb * 54 + kk) * 1024);
        __builtin_amdgcn_sched_barrier(0);
      }
    }
    TS(2, 5);
#pragma unroll
    for (int tb = 0; tb < 4; ++tb)
#pragma unroll
      for (int cb = 0; cb < 5; ++cb) {
        const int owner = tb + (cb < 3 ? 0 : 4);
        if (owner != wv) {
          const int slot = wv < owner ? wv : wv - 1;
          *(f32x4*)(smem + M2_RED + (((tb * 5 + cb) * 7 + slot) * 64 + lane) * 16) = acc[cb][tb];
        }
      }
    __syncthreads();
    TS(2, 6);
    const int tok = 16 * w4 + r16;
#pragma unroll
    for (int j = 0; j < 3; ++j) {
      if (j < 2 || wv < 4) {
        const int cb = wv < 4 ? j : 3 + j;
        f32x4 y;
#pragma unroll
        for (int tb = 0; tb < 4; ++tb)
#pragma unroll
          for (int c2 = 0; c2 < 5; ++c2) if (tb == w4 && c2 == cb) y = acc[c2][tb];
#pragma unroll
        for (int s7 = 0; s7 < 7; ++s7) y += *(const f32x4*)(smem + M2_RED + (((w4 * 5 + cb) * 7 + s7) * 64 + lane) * 16);
        const f32x4 gb4 = *(const f32x4*)(smem + MN_GB + (16 * cb + 4 * g) * 4);
        const float g0 = gb4[0], g1 = gb4[1], g2 = gb4[2], g3 = gb4[3];
        const u32x2 xr = *(const u32x2*)(smem + M2_X + tok * M2_XS + 2 * (16 * cb + 4 * g));
        const u32 y0 = cvtpk(y[0], y[1]), y1 = cvtpk(y[2], y[3]);
        const u32 v0 = cvtpk(lo2f(y0) * g0, hi2f(y0) * g1), v1 = cvtpk(lo2f(y1) * g2, hi2f(y1) * g3);
        u32x2 o;
        o[0] = cvtpk(lo2f(xr[0]) + lo2f(v0), hi2f(xr[0]) + hi2f(v0));
        o[1] = cvtpk(lo2f(xr[1]) + lo2f(v1), hi2f(xr[1]) + hi2f(v1));
        *(u32x2*)(out + (size_t)(tt * 64 + tok) * D_ + sl * 80 + 16 * cb + 4 * g) = o;
      }
    }
  }
  TS(1, 7);
}

extern "C" void kernel_launch(void* const* d_in, const int* in_sizes, int n_in, void* d_out, int out_size, void* d_ws,
                              size_t ws_size, hipStream_t stream) {
  char* ws = (char*)d_ws;
  k_pre<<<NB_LN + NB_KG + NB_MOD + NB_RL, 256, 0, stream>>>(
      (const u16*)d_in[0], (const u16*)d_in[1], (const u16*)d_in[2], (const u16*)d_in[3], (const u16*)d_in[4],
      (const u16*)d_in[5], (const u16*)d_in[6], (const u16*)d_in[7], (const u16*)d_in[10], (const u16*)d_in[11],
      (const u16*)d_in[12], (const u16*)d_in[13], ws);
  k_main<<<256, 512, 0, stream>>>((const u16*)d_in[8], ws, (u16*)d_out);
}
```

```cpp
#include <hip/hip_runtime.h>
#include <hip/hip_bf16.h>
#include <stdint.h>

typedef unsigned short u16;
typedef unsigned int u32;
typedef __attribute__((ext_vector_type(8))) short bf16x8;
typedef __attribute__((ext_vector_type(4))) short s16x4;
typedef __attribute__((ext_vector_type(4))) float f32x4;
typedef __attribute__((ext_vector_type(2))) float f32x2;
typedef __attribute__((ext_vector_type(4))) unsigned int u32x4;
typedef __attribute__((ext_vector_type(2))) unsigned int u32x2;
typedef __attribute__((ext_vector_type(3))) unsigned int u32x3;
typedef __bf16 bf16x2_t __attribute__((ext_vector_type(2)));
#define LDS_AS __attribute__((address_space(3)))
#define DI __device__ __forceinline__

constexpr int L_ = 2048, D_ = 640, N_ = 128, F_ = 1728;
constexpr int KSM = 4;

constexpr size_t WS_MODP = 0;
constexpr size_t WS_CNT = 126976;
constexpr size_t WS_NT = 262144;
constexpr size_t WS_XT = WS_NT + (size_t)D_ * L_ * 2;
constexpr size_t WS_X1T = WS_XT + (size_t)D_ * L_ * 2;
constexpr size_t WS_X1S = WS_X1T + (size_t)D_ * L_ * 2;
constexpr int KTAB = 1296;
constexpr size_t WS_KT = WS_X1S + (size_t)D_ * L_ * 2;
constexpr size_t WS_SB = WS_KT + (size_t)D_ * KTAB;
constexpr size_t WS_W1F = WS_SB + 2 * 20 * 1728 * 4;
constexpr size_t WS_W2F = WS_W1F + (size_t)108 * 20 * 1024;
constexpr size_t WS_HID = WS_W2F + (size_t)40 * 54 * 1024;
constexpr size_t WS_ST = WS_HID + (size_t)128 * 54 * 1024;

#define TS(kern, slot) do {} while (0)
#define TC(kern, slot) do {} while (0)

DI void st_wt(void* p, u32x4 v) { asm volatile("global_store_dwordx4 %0, %1, off sc1\n\ts_nop 1" ::"v"(p), "v"(v) : "memory"); }
#define ST_PRE(p, v) (*(p) = (v))
#define ST_PREW(p, v) (*(p) = (v))
DI u32 cvtpk(float lo, float hi) {
  f32x2 v = {lo, hi};
  bf16x2_t r = __builtin_convertvector(v, bf16x2_t);
  return __builtin_bit_cast(u32, r);
}
typedef _Float16 f16x2_t __attribute__((ext_vector_type(2)));
typedef _Float16 f16x8_t __attribute__((ext_vector_type(8)));
DI u32 cvtph(float lo, float hi) {
  f32x2 v = {lo, hi};
  f16x2_t r = __builtin_convertvector(v, f16x2_t);
  return __builtin_bit_cast(u32, r);
}
DI float dot2bf(u32 a, u32 b, float c) {
  return __builtin_amdgcn_fdot2_f32_bf16(__builtin_bit_cast(bf16x2_t, a), __builtin_bit_cast(bf16x2_t, b), c, false);
}
DI float lo2f(u32 v) { return __uint_as_float(v << 16); }
DI float hi2f(u32 v) { return __uint_as_float(v & 0xffff0000u); }
DI float bf2f(u16 v) { return __uint_as_float(((u32)v) << 16); }
DI float rb(float x) { return lo2f(cvtpk(x, x)); }
DI f32x4 mfma16(bf16x8 a, bf16x8 b, f32x4 c) { return __builtin_amdgcn_mfma_f32_16x16x32_bf16(a, b, c, 0, 0, 0); }
DI s16x4 tr_read(const char* p) {
  return __builtin_amdgcn_ds_read_tr16_b64_v4i16((LDS_AS s16x4*)p);
}
DI bf16x8 cat8(s16x4 a, s16x4 b) { return __builtin_shufflevector(a, b, 0, 1, 2, 3, 4, 5, 6, 7); }
DI void wave_sync() {
  __builtin_amdgcn_fence(__ATOMIC_RELEASE, "wavefront");
  __builtin_amdgcn_wave_barrier();
  __builtin_amdgcn_fence(__ATOMIC_ACQUIRE, "wavefront");
}

struct c32 { float r, i; };
DI c32 cmul(c32 a, c32 b) { return {a.r * b.r - a.i * b.i, a.r * b.i + a.i * b.r}; }
DI c32 cexp_scaled(float ar, float ai, float e) {
  const float mag = __expf(ar * e);
  const float x = ai * e;
  const float C_HI = 0.15915494f, C_LO = 6.4206382e-9f;
  const float r = x * C_HI;
  const float rl = fmaf(x, C_HI, -r) + x * C_LO;
  const float fr = (r - floorf(r)) + rl;
  return {mag * __builtin_amdgcn_cosf(fr), mag * __builtin_amdgcn_sinf(fr)};
}
DI int tap_index(int m) {
  u32 bits = __float_as_uint((float)m);
  bits += 0x7fffu + ((bits >> 16) & 1u);
  const int id2 = (int)(bits >> 16) - (0x4300 - 128);
  return m < 128 ? m : id2;
}

constexpr int NB_LN = 64, NB_KG = 320, NB_MOD = 30 * KSM, NB_RL = 138;
constexpr int PRE_SMEM = 43008;

DI void role_ln1(int b, const u16* __restrict__ x, u32* __restrict__ st1, u16* __restrict__ xT, char* smem) {
  const int tid = threadIdx.x, u = tid >> 4, j = tid & 15;
  const int t0 = b * 32;
  const u32x4* xa = (const u32x4*)(x + (size_t)(t0 + 2 * u) * D_);
  u32x4 A[5], B[5];
#pragma unroll
  for (int q = 0; q < 5; ++q) { A[q] = xa[j + 16 * q]; B[q] = xa[80 + j + 16 * q]; }
  float sa = 0.f, sb = 0.f, qa = 0.f, qb = 0.f;
#pragma unroll
  for (int q = 0; q < 5; ++q)
#pragma unroll
    for (int k = 0; k < 4; ++k) {
      sa = dot2bf(A[q][k], 0x3f803f80u, sa); qa = dot2bf(A[q][k], A[q][k], qa);
      sb = dot2bf(B[q][k], 0x3f803f80u, sb); qb = dot2bf(B[q][k], B[q][k], qb);
    }
#pragma unroll
  for (int o = 1; o < 16; o <<= 1) {
    sa += __shfl_xor(sa, o, 16); sb += __shfl_xor(sb, o, 16); qa += __shfl_xor(qa, o, 16); qb += __shfl_xor(qb, o, 16);
  }
  const float ma = sa * (1.f / D_), mb = sb * (1.f / D_);
  const float va = fmaxf(qa - sa * ma, 0.f), vb = fmaxf(qb - sb * mb, 0.f);
  if (j == 0) {
    const u32 s0 = cvtpk(ma, sqrtf(rb(rb(va * (1.f / D_)) + 1e-5f))), s1 = cvtpk(mb, sqrtf(rb(rb(vb * (1.f / D_)) + 1e-5f)));
    *(u32x2*)(st1 + t0 + 2 * u) = u32x2{s0, s1};
  }
  char* wbase = smem + (8 * j + (j >> 2)) * 64 + ((((u >> 2) ^ (j & 3)) << 2) + (u & 3)) * 4;
#pragma unroll
  for (int q = 0; q < 5; ++q)
#pragma unroll
    for (int k = 0; k < 4; ++k) {
      *(u32*)(wbase + (132 * q + 2 * k) * 64) = (A[q][k] & 0xffffu) | (B[q][k] << 16);
      *(u32*)(wbase + (132 * q + 2 * k + 1) * 64) = (A[q][k] >> 16) | (B[q][k] & 0xffff0000u);
    }
  __syncthreads();
#pragma unroll
  for (int i = 0; i < 10; ++i) {
    const int id = tid + 256 * i, c = id >> 2, o = id & 3;
    const u32x4 v = *(const u32x4*)(smem + (c + (c >> 5)) * 64 + ((o ^ ((c >> 3) & 3)) << 4));
    ST_PRE((u32x4*)(xT + (size_t)c * L_ + t0 + 8 * o), v);
  }
}

DI void role_kgen(int bk, const u16* __restrict__ Are, const u16* __restrict__ Aim, const u16* __restrict__ Bre,
                  const u16* __restrict__ Bim, const u16* __restrict__ Cre, const u16* __restrict__ Cim,
                  const u16* __restrict__ ldt, char* __restrict__ KT, char* smem) {
  const int tid = threadIdx.x, w = tid >> 6, lane = tid & 63, r16 = lane & 15, g = lane >> 4;
  const int d = 2 * bk + (w >> 1), n = (w & 1) * 64 + lane;
  const int gi = d * N_ + n;
  const float dt = rb(expf(bf2f(ldt[d])));
  const float ar = dt * bf2f(Are[gi]), ai = dt * bf2f(Aim[gi]);
  const c32 dB = {dt * bf2f(Bre[gi]), dt * bf2f(Bim[gi])};
  const c32 Cc = {bf2f(Cre[gi]), bf2f(Cim[gi])};
  const c32 W = cmul(Cc, dB);
  const c32 E1 = cexp_scaled(ar, ai, 1.f), E8 = cexp_scaled(ar, ai, 8.f), E128 = cexp_scaled(ar, ai, 128.f);

  float wmax = fmaxf(fabsf(W.r), fabsf(W.i));
#pragma unroll
  for (int o = 1; o < 64; o <<= 1) wmax = fmaxf(wmax, __shfl_xor(wmax, o));
  int ew = (int)((__float_as_uint(wmax) >> 23) & 0xff);
  ew = ew < 20 ? 20 : (ew > 240 ? 240 : ew);
  const float wsc = __uint_as_float((u32)(127 + 10 + 127 - ew - 1) << 23);
  const float unsc = __uint_as_float((u32)(ew + 1 - 20) << 23);

  char* buf = smem + w * 8704;
  char* wr = buf + lane * 4;
  const char* rd = buf + r16 * 272 + g * 16;
  auto put = [&](int row, float re, float im) { *(u32*)(wr + row * 272) = cvtph(re, im); };
  {
    c32 p = {1024.f, 0.f};
#pragma unroll
    for (int jj = 0; jj < 16; ++jj) { put(jj, p.r, p.i); if (jj < 15) p = cmul(p, E8); }
  }
  wave_sync();
  f16x8_t bh[4];
#pragma unroll
  for (int ks = 0; ks < 4; ++ks) bh[ks] = *(const f16x8_t*)(rd + ks * 64);
  wave_sync();
  c32 sm[8];
  sm[0] = {1.f, 0.f}; sm[1] = E1;
#pragma unroll
  for (int i = 2; i < 8; ++i) sm[i] = cmul(sm[i - 1], E1);
  c32 WG[17];
  WG[0] = {W.r * wsc, W.i * wsc};
#pragma unroll
  for (int i = 1; i < 17; ++i) WG[i] = cmul(WG[i - 1], E128);
  f32x4 acc[3];
  auto tile_mma = [&](int t) {
    wave_sync();
    f32x4 a = {0.f, 0.f, 0.f, 0.f};
#pragma unroll
    for (int ks = 0; ks < 4; ++ks) a = __builtin_amdgcn_mfma_f32_16x16x32_f16(*(const f16x8_t*)(rd + ks * 64), bh[ks], a, 0, 0, 0);
    acc[t] = a * unsc;
    wave_sync();
  };
#pragma unroll
  for (int i = 0; i < 2; ++i)
#pragma unroll
    for (int rho = 0; rho < 8; ++rho) { const c32 v = rho ? cmul(WG[i], sm[rho]) : WG[i]; put(8 * i + rho, v.r, -v.i); }
  tile_mma(0);
#pragma unroll
  for (int i = 0; i < 2; ++i)
#pragma unroll
    for (int rho = 0; rho < 4; ++rho) { const c32 v = rho ? cmul(WG[2 + i], sm[2 * rho]) : WG[2 + i]; put(4 * i + rho, v.r, -v.i); }
#pragma unroll
  for (int i = 0; i < 4; ++i)
#pragma unroll
    for (int rho = 0; rho < 2; ++rho) { const c32 v = rho ? cmul(WG[4 + i], sm[4]) : WG[4 + i]; put(8 + 2 * i + rho, v.r, -v.i); }
  tile_mma(1);
#pragma unroll
  for (int i = 0; i < 9; ++i) put(i, WG[8 + i].r, -WG[8 + i].i);
  tile_mma(2);

#pragma unroll
  for (int t = 0; t < 3; ++t) *(f32x4*)(buf + t * 1024 + lane * 16) = acc[t];
  __syncthreads();
  u16* T = (u16*)(smem + (w >> 1) * 2 * 8704 + 4352);
  {
    const char* ob = smem + (w ^ 1) * 8704 + lane * 16;
    if (!(w & 1)) {
      const f32x4 a0 = acc[0] + *(const f32x4*)(ob), a2 = acc[2] + *(const f32x4*)(ob + 2048);
      u16* t0 = T + 128 * (g >> 1) + 4 * (g & 1) + 8 * r16;
      *(u32x2*)t0 = u32x2{cvtpk(a0[0], a0[1]), cvtpk(a0[2], a0[3])};
      if (g < 2) {
#pragma unroll
        for (int rg = 0; rg < 4; ++rg) T[512 + 16 * (4 * g + rg) + r16] = (u16)cvtpk(a2[rg], 0.f);
      } else if (lane == 32) {
        T[640] = (u16)cvtpk(a2[0], 0.f);
      } else if (lane >= 48 && lane < 55) {
        T[641 + lane - 48] = 0;
      }
    } else {
      const f32x4 a1 = acc[1] + *(const f32x4*)(ob + 1024);
      if (g < 2) {
        *(u32x2*)(T + 256 + 64 * g + 4 * r16) = u32x2{cvtpk(a1[0], a1[1]), cvtpk(a1[2], a1[3])};
      } else {
        *(u32*)(T + 384 + 64 * (g - 2) + 2 * r16) = cvtpk(a1[0], a1[1]);
        *(u32*)(T + 384 + 64 * (g - 2) + 32 + 2 * r16) = cvtpk(a1[2], a1[3]);
      }
    }
  }
  __syncthreads();
  const int t128 = tid & 127;
  if (t128 < 81) *(u32x4*)(KT + (size_t)d * KTAB + t128 * 16) = *(const u32x4*)((const char*)T + t128 * 16);
}

DI void role_mod(int bm, const u16* __restrict__ c, const u16* __restrict__ wm, float* __restrict__ modp, char* smem) {
  const int tid = threadIdx.x, cj = tid & 15, rg = tid >> 4, w = tid >> 6;
  const int cbk = bm % 30, ks = bm / 30;
  constexpr int NR = 640 / KSM / 16;
  u32x4 v[NR];
  u16 cr[NR];
#pragma unroll
  for (int i = 0; i < NR; ++i) {
    const int row = (640 / KSM) * ks + rg + 16 * i;
    v[i] = *(const u32x4*)(wm + (size_t)row * (6 * D_) + 128 * cbk + 8 * cj);
    cr[i] = c[row];
  }
  float a[8];
#pragma unroll
  for (int k = 0; k < 8; ++k) a[k] = 0.f;
#pragma unroll
  for (int i = 0; i < NR; ++i) {
    const float cv = bf2f(cr[i]);
    const float sv = rb(cv / (1.f + __expf(-cv)));
#pragma unroll
    for (int k = 0; k < 4; ++k) { a[2 * k] += sv * lo2f(v[i][k]); a[2 * k + 1] += sv * hi2f(v[i][k]); }
  }
#pragma unroll
  for (int k = 0; k < 8; ++k) { a[k] += __shfl_xor(a[k], 16); a[k] += __shfl_xor(a[k], 32); }
  float* red = (float*)smem;
  if ((tid & 63) < 16) {
#pragma unroll
    for (int k = 0; k < 8; ++k) red[w * 128 + 8 * cj + k] = a[k];
  }
  __syncthreads();
  if (tid < 128) modp[ks * (6 * D_) + 128 * cbk + tid] = red[tid] + red[128 + tid] + red[256 + tid] + red[384 + tid];
}

DI int rl_swz(int row) { return (row & 3) | ((((row >> 2) ^ (row >> 3)) & 1) << 2); }

DI void role_relayout(int br, const u16* __restrict__ w1, const u16* __restrict__ w2, char* __restrict__ ws, char* smem) {
  const int tid = threadIdx.x, w = tid >> 6, lane = tid & 63;
  const int tile = br * 4 + w;
  if (tile >= 550) return;
  const bool is1 = tile < 280;
  int ks, nb, ld, nblk;
  const u16* src;
  if (is1) { ks = tile / 14; nb = tile % 14; ld = F_; src = w1; nblk = nb == 13 ? 4 : 8; }
  else { const int t2 = tile - 280; ks = t2 / 5; nb = t2 % 5; ld = D_; src = w2; nblk = 8; }
  char* buf = smem + w * 8192;
  const int lr = lane >> 4, lc = lane & 15;
  u32x4 v[8];
#pragma unroll
  for (int i = 0; i < 8; ++i) v[i] = *(const u32x4*)(src + (size_t)(32 * ks + 4 * i + lr) * ld + 128 * nb + 8 * (lc < 2 * nblk ? lc : 0));
#pragma unroll
  for (int i = 0; i < 8; ++i) {
    const int row = 4 * i + lr;
    *(u32x4*)(buf + row * 256 + (((lc >> 1) ^ rl_swz(row)) << 5) + ((lc & 1) << 4)) = v[i];
  }
  wave_sync();
  const int g = lane >> 4, q = (lane & 15) >> 2, p = lane & 3;
  const int row0 = is1 ? 8 * g + q : 4 * g + q, row1 = is1 ? row0 + 4 : row0 + 16;
  char* dst = is1 ? ws + WS_W1F + ((size_t)(8 * nb) * 20 + ks) * 1024 : ws + WS_W2F + ((size_t)(8 * nb) * 54 + ks) * 1024;
  const int dstride = is1 ? 20 * 1024 : 54 * 1024;
#pragma unroll
  for (int cb = 0; cb < 8; ++cb) {
    const s16x4 lo = tr_read(buf + row0 * 256 + ((cb ^ rl_swz(row0)) << 5) + 8 * p);
    const s16x4 hi = tr_read(buf + row1 * 256 + ((cb ^ rl_swz(row1)) << 5) + 8 * p);
    if (cb < nblk) ST_PREW((u32x4*)(dst + (size_t)cb * dstride + lane * 16), __builtin_bit_cast(u32x4, cat8(lo, hi)));
  }
}

__global__ __launch_bounds__(256) void k_pre(const u16* __restrict__ x, const u16* __restrict__ Are, const u16* __restrict__ Aim,
                                             const u16* __restrict__ Bre, const u16* __restrict__ Bim, const u16* __restrict__ Cre,
                                             const u16* __restrict__ Cim, const u16* __restrict__ ldt, const u16* __restrict__ w1,
                                             const u16* __restrict__ w2, const u16* __restrict__ c, const u16* __restrict__ wm,
                                             char* __restrict__ ws) {
  __shared__ __attribute__((aligned(16))) char smem[PRE_SMEM];
  int b = blockIdx.x;
  TS(0, 0);
  if (b < NB_LN) {
    if (b < 34 && threadIdx.x < 64) ((u32*)(ws + WS_CNT))[(b * 8 + (threadIdx.x >> 3)) * 64 + (threadIdx.x & 7)] = 0u;
    *(u32x2*)(ws + WS_ST + (b * 256 + threadIdx.x) * 8) = u32x2{0u, 0u};
    role_ln1(b, x, (u32*)(ws + WS_NT), (u16*)(ws + WS_XT), smem); TS(0, 7); return;
  }
  b -= NB_LN;
  if (b < NB_KG) { role_kgen(b, Are, Aim, Bre, Bim, Cre, Cim, ldt, ws + WS_KT, smem); TS(0, 7); return; }
  b -= NB_KG;
  if (b < NB_MOD) { role_mod(b, c, wm, (float*)(ws + WS_MODP), smem); TS(0, 7); return; }
  b -= NB_MOD;
  role_relayout(b, w1, w2, ws, smem);
  TS(0, 7);
}

constexpr int CV_H0 = 0, CV_HS = 4160, CV_K = 12544, CV_T = 21760, CV_H3 = 32768 + 192, CV_CH = 37376;
static_assert(CV_CH % 256 == 0 && CV_H3 + 4160 <= CV_CH && CV_T + 4 * KTAB <= 32768, "conv lds");
constexpr int M1_A = 0, M1_ST = 81920;
constexpr int MN_SB = 3 * CV_CH;
constexpr int MN_R1 = MN_SB + 2048;
constexpr int M2_RED = 0, M2_X = 143360, M2_XS = 176;
constexpr int MN_GB = M2_X + 64 * M2_XS;
constexpr int MN_SMEM = MN_GB + 320;
static_assert(MN_R1 + 5 * 4864 <= M2_X && M1_ST + 512 <= MN_SB, "mlp lds");

typedef __amdgpu_buffer_rsrc_t rsrc_t;
DI u32x4 ld_sc1(rsrc_t r, u32 voff, u32 soff) { return __builtin_amdgcn_raw_buffer_load_b128(r, voff, soff, 16); }
DI void st_sc1(rsrc_t r, u32 voff, u32 soff, u32x4 v) { __builtin_amdgcn_raw_buffer_store_b128(v, r, voff + soff, 0, 16); }
DI bf16x8 ld_frag1(rsrc_t r, u32 voff, u32 soff) { return __builtin_bit_cast(bf16x8, __builtin_amdgcn_raw_buffer_load_b128(r, voff, soff, 16)); }
DI bf16x8 ld_frag(rsrc_t r, u32 voff, u32 soff) { return __builtin_bit_cast(bf16x8, __builtin_amdgcn_raw_buffer_load_b128(r, voff, soff, 0)); }

DI int h2_swz(int k) { return ((k >> 1) & 1) | (((k >> 3) & 1) << 1); }

DI void conv_stage_k(char* smem, u32x4 tv0, u32x4 tv1, int w, int lane, int tid) {
  char* tb = smem + CV_T + w * KTAB;
  *(u32x4*)(tb + lane * 16) = tv0;
  if (lane < 17) *(u32x4*)(tb + (64 + lane) * 16) = tv1;
  wave_sync();
  const u16* T = (const u16*)tb;
#pragma unroll
  for (int i = 0; i < 2; ++i) {
    const int cp = tid + 256 * i;
    if (cp < 284) {
      u32 e[9];
#pragma unroll
      for (int j = 0; j < 9; ++j) {
        const int m = 2047 - 8 * cp - j;
        e[j] = T[m < 0 ? 641 : tap_index(m)];
      }
      *(u32x4*)(smem + CV_K + 4576 + cp * 16) = u32x4{e[0] | (e[1] << 16), e[2] | (e[3] << 16), e[4] | (e[5] << 16), e[6] | (e[7] << 16)};
      *(u32x4*)(smem + CV_K + cp * 16) = u32x4{e[1] | (e[2] << 16), e[3] | (e[4] << 16), e[5] | (e[6] << 16), e[7] | (e[8] << 16)};
    }
  }
}

DI void conv_stage_h(char* smem, u32x4 xq0, u32x4 xq1, const float (&mn)[16], const float (&rs)[16], float ca1, float sa, int tid) {
  u32 h[8];
#pragma unroll
  for (int k = 1; k < 8; ++k) {
    const u32 xv = k < 4 ? xq0[k] : xq1[k - 4];
    const u32 c = cvtpk(lo2f(xv) - mn[2 * k], hi2f(xv) - mn[2 * k + 1]);
    const u32 n = cvtpk(lo2f(c) * rs[2 * k], hi2f(c) * rs[2 * k + 1]);
    const u32 p = cvtpk(lo2f(n) * ca1, hi2f(n) * ca1);
    const u32 hv = cvtpk(lo2f(p) + sa, hi2f(p) + sa);
    h[k] = (k >= 4 || tid > 0) ? hv : 0u;
  }
  const int yo = (tid + 3) * 16;
  *(u32x4*)(smem + CV_H0 + yo) = u32x4{h[1], h[2], h[3], h[4]};
  *(u32x4*)(smem + CV_H0 + CV_HS + yo) = u32x4{h[2], h[3], h[4], h[5]};
  *(u32x4*)(smem + CV_H0 + 2 * CV_HS + yo) = u32x4{h[3], h[4], h[5], h[6]};
  *(u32x4*)(smem + CV_H3 + yo) = u32x4{h[4], h[5], h[6], h[7]};
  if (tid < 12) {
    const int cc = tid / 3, yc = tid % 3;
    *(u32x4*)(smem + (cc == 3 ? CV_H3 : CV_H0 + cc * CV_HS) + yc * 16) = u32x4{0u, 0u, 0u, 0u};
  }
}

template <class CB>
DI void conv_mma(const char* smem, int w, int lane, f32x4 (&acc)[8], CB&& side) {
  const int r16 = lane & 15, g = lane >> 4, cpy = r16 & 3;
  const char* hb = smem + (cpy == 3 ? CV_H3 : CV_H0 + cpy * CV_HS) + 16 * ((r16 >> 2) + 4 * w + g);
  const char* kb = smem + CV_K + (r16 & 1) * 4576 + 16 * (252 + 4 * w - 4 * (r16 >> 1) + g) - 256 * 14;
#pragma unroll
  for (int i = 0; i < 8; ++i) acc[i] = f32x4{0.f, 0.f, 0.f, 0.f};
#pragma unroll
  for (int par = 1; par >= 0; --par) {
    bf16x8 Bf[8];
#pragma unroll
    for (int t = 0; t < 8; ++t) Bf[t] = *(const bf16x8*)(hb + 256 * (2 * t + par));
#pragma unroll
    for (int sg = -par; sg <= 14; sg += 2) {
      const bf16x8 Af = *(const bf16x8*)(kb + 256 * (14 - sg));
#pragma unroll
      for (int i = 0; i < 8; ++i) {
        const int tau = 2 * i - sg;
        if (tau >= 0 && tau <= 15) acc[i] = mfma16(Af, Bf[tau >> 1], acc[i]);
      }
      side((1 - par) * 8 + ((sg + par) >> 1));
    }
  }
}

DI void conv_put(char* smem, int w, int lane, const f32x4 (&acc)[8]) {
  char* pb = smem + w * 8192 + (64 * (lane >> 4) + 2 * (lane & 15)) * 4;
#pragma unroll
  for (int i = 0; i < 8; ++i) {
    *(f32x2*)(pb + i * 1024) = f32x2{acc[i][0], acc[i][1]};
    *(f32x2*)(pb + i * 1024 + 128) = f32x2{acc[i][2], acc[i][3]};
  }
}

DI u32x4 conv_fin(const char* smem, int t, u32x4 xv, float dsk, float ga, float c1, u32x4& os) {
  f32x4 y0 = *(const f32x4*)(smem + t * 32), y1 = *(const f32x4*)(smem + t * 32 + 16);
#pragma unroll
  for (int w = 1; w < 4; ++w) { y0 += *(const f32x4*)(smem + w * 8192 + t * 32); y1 += *(const f32x4*)(smem + w * 8192 + t * 32 + 16); }
  const u32x4 hv = *(const u32x4*)(smem + CV_H3 + 48 + t * 16);
  u32x4 o;
#pragma unroll
  for (int k = 0; k < 4; ++k) {
    const u32 yr = k < 2 ? cvtpk(y0[2 * k], y0[2 * k + 1]) : cvtpk(y1[2 * k - 4], y1[2 * k - 3]);
    const u32 hd = cvtpk(lo2f(hv[k]) * dsk, hi2f(hv[k]) * dsk);
    const u32 v = cvtpk(lo2f(yr) + lo2f(hd), hi2f(yr) + hi2f(hd));
    const u32 gv = cvtpk(lo2f(v) * ga, hi2f(v) * ga);
    o[k] = cvtpk(lo2f(xv[k]) + lo2f(gv), hi2f(xv[k]) + hi2f(gv));
    os[k] = cvtpk(lo2f(o[k]) * c1, hi2f(o[k]) * c1);
  }
  return o;
}

template <int NW>
DI void poll_words(const u32* cnt, u32 target, int lane) {
  for (int spins = 0; spins < (1 << 20); ++spins) {
    u32 v = target;
    if (lane < NW) v = __hip_atomic_load(cnt + lane * 64, __ATOMIC_RELAXED, __HIP_MEMORY_SCOPE_AGENT);
    if (__all(v >= target)) break;
    __builtin_amdgcn_s_sleep(1);
  }
}

constexpr int W1_RING = 10;
constexpr int W2_MID = 4;

__global__ __launch_bounds__(512, 2) void k_main(const u16* __restrict__ Dsk, char* __restrict__ ws, u16* __restrict__ out) {
  __shared__ __attribute__((aligned(256))) char smem[MN_SMEM];
  const int tid = threadIdx.x, wv = __builtin_amdgcn_readfirstlane(tid >> 6), lane = tid & 63;
  const int r16 = lane & 15, g = lane >> 4, q = r16 >> 2, p = lane & 3;
  const int b = blockIdx.x, hw = wv >> 2, w4 = wv & 3, t256 = tid & 255;
  const rsrc_t rs = __builtin_amdgcn_make_buffer_rsrc((void*)ws, 0, 0x7fffffff, 0x00020000);
  u32* cnt = (u32*)(ws + WS_CNT);
  const int idx = b >> 3, tt = ((b & 7) >> 1) * 8 + (idx & 7), sl = (b & 1) * 4 + (idx >> 3);
  const int pbase = 7 * sl - (sl >> 2), pcnt = (sl & 3) == 3 ? 6 : 7;
  const bool active = wv < pcnt;
  const int p0 = pbase + (active ? wv : 0);
  TS(1, 0);

  const bool three = b < 128;
  const int d0 = three ? 3 * b : 384 + 2 * (b - 128);
  const int dA = d0 + hw, dC = three ? d0 + 2 : d0;
  bf16x8 af[W1_RING][2];
  const u32 l16 = lane * 16;
  const u32 w1o = (u32)WS_W1F + (u32)(2 * p0) * 20 * 1024;
  {
    const u16* xT = (const u16*)(ws + WS_XT);
    const float* modp = (const float*)(ws + WS_MODP);
    const char* ktA = ws + WS_KT + (size_t)dA * KTAB;
    const u32x4 tA0 = *(const u32x4*)(ktA + lane * 16), tA1 = *(const u32x4*)(ktA + (lane < 17 ? 64 + lane : 0) * 16);
    const int mj = (lane & 15) >> 2;
    float mp = modp[(lane & 3) * (6 * D_) + (mj < 3 ? mj : 4) * D_ + ((lane & 16) ? dC : dA)];
    const int tm1 = t256 > 0 ? t256 - 1 : 0;
    const u32x4 xA0 = *(const u32x4*)(xT + (size_t)dA * L_ + 8 * tm1);
    const u32x4 xA = *(const u32x4*)(xT + (size_t)dA * L_ + 8 * t256);
    u32x4 sq[4];
#pragma unroll
    for (int i = 0; i < 4; ++i) sq[i] = *(const u32x4*)(ws + WS_NT + (i < 2 ? tm1 : t256) * 32 + (i & 1) * 16);
    const char* pc0 = hw == 0 ? ws + WS_KT + (size_t)dC * KTAB + lane * 16 : (const char*)(xT + (size_t)dC * L_ + 8 * tm1);
    const char* pc1 = hw == 0 ? ws + WS_KT + (size_t)dC * KTAB + (lane < 17 ? 64 + lane : 0) * 16 : (const char*)(xT + (size_t)dC * L_ + 8 * t256);
    const u32x4 c0 = *(const u32x4*)pc0, c1 = *(const u32x4*)pc1;
    const u16 dskAr = Dsk[dA], dskCr = Dsk[dC];
    __builtin_amdgcn_sched_barrier(0);
    asm volatile("" ::"v"(xA0), "v"(c0), "v"(sq[0]));
    TS(3, 2);
    const float dskA = bf2f(dskAr), dskC = bf2f(dskCr);
    char* smA = smem + hw * CV_CH;
    char* smC = smem + 2 * CV_CH;
    conv_stage_k(smA, tA0, tA1, w4, lane, t256);
    if (three && hw == 0) conv_stage_k(smC, c0, c1, w4, lane, t256);
    mp += __shfl_xor(mp, 1); mp += __shfl_xor(mp, 2);
    mp = rb(mp);
    auto rl = [&](int l) { return __uint_as_float(__builtin_amdgcn_readlane(__float_as_uint(mp), l)); };
    const float saA = rl(0), caA = rl(4), gaA = rl(8), c1A = rb(1.f + rl(12));
    const float saC = rl(16), caC = rl(20), gaC = rl(24), c1C = rb(1.f + rl(28));
    float mn[16], rsd[16];
#pragma unroll
    for (int i = 2; i < 16; ++i) { mn[i] = lo2f(sq[i >> 2][i & 3]); rsd[i] = __builtin_amdgcn_rcpf(hi2f(sq[i >> 2][i & 3])); }
    mn[0] = mn[1] = rsd[0] = rsd[1] = 0.f;
    conv_stage_h(smA, xA0, xA, mn, rsd, rb(1.f + caA), saA, t256);
    if (three && hw == 1) conv_stage_h(smC, c0, c1, mn, rsd, rb(1.f + caC), saC, t256);
    __builtin_amdgcn_sched_barrier(0);
    TS(3, 3);
    const int t1 = (b - 128) * 4 + wv;
    const bool do1 = !three && wv < 4 && t1 < 432;
    u32x4 rv[5];
    f32x4 accA[8], accC[8];
    if (do1) {
#pragma unroll
      for (int i = 0; i < 5; ++i) rv[i] = __builtin_bit_cast(u32x4, ld_frag(rs, l16, (u32)WS_W1F + (t1 * 5 + i) * 1024));
      const float* mq = modp + 160 * (t1 & 3) + 4 * (lane < 40 ? lane : 0);
#pragma unroll
      for (int k = 0; k < KSM; ++k) { accC[k] = *(const f32x4*)(mq + k * (6 * D_) + 4 * D_); accC[4 + k] = *(const f32x4*)(mq + k * (6 * D_) + 3 * D_); }
    }
    f32x4 gbp;
#pragma unroll
    for (int k = 0; k < KSM; ++k) gbp[k] = modp[k * (6 * D_) + 5 * D_ + sl * 80 + (tid < 80 ? tid : 0)];
    __builtin_amdgcn_sched_barrier(0);
    __syncthreads();
    TS(1, 1);
    conv_mma(smA, w4, lane, accA, [&](int it) {
      if (it < 5 || (it >= 8 && it < 13)) {
        const int ks = it < 5 ? it : it - 3;
#pragma unroll
        for (int fb = 0; fb < 2; ++fb) af[ks][fb] = ld_frag(rs, l16, w1o + (fb * 20 + ks) * 1024);
      }
    });
    if (three && hw == 0) conv_mma(smC, w4, lane, accC, [](int) {});
    if (tid < 80) ((float*)(smem + MN_GB))[tid] = rb((gbp[0] + gbp[1]) + (gbp[2] + gbp[3]));
    if (do1) {
      char* buf = smC + wv * 1024;
      const f32x4 mc = (accC[0] + accC[1]) + (accC[2] + accC[3]), ms = (accC[4] + accC[5]) + (accC[6] + accC[7]);
      if (lane < 40) {
        *(u32x2*)(buf + lane * 8) = u32x2{cvtpk(1.f + rb(mc[0]), 1.f + rb(mc[1])), cvtpk(1.f + rb(mc[2]), 1.f + rb(mc[3]))};
        *(u32x2*)(buf + 512 + lane * 8) = u32x2{cvtpk(ms[0], ms[1]), cvtpk(ms[2], ms[3])};
      }
      wave_sync();
      float S = 0.f, B = 0.f;
#pragma unroll
      for (int i = 0; i < 5; ++i) {
        const u32x4 c1p = *(const u32x4*)(buf + i * 64 + g * 16), sbp = *(const u32x4*)(buf + 512 + i * 64 + g * 16);
#pragma unroll
        for (int k = 0; k < 4; ++k) { S = dot2bf(rv[i][k], c1p[k], S); B = dot2bf(rv[i][k], sbp[k], B); }
      }
      S += __shfl_xor(S, 16); S += __shfl_xor(S, 32);
      B += __shfl_xor(B, 16); B += __shfl_xor(B, 32);
      if (lane < 32)
        __builtin_amdgcn_raw_buffer_store_b32(__float_as_uint(lane < 16 ? S : B), rs, (g * 4 * F_ + r16) * 4, (u32)WS_SB + ((t1 & 3) * F_ + 16 * (t1 >> 2)) * 4, 16);
    }
    __syncthreads();
    TS(1, 2);
    conv_put(smA, w4, lane, accA);
    if (three && hw == 0) conv_put(smC, w4, lane, accC);
    __syncthreads();
    u32x4 os;
    st_sc1(rs, t256 * 16, (u32)WS_X1T + dA * (L_ * 2), conv_fin(smA, t256, xA, dskA, gaA, c1A, os));
    st_sc1(rs, t256 * 16, (u32)WS_X1S + dA * (L_ * 2), os);
    if (three && hw == 1) {
      st_sc1(rs, t256 * 16, (u32)WS_X1T + dC * (L_ * 2), conv_fin(smC, t256, c1, dskC, gaC, c1C, os));
      st_sc1(rs, t256 * 16, (u32)WS_X1S + dC * (L_ * 2), os);
    }
  }
  asm volatile("s_waitcnt vmcnt(0)" ::: "memory");
  __syncthreads();
  TS(1, 3);
  if (wv == 7) {
    u32 o = 0;
    if (lane == 0) o = __hip_atomic_fetch_add(cnt + (b & 7) * 64, 1u, __ATOMIC_RELAXED, __HIP_MEMORY_SCOPE_AGENT);
    o = __builtin_amdgcn_readfirstlane(o);
    if (o == 31u && lane < 8) __hip_atomic_store(cnt + (8 + lane) * 64 + (b & 7), 1u, __ATOMIC_RELAXED, __HIP_MEMORY_SCOPE_AGENT);
    for (int spins = 0; spins < (1 << 20); ++spins) {
      u32 v = 1u;
      if (lane < 8) v = __hip_atomic_load(cnt + (8 + (b & 7)) * 64 + lane, __ATOMIC_RELAXED, __HIP_MEMORY_SCOPE_AGENT);
      if (__all(v != 0u)) break;
      __builtin_amdgcn_s_sleep(1);
    }
  }
  __syncthreads();
  TS(1, 4);

  const int k0 = 7 * wv - (wv >> 2), nk = (wv & 3) == 3 ? 6 : 7;
  const u32 w2o = (u32)WS_W2F + (u32)((sl * 5) * 54 + k0) * 1024;
  bf16x8 wf[7][5];
  {
    const int l8 = tid & 7, crow = tid >> 3;
    u32x4 xv[12];
    f32x4 sb4;
    f32x4 acc[2][4];
#pragma unroll
    for (int fb = 0; fb < 2; ++fb)
#pragma unroll
      for (int tb = 0; tb < 4; ++tb) acc[fb][tb] = f32x4{0.f, 0.f, 0.f, 0.f};
    if (wv < 7) {
#pragma unroll
      for (int qq = 0; qq < 12; ++qq)
        xv[qq] = ld_sc1(rs, (qq < 11 || crow < 24 ? crow : 0) * (L_ * 2) + l8 * 16, (u32)WS_X1S + qq * (56 * L_ * 2) + tt * 128);
      __builtin_amdgcn_sched_barrier(0);
#pragma unroll
      for (int qq = 0; qq < 12; ++qq) {
        const int c = crow + 56 * qq;
        if (qq < 11 || crow < 24) *(u32x4*)(smem + M1_A + c * 128 + (((l8 >> 1) ^ h2_swz(c)) << 5) + ((l8 & 1) << 4)) = xv[qq];
      }
    } else {
      __builtin_amdgcn_s_setprio(3);
#pragma unroll
      for (int i = 0; i < 10; ++i) af[i >> 1][i & 1] = ld_frag1(rs, (lane >> 3) * (L_ * 2) + (lane & 7) * 16, (u32)WS_X1T + (sl * 80 + 8 * i) * (L_ * 2) + tt * 128);
    }
    __syncthreads();
    TS(1, 5);
    TC(3, 0);
    const int row0 = 8 * g + q, sw = h2_swz(row0);
    const char* rb0 = smem + M1_A + row0 * 128 + 8 * p;
    float* st = (float*)(smem + M1_ST);
    auto ldb = [&](int ks, bf16x8 (&bfr)[4]) {
#pragma unroll
      for (int tb = 0; tb < 4; ++tb) {
        const s16x4 lo = tr_read(rb0 + ks * 4096 + ((tb ^ sw) << 5));
        const s16x4 hi = tr_read(rb0 + ks * 4096 + 512 + ((tb ^ sw) << 5));
        bfr[tb] = cat8(lo, hi);
      }
    };
    bf16x8 bb[2][4];
    ldb(0, bb[0]);
#pragma unroll
    for (int ks = 0; ks < 20; ++ks) {
      if (active) {
        if (ks < 19) ldb(ks + 1, bb[(ks + 1) & 1]);
#pragma unroll
        for (int fb = 0; fb < 2; ++fb)
#pragma unroll
          for (int tb = 0; tb < 4; ++tb) acc[fb][tb] = mfma16(af[ks % W1_RING][fb], bb[ks & 1][tb], acc[fb][tb]);
        if (ks + W1_RING < 20) {
#pragma unroll
          for (int fb = 0; fb < 2; ++fb) af[ks % W1_RING][fb] = ld_frag(rs, l16, w1o + (fb * 20 + ks + W1_RING) * 1024);
        }
      } else if (ks == 0 && wv == 7) {
#pragma unroll
        for (int i = 0; i < 10; ++i) {
          const u32x4 v = __builtin_bit_cast(u32x4, af[i >> 1][i & 1]);
          const f32x4 a0 = {lo2f(v[0]), hi2f(v[0]), lo2f(v[1]), hi2f(v[1])}, a1 = {lo2f(v[2]), hi2f(v[2]), lo2f(v[3]), hi2f(v[3])};
          acc[0][0] += a0; acc[0][1] += a1; acc[0][2] += a0 * a0; acc[0][3] += a1 * a1;
        }
        const bool b5 = lane & 32, b4 = lane & 16, b3 = lane & 8;
        float v4[2][4], v2[2][2], v1[2];
#pragma unroll
        for (int h = 0; h < 2; ++h) {
#pragma unroll
          for (int j = 0; j < 4; ++j) {
            const float lo = acc[0][2 * h][j], hi = acc[0][2 * h + 1][j];
            v4[h][j] = (b5 ? hi : lo) + __shfl_xor(b5 ? lo : hi, 32);
          }
#pragma unroll
          for (int j = 0; j < 2; ++j) v2[h][j] = (b4 ? v4[h][2 + j] : v4[h][j]) + __shfl_xor(b4 ? v4[h][j] : v4[h][2 + j], 16);
          v1[h] = (b3 ? v2[h][1] : v2[h][0]) + __shfl_xor(b3 ? v2[h][0] : v2[h][1], 8);
        }
        const u32 toff = (8 * (lane & 7) + (lane >> 3)) * 8;
        __builtin_amdgcn_raw_buffer_store_b64(u32x2{__float_as_uint(v1[0]), __float_as_uint(v1[1]) | 0x80000000u}, rs, toff,
                                              (u32)WS_ST + (tt * 8 + sl) * 512, 16);
#pragma unroll
        for (int i = 0; i < 10; ++i) {
          const u32x4 v = __builtin_bit_cast(u32x4, af[i >> 1][i & 1]);
          char* dp = smem + M2_X + 8 * (lane & 7) * M2_XS + 2 * (8 * i + (lane >> 3));
#pragma unroll
          for (int k = 0; k < 4; ++k) {
            *(u16*)(dp + (2 * k) * M2_XS) = (u16)(v[k] & 0xffffu);
            *(u16*)(dp + (2 * k + 1) * M2_XS) = (u16)(v[k] >> 16);
          }
        }
        u32x2 gv[8];
        for (int spins = 0; spins < (1 << 20); ++spins) {
          u32 ok = 0x80000000u;
#pragma unroll
          for (int j = 0; j < 8; ++j) { gv[j] = __builtin_amdgcn_raw_buffer_load_b64(rs, lane * 8, (u32)WS_ST + (tt * 8 + j) * 512, 16); ok &= gv[j][1]; }
          if (__all(ok != 0u)) break;
          __builtin_amdgcn_s_sleep(2);
        }
        float sm = 0.f, sq = 0.f;
#pragma unroll
        for (int j = 0; j < 8; ++j) { sm += __uint_as_float(gv[j][0]); sq += __uint_as_float(gv[j][1] & 0x7fffffffu); }
        const float mean = sm * (1.f / D_);
        const float var = fmaxf(sq * (1.f / D_) - mean * mean, 0.f);
        st[lane] = rb(mean);
        st[64 + lane] = 1.f / rb(sqrtf(rb(rb(var) + 1e-5f)));
        __builtin_amdgcn_s_setprio(0);
      }
      if (ks >= 10) {
#pragma unroll
        for (int j = 0; j < 2; ++j) {
          const int f = 2 * (ks - 10) + j;
          wf[f / 5][f % 5] = ld_frag(rs, l16, w2o + ((f % 5) * 54 + f / 5) * 1024);
        }
      }
      if (ks == 9) {
        sb4 = __builtin_bit_cast(f32x4, ld_sc1(rs, ((lane >> 3) * F_ + 4 * (lane & 7)) * 4, (u32)WS_SB + 32 * p0 * 4));
      }
      if (ks == 17) {
#pragma unroll
        for (int e = 0; e < 4; ++e) { sb4[e] += __shfl_xor(sb4[e], 8); sb4[e] += __shfl_xor(sb4[e], 16); }
        if ((lane & 24) == 0) *(f32x4*)(smem + MN_SB + wv * 256 + (lane >> 5) * 128 + (lane & 7) * 16) = sb4;
      }
      __builtin_amdgcn_sched_barrier(0);
    }
    TS(2, 1);
    TC(3, 1);
#pragma unroll
    for (int i = W2_MID; i < 7; ++i)
#pragma unroll
      for (int cb = 0; cb < 5; ++cb) wf[i][cb] = ld_frag(rs, l16, w2o + (cb * 54 + (i < 6 || nk == 7 ? i : 5)) * 1024);
    __builtin_amdgcn_sched_barrier(0);
    __syncthreads();
    TS(2, 2);
    if (active) {
      const float* sbp = (const float*)(smem + MN_SB) + wv * 64 + 4 * g;
      const u32 hoff = (u32)WS_HID + (u32)((tt * 4) * 54 + p0) * 1024;
#pragma unroll
      for (int tb = 0; tb < 4; ++tb) {
        const float mb = st[tb * 16 + r16], rsd = st[64 + tb * 16 + r16];
        u32x4 v;
#pragma unroll
        for (int hh = 0; hh < 2; ++hh) {
          const f32x4 sv = *(const f32x4*)(sbp + 16 * hh), bv = *(const f32x4*)(sbp + 32 + 16 * hh);
          const f32x4 a = (acc[hh][tb] - mb * sv) * rsd + bv;
          v[2 * hh] = cvtpk(fmaxf(a[0], 0.f), fmaxf(a[1], 0.f));
          v[2 * hh + 1] = cvtpk(fmaxf(a[2], 0.f), fmaxf(a[3], 0.f));
        }
        st_sc1(rs, l16, hoff + tb * 54 * 1024, v);
      }
    }
  }
  TS(2, 3);
  asm volatile("s_waitcnt vmcnt(0)" ::: "memory");
  __syncthreads();
  TS(2, 4);
  if (tid == 448) __hip_atomic_store(cnt + (16 + tt * 8 + sl) * 64, 1u, __ATOMIC_RELAXED, __HIP_MEMORY_SCOPE_AGENT);
  __builtin_amdgcn_sched_barrier(0);
  poll_words<1>(cnt + (16 + tt * 8 + wv) * 64, 1u, lane);
  TS(1, 6);

  {
    const u32 hoff = (u32)WS_HID + (u32)((tt * 4) * 54 + k0) * 1024;
    u32x4 hf[2][4];
#pragma unroll
    for (int i = 0; i < 2; ++i)
#pragma unroll
      for (int tb = 0; tb < 4; ++tb) hf[i][tb] = ld_sc1(rs, l16, hoff + (tb * 54 + i) * 1024);
    f32x4 acc[5][4];
#pragma unroll
    for (int cb = 0; cb < 5; ++cb)
#pragma unroll
      for (int tb = 0; tb < 4; ++tb) acc[cb][tb] = f32x4{0.f, 0.f, 0.f, 0.f};
#pragma unroll
    for (int i = 0; i < 7; ++i) {
      if (i < 6 || nk == 7) {
#pragma unroll
        for (int tb = 0; tb < 4; ++tb)
#pragma unroll
          for (int cb = 0; cb < 5; ++cb) acc[cb][tb] = mfma16(wf[i][cb], __builtin_bit_cast(bf16x8, hf[i & 1][tb]), acc[cb][tb]);
      }
      __builtin_amdgcn_sched_barrier(0);
      if (i + 2 < 7) {
        const int kk = (i + 2 < 6 || nk == 7) ? i + 2 : 5;
#pragma unroll
        for (int tb = 0; tb < 4; ++tb) hf[i & 1][tb] = ld_sc1(rs, l16, hoff + (tb * 54 + kk) * 1024);
        __builtin_amdgcn_sched_barrier(0);
      }
    }
    TS(2, 5);
#pragma unroll
    for (int tb = 0; tb < 4; ++tb)
#pragma unroll
      for (int cb = 0; cb < 5; ++cb) {
        const int owner = tb + (cb < 3 ? 0 : 4);
        if (owner != wv) {
          const int slot = wv < owner ? wv : wv - 1;
          *(f32x4*)(smem + M2_RED + (((tb * 5 + cb) * 7 + slot) * 64 + lane) * 16) = acc[cb][tb];
        }
      }
    f32x4 own[3];
#pragma unroll
    for (int o = 0; o < 8; ++o) {
      if (wv == o) {
        asm volatile("" ::: "memory");
#pragma unroll
        for (int j = 0; j < 3; ++j) own[j] = acc[o < 4 ? j : (j < 2 ? 3 + j : 4)][o & 3];
      }
    }
    __syncthreads();
    TS(2, 6);
    const int tok = 16 * w4 + r16, cb0 = wv < 4 ? 0 : 3;
    f32x4 pr[3][7], gb4[3];
    u32x2 xr[3];
#pragma unroll
    for (int j = 0; j < 3; ++j) {
      const int cb = (j < 2 || wv < 4) ? cb0 + j : 4;
#pragma unroll
      for (int s7 = 0; s7 < 7; ++s7) pr[j][s7] = *(const f32x4*)(smem + M2_RED + (((w4 * 5 + cb) * 7 + s7) * 64 + lane) * 16);
      gb4[j] = *(const f32x4*)(smem + MN_GB + (16 * cb + 4 * g) * 4);
      xr[j] = *(const u32x2*)(smem + M2_X + tok * M2_XS + 2 * (16 * cb + 4 * g));
    }
    __builtin_amdgcn_sched_barrier(0);
#pragma unroll
    for (int j = 0; j < 3; ++j) {
      const f32x4 y = ((own[j] + pr[j][0]) + (pr[j][1] + pr[j][2])) + ((pr[j][3] + pr[j][4]) + (pr[j][5] + pr[j][6]));
      const u32 y0 = cvtpk(y[0], y[1]), y1 = cvtpk(y[2], y[3]);
      const u32 v0 = cvtpk(lo2f(y0) * gb4[j][0], hi2f(y0) * gb4[j][1]), v1 = cvtpk(lo2f(y1) * gb4[j][2], hi2f(y1) * gb4[j][3]);
      u32x2 o;
      o[0] = cvtpk(lo2f(xr[j][0]) + lo2f(v0), hi2f(xr[j][0]) + hi2f(v0));
      o[1] = cvtpk(lo2f(xr[j][1]) + lo2f(v1), hi2f(xr[j][1]) + hi2f(v1));
      if (j < 2 || wv < 4) *(u32x2*)(out + (size_t)(tt * 64 + tok) * D_ + sl * 80 + 16 * (cb0 + j) + 4 * g) = o;
    }
  }
  TS(1, 7);
}

extern "C" void kernel_launch(void* const* d_in, const int* in_sizes, int n_in, void* d_out, int out_size, void* d_ws,
                              size_t ws_size, hipStream_t stream) {
  char* ws = (char*)d_ws;
  k_pre<<<NB_LN + NB_KG + NB_MOD + NB_RL, 256, 0, stream>>>(
      (const u16*)d_in[0], (const u16*)d_in[1], (const u16*)d_in[2], (const u16*)d_in[3], (const u16*)d_in[4],
      (const u16*)d_in[5], (const u16*)d_in[6], (const u16*)d_in[7], (const u16*)d_in[10], (const u16*)d_in[11],
      (const u16*)d_in[12], (const u16*)d_in[13], ws);
  k_main<<<256, 512, 0, stream>>>((const u16*)d_in[8], ws, (u16*)d_out);
}
```

```cpp
#include <hip/hip_runtime.h>
#include <hip/hip_bf16.h>
#include <stdint.h>

typedef unsigned short u16;
typedef unsigned int u32;
typedef __attribute__((ext_vector_type(8))) short bf16x8;
typedef __attribute__((ext_vector_type(4))) short s16x4;
typedef __attribute__((ext_vector_type(4))) float f32x4;
typedef __attribute__((ext_vector_type(2))) float f32x2;
typedef __attribute__((ext_vector_type(4))) unsigned int u32x4;
typedef __attribute__((ext_vector_type(2))) unsigned int u32x2;
typedef __attribute__((ext_vector_type(3))) unsigned int u32x3;
typedef __bf16 bf16x2_t __attribute__((ext_vector_type(2)));
#define LDS_AS __attribute__((address_space(3)))
#define DI __device__ __forceinline__

constexpr int L_ = 2048, D_ = 640, N_ = 128, F_ = 1728;
constexpr int KSM = 4;

constexpr size_t WS_MODP = 0;
constexpr size_t WS_CNT = 126976;
constexpr size_t WS_NT = 262144;
constexpr size_t WS_XT = WS_NT + (size_t)D_ * L_ * 2;
constexpr size_t WS_X1T = WS_XT + (size_t)D_ * L_ * 2;
constexpr size_t WS_X1S = WS_X1T + (size_t)D_ * L_ * 2;
constexpr int KTAB = 1296;
constexpr size_t WS_KT = WS_X1S + (size_t)D_ * L_ * 2;
constexpr size_t WS_SB = WS_KT + (size_t)D_ * KTAB;
constexpr size_t WS_W1F = WS_SB + 2 * 20 * 1728 * 4;
constexpr size_t WS_W2F = WS_W1F + (size_t)108 * 20 * 1024;
constexpr size_t WS_HID = WS_W2F + (size_t)40 * 54 * 1024;
constexpr size_t WS_ST = WS_HID + (size_t)128 * 54 * 1024;

#define TS(kern, slot) do {} while (0)
#define TC(kern, slot) do {} while (0)

DI void st_wt(void* p, u32x4 v) { asm volatile("global_store_dwordx4 %0, %1, off sc1\n\ts_nop 1" ::"v"(p), "v"(v) : "memory"); }
#define ST_PRE(p, v) (*(p) = (v))
#define ST_PREW(p, v) (*(p) = (v))
DI u32 cvtpk(float lo, float hi) {
  f32x2 v = {lo, hi};
  bf16x2_t r = __builtin_convertvector(v, bf16x2_t);
  return __builtin_bit_cast(u32, r);
}
typedef _Float16 f16x2_t __attribute__((ext_vector_type(2)));
typedef _Float16 f16x8_t __attribute__((ext_vector_type(8)));
DI u32 cvtph(float lo, float hi) {
  f32x2 v = {lo, hi};
  f16x2_t r = __builtin_convertvector(v, f16x2_t);
  return __builtin_bit_cast(u32, r);
}
DI float dot2bf(u32 a, u32 b, float c) {
  return __builtin_amdgcn_fdot2_f32_bf16(__builtin_bit_cast(bf16x2_t, a), __builtin_bit_cast(bf16x2_t, b), c, false);
}
DI float lo2f(u32 v) { return __uint_as_float(v << 16); }
DI float hi2f(u32 v) { return __uint_as_float(v & 0xffff0000u); }
DI float bf2f(u16 v) { return __uint_as_float(((u32)v) << 16); }
DI float rb(float x) { return lo2f(cvtpk(x, x)); }
DI f32x4 mfma16(bf16x8 a, bf16x8 b, f32x4 c) { return __builtin_amdgcn_mfma_f32_16x16x32_bf16(a, b, c, 0, 0, 0); }
DI s16x4 tr_read(const char* p) {
  return __builtin_amdgcn_ds_read_tr16_b64_v4i16((LDS_AS s16x4*)p);
}
DI bf16x8 cat8(s16x4 a, s16x4 b) { return __builtin_shufflevector(a, b, 0, 1, 2, 3, 4, 5, 6, 7); }
DI void wave_sync() {
  __builtin_amdgcn_fence(__ATOMIC_RELEASE, "wavefront");
  __builtin_amdgcn_wave_barrier();
  __builtin_amdgcn_fence(__ATOMIC_ACQUIRE, "wavefront");
}

DI f32x2 cmul2(f32x2 a, f32x2 b) {
  const f32x2 bj = {-b.y, b.x};
  return __builtin_elementwise_fma(a.yy, bj, a.xx * b);
}
struct c32 { float r, i; };
DI c32 cmul(c32 a, c32 b) { return {a.r * b.r - a.i * b.i, a.r * b.i + a.i * b.r}; }
DI c32 cexp_scaled(float ar, float ai, float e) {
  const float mag = __expf(ar * e);
  const float x = ai * e;
  const float C_HI = 0.15915494f, C_LO = 6.4206382e-9f;
  const float r = x * C_HI;
  const float rl = fmaf(x, C_HI, -r) + x * C_LO;
  const float fr = (r - floorf(r)) + rl;
  return {mag * __builtin_amdgcn_cosf(fr), mag * __builtin_amdgcn_sinf(fr)};
}
DI int tap_index(int m) {
  u32 bits = __float_as_uint((float)m);
  bits += 0x7fffu + ((bits >> 16) & 1u);
  const int id2 = (int)(bits >> 16) - (0x4300 - 128);
  return m < 128 ? m : id2;
}

constexpr int NB_LN = 64, NB_KG = 320, NB_MOD = 30 * KSM;
constexpr int NB_RL = 70;
constexpr int PRE_SMEM = 43008;

DI void role_ln1(int b, const u16* __restrict__ x, u32* __restrict__ st1, u16* __restrict__ xT, char* smem) {
  const int tid = threadIdx.x, u = tid >> 4, j = tid & 15;
  const int t0 = b * 32;
  const u32x4* xa = (const u32x4*)(x + (size_t)(t0 + 2 * u) * D_);
  u32x4 A[5], B[5];
#pragma unroll
  for (int q = 0; q < 5; ++q) { A[q] = xa[j + 16 * q]; B[q] = xa[80 + j + 16 * q]; }
  float sa = 0.f, sb = 0.f, qa = 0.f, qb = 0.f;
#pragma unroll
  for (int q = 0; q < 5; ++q)
#pragma unroll
    for (int k = 0; k < 4; ++k) {
      sa = dot2bf(A[q][k], 0x3f803f80u, sa); qa = dot2bf(A[q][k], A[q][k], qa);
      sb = dot2bf(B[q][k], 0x3f803f80u, sb); qb = dot2bf(B[q][k], B[q][k], qb);
    }
#pragma unroll
  for (int o = 1; o < 16; o <<= 1) {
    sa += __shfl_xor(sa, o, 16); sb += __shfl_xor(sb, o, 16); qa += __shfl_xor(qa, o, 16); qb += __shfl_xor(qb, o, 16);
  }
  const float ma = sa * (1.f / D_), mb = sb * (1.f / D_);
  const float va = fmaxf(qa - sa * ma, 0.f), vb = fmaxf(qb - sb * mb, 0.f);
  if (j == 0) {
    const u32 s0 = cvtpk(ma, sqrtf(rb(rb(va * (1.f / D_)) + 1e-5f))), s1 = cvtpk(mb, sqrtf(rb(rb(vb * (1.f / D_)) + 1e-5f)));
    *(u32x2*)(st1 + t0 + 2 * u) = u32x2{s0, s1};
  }
  char* wbase = smem + (8 * j + (j >> 2)) * 64 + ((((u >> 2) ^ (j & 3)) << 2) + (u & 3)) * 4;
#pragma unroll
  for (int q = 0; q < 5; ++q)
#pragma unroll
    for (int k = 0; k < 4; ++k) {
      *(u32*)(wbase + (132 * q + 2 * k) * 64) = (A[q][k] & 0xffffu) | (B[q][k] << 16);
      *(u32*)(wbase + (132 * q + 2 * k + 1) * 64) = (A[q][k] >> 16) | (B[q][k] & 0xffff0000u);
    }
  __syncthreads();
#pragma unroll
  for (int i = 0; i < 10; ++i) {
    const int id = tid + 256 * i, c = id >> 2, o = id & 3;
    const u32x4 v = *(const u32x4*)(smem + (c + (c >> 5)) * 64 + ((o ^ ((c >> 3) & 3)) << 4));
    ST_PRE((u32x4*)(xT + (size_t)c * L_ + t0 + 8 * o), v);
  }
}

DI void role_kgen(int bk, const u16* __restrict__ Are, const u16* __restrict__ Aim, const u16* __restrict__ Bre,
                  const u16* __restrict__ Bim, const u16* __restrict__ Cre, const u16* __restrict__ Cim,
                  const u16* __restrict__ ldt, char* __restrict__ KT, char* smem) {
  const int tid = threadIdx.x, w = tid >> 6, lane = tid & 63, r16 = lane & 15, g = lane >> 4;
  const int d = 2 * bk + (w >> 1), n = (w & 1) * 64 + lane;
  const int gi = d * N_ + n;
  const float dt = rb(expf(bf2f(ldt[d])));
  const float ar = dt * bf2f(Are[gi]), ai = dt * bf2f(Aim[gi]);
  const c32 dB = {dt * bf2f(Bre[gi]), dt * bf2f(Bim[gi])};
  const c32 Cc = {bf2f(Cre[gi]), bf2f(Cim[gi])};
  const c32 W = cmul(Cc, dB);
  const c32 E1 = cexp_scaled(ar, ai, 1.f), E8 = cexp_scaled(ar, ai, 8.f), E128 = cexp_scaled(ar, ai, 128.f);

  float wmax = fmaxf(fabsf(W.r), fabsf(W.i));
#pragma unroll
  for (int o = 1; o < 64; o <<= 1) wmax = fmaxf(wmax, __shfl_xor(wmax, o));
  int ew = (int)((__float_as_uint(wmax) >> 23) & 0xff);
  ew = ew < 20 ? 20 : (ew > 240 ? 240 : ew);
  const float wsc = __uint_as_float((u32)(127 + 10 + 127 - ew - 1) << 23);
  const float unsc = __uint_as_float((u32)(ew + 1 - 20) << 23);

  char* buf = smem + w * 8704;
  char* wr = buf + lane * 4;
  const char* rd = buf + r16 * 272 + g * 16;
  auto put = [&](int row, f32x2 v) { *(u32*)(wr + row * 272) = cvtph(v.x, v.y); };
  {
    const f32x2 e8 = {E8.r, E8.i};
    f32x2 p = {1024.f, 0.f};
#pragma unroll
    for (int jj = 0; jj < 16; ++jj) { put(jj, p); if (jj < 15) p = cmul2(p, e8); }
  }
  wave_sync();
  f16x8_t bh[4];
#pragma unroll
  for (int ks = 0; ks < 4; ++ks) bh[ks] = *(const f16x8_t*)(rd + ks * 64);
  wave_sync();
  const f32x2 e1 = {E1.r, -E1.i}, e128 = {E128.r, -E128.i};
  f32x2 sm[8];
  sm[1] = e1;
#pragma unroll
  for (int i = 2; i < 8; ++i) sm[i] = cmul2(sm[i - 1], e1);
  f32x2 WG[17];
  WG[0] = f32x2{W.r * wsc, -W.i * wsc};
#pragma unroll
  for (int i = 1; i < 17; ++i) WG[i] = cmul2(WG[i - 1], e128);
  f32x4 acc[3];
  auto tile_mma = [&](int t) {
    wave_sync();
    f32x4 a = {0.f, 0.f, 0.f, 0.f};
#pragma unroll
    for (int ks = 0; ks < 4; ++ks) a = __builtin_amdgcn_mfma_f32_16x16x32_f16(*(const f16x8_t*)(rd + ks * 64), bh[ks], a, 0, 0, 0);
    acc[t] = a * unsc;
    wave_sync();
  };
#pragma unroll
  for (int i = 0; i < 2; ++i)
#pragma unroll
    for (int rho = 0; rho < 8; ++rho) put(8 * i + rho, rho ? cmul2(sm[rho], WG[i]) : WG[i]);
  tile_mma(0);
#pragma unroll
  for (int i = 0; i < 2; ++i)
#pragma unroll
    for (int rho = 0; rho < 4; ++rho) put(4 * i + rho, rho ? cmul2(sm[2 * rho], WG[2 + i]) : WG[2 + i]);
#pragma unroll
  for (int i = 0; i < 4; ++i)
#pragma unroll
    for (int rho = 0; rho < 2; ++rho) put(8 + 2 * i + rho, rho ? cmul2(sm[4], WG[4 + i]) : WG[4 + i]);
  tile_mma(1);
#pragma unroll
  for (int i = 0; i < 9; ++i) put(i, WG[8 + i]);
  tile_mma(2);

#pragma unroll
  for (int t = 0; t < 3; ++t) *(f32x4*)(buf + t * 1024 + lane * 16) = acc[t];
  __syncthreads();
  u16* T = (u16*)(smem + (w >> 1) * 2 * 8704 + 4352);
  {
    const char* ob = smem + (w ^ 1) * 8704 + lane * 16;
    if (!(w & 1)) {
      const f32x4 a0 = acc[0] + *(const f32x4*)(ob), a2 = acc[2] + *(const f32x4*)(ob + 2048);
      u16* t0 = T + 128 * (g >> 1) + 4 * (g & 1) + 8 * r16;
      *(u32x2*)t0 = u32x2{cvtpk(a0[0], a0[1]), cvtpk(a0[2], a0[3])};
      if (g < 2) {
#pragma unroll
        for (int rg = 0; rg < 4; ++rg) T[512 + 16 * (4 * g + rg) + r16] = (u16)cvtpk(a2[rg], 0.f);
      } else if (lane == 32) {
        T[640] = (u16)cvtpk(a2[0], 0.f);
      } else if (lane >= 48 && lane < 55) {
        T[641 + lane - 48] = 0;
      }
    } else {
      const f32x4 a1 = acc[1] + *(const f32x4*)(ob + 1024);
      if (g < 2) {
        *(u32x2*)(T + 256 + 64 * g + 4 * r16) = u32x2{cvtpk(a1[0], a1[1]), cvtpk(a1[2], a1[3])};
      } else {
        *(u32*)(T + 384 + 64 * (g - 2) + 2 * r16) = cvtpk(a1[0], a1[1]);
        *(u32*)(T + 384 + 64 * (g - 2) + 32 + 2 * r16) = cvtpk(a1[2], a1[3]);
      }
    }
  }
  __syncthreads();
  const int t128 = tid & 127;
  if (t128 < 81) *(u32x4*)(KT + (size_t)d * KTAB + t128 * 16) = *(const u32x4*)((const char*)T + t128 * 16);
}

DI void role_mod(int bm, const u16* __restrict__ c, const u16* __restrict__ wm, float* __restrict__ modp, char* smem) {
  const int tid = threadIdx.x, cj = tid & 15, rg = tid >> 4, w = tid >> 6;
  const int cbk = bm % 30, ks = bm / 30;
  constexpr int NR = 640 / KSM / 16;
  u32x4 v[NR];
  u16 cr[NR];
#pragma unroll
  for (int i = 0; i < NR; ++i) {
    const int row = (640 / KSM) * ks + rg + 16 * i;
    v[i] = *(const u32x4*)(wm + (size_t)row * (6 * D_) + 128 * cbk + 8 * cj);
    cr[i] = c[row];
  }
  float a[8];
#pragma unroll
  for (int k = 0; k < 8; ++k) a[k] = 0.f;
#pragma unroll
  for (int i = 0; i < NR; ++i) {
    const float cv = bf2f(cr[i]);
    const float sv = rb(cv / (1.f + __expf(-cv)));
#pragma unroll
    for (int k = 0; k < 4; ++k) { a[2 * k] += sv * lo2f(v[i][k]); a[2 * k + 1] += sv * hi2f(v[i][k]); }
  }
#pragma unroll
  for (int k = 0; k < 8; ++k) { a[k] += __shfl_xor(a[k], 16); a[k] += __shfl_xor(a[k], 32); }
  float* red = (float*)smem;
  if ((tid & 63) < 16) {
#pragma unroll
    for (int k = 0; k < 8; ++k) red[w * 128 + 8 * cj + k] = a[k];
  }
  __syncthreads();
  if (tid < 128) modp[ks * (6 * D_) + 128 * cbk + tid] = red[tid] + red[128 + tid] + red[256 + tid] + red[384 + tid];
}

DI int rl_swz(int row) { return (row & 3) | ((((row >> 2) ^ (row >> 3)) & 1) << 2); }

DI void role_relayout(int br, const u16* __restrict__ w1, const u16* __restrict__ w2, char* __restrict__ ws, char* smem) {
  const int tid = threadIdx.x, w = tid >> 6, lane = tid & 63;
  const int tile = br * 4 + w;
  if (tile >= 550) return;
  const bool is1 = tile < 280;
  int ks, nb, ld, nblk;
  const u16* src;
  if (is1) { ks = tile / 14; nb = tile % 14; ld = F_; src = w1; nblk = nb == 13 ? 4 : 8; }
  else { const int t2 = tile - 280; ks = t2 / 5; nb = t2 % 5; ld = D_; src = w2; nblk = 8; }
  char* buf = smem + w * 8192;
  const int lr = lane >> 4, lc = lane & 15;
  u32x4 v[8];
#pragma unroll
  for (int i = 0; i < 8; ++i) v[i] = *(const u32x4*)(src + (size_t)(32 * ks + 4 * i + lr) * ld + 128 * nb + 8 * (lc < 2 * nblk ? lc : 0));
#pragma unroll
  for (int i = 0; i < 8; ++i) {
    const int row = 4 * i + lr;
    *(u32x4*)(buf + row * 256 + (((lc >> 1) ^ rl_swz(row)) << 5) + ((lc & 1) << 4)) = v[i];
  }
  wave_sync();
  const int g = lane >> 4, q = (lane & 15) >> 2, p = lane & 3;
  const int row0 = is1 ? 8 * g + q : 4 * g + q, row1 = is1 ? row0 + 4 : row0 + 16;
  char* dst = is1 ? ws + WS_W1F + ((size_t)(8 * nb) * 20 + ks) * 1024 : ws + WS_W2F + ((size_t)(8 * nb) * 54 + ks) * 1024;
  const int dstride = is1 ? 20 * 1024 : 54 * 1024;
#pragma unroll
  for (int cb = 0; cb < 8; ++cb) {
    const s16x4 lo = tr_read(buf + row0 * 256 + ((cb ^ rl_swz(row0)) << 5) + 8 * p);
    const s16x4 hi = tr_read(buf + row1 * 256 + ((cb ^ rl_swz(row1)) << 5) + 8 * p);
    if (cb < nblk) ST_PREW((u32x4*)(dst + (size_t)cb * dstride + lane * 16), __builtin_bit_cast(u32x4, cat8(lo, hi)));
  }
}

__global__ __launch_bounds__(256) void k_pre(const u16* __restrict__ x, const u16* __restrict__ Are, const u16* __restrict__ Aim,
                                             const u16* __restrict__ Bre, const u16* __restrict__ Bim, const u16* __restrict__ Cre,
                                             const u16* __restrict__ Cim, const u16* __restrict__ ldt, const u16* __restrict__ w1,
                                             const u16* __restrict__ w2, const u16* __restrict__ c, const u16* __restrict__ wm,
                                             char* __restrict__ ws) {
  __shared__ __attribute__((aligned(16))) char smem[PRE_SMEM];
  int b = blockIdx.x;
  b = b < NB_KG ? b + NB_LN : (b < NB_KG + NB_LN ? b - NB_KG : b);
  TS(0, 0);
  if (b < NB_LN) {
    if (b < 34 && threadIdx.x < 64) ((u32*)(ws + WS_CNT))[(b * 8 + (threadIdx.x >> 3)) * 64 + (threadIdx.x & 7)] = 0u;
    *(u32x2*)(ws + WS_ST + (b * 256 + threadIdx.x) * 8) = u32x2{0u, 0u};
    role_ln1(b, x, (u32*)(ws + WS_NT), (u16*)(ws + WS_XT), smem); TS(0, 7); return;
  }
  b -= NB_LN;
  if (b < NB_KG) { role_kgen(b, Are, Aim, Bre, Bim, Cre, Cim, ldt, ws + WS_KT, smem); TS(0, 7); return; }
  b -= NB_KG;
  if (b < NB_MOD) { role_mod(b, c, wm, (float*)(ws + WS_MODP), smem); TS(0, 7); return; }
  b -= NB_MOD;
  role_relayout(b, w1, w2, ws, smem);
  TS(0, 7);
}

constexpr int CV_H0 = 0, CV_HS = 4160, CV_K = 12544, CV_T = 21760, CV_H3 = 32768 + 192, CV_CH = 37376;
static_assert(CV_CH % 256 == 0 && CV_H3 + 4160 <= CV_CH && CV_T + 4 * KTAB <= 32768, "conv lds");
constexpr int M1_A = 0, M1_ST = 81920;
constexpr int MN_SB = 3 * CV_CH;
constexpr int MN_R1 = MN_SB + 2048;
constexpr int M2_RED = 0, M2_X = 143360, M2_XS = 176;
constexpr int MN_GB = M2_X + 64 * M2_XS;
constexpr int MN_SMEM = MN_GB + 320;
static_assert(MN_R1 + 5 * 4864 <= M2_X && M1_ST + 512 <= MN_SB, "mlp lds");

typedef __amdgpu_buffer_rsrc_t rsrc_t;
DI u32x4 ld_sc1(rsrc_t r, u32 voff, u32 soff) { return __builtin_amdgcn_raw_buffer_load_b128(r, voff, soff, 16); }
DI void st_sc1(rsrc_t r, u32 voff, u32 soff, u32x4 v) { __builtin_amdgcn_raw_buffer_store_b128(v, r, voff + soff, 0, 16); }
DI bf16x8 ld_frag1(rsrc_t r, u32 voff, u32 soff) { return __builtin_bit_cast(bf16x8, __builtin_amdgcn_raw_buffer_load_b128(r, voff, soff, 16)); }
DI bf16x8 ld_frag(rsrc_t r, u32 voff, u32 soff) { return __builtin_bit_cast(bf16x8, __builtin_amdgcn_raw_buffer_load_b128(r, voff, soff, 0)); }

DI int h2_swz(int k) { return ((k >> 1) & 1) | (((k >> 3) & 1) << 1); }

DI void conv_stage_k(char* smem, u32x4 tv0, u32x4 tv1, int w, int lane, int tid) {
  char* tb = smem + CV_T + w * KTAB;
  *(u32x4*)(tb + lane * 16) = tv0;
  if (lane < 17) *(u32x4*)(tb + (64 + lane) * 16) = tv1;
  wave_sync();
  const u16* T = (const u16*)tb;
#pragma unroll
  for (int i = 0; i < 2; ++i) {
    const int cp = tid + 256 * i;
    if (cp < 284) {
      u32 e[9];
#pragma unroll
      for (int j = 0; j < 9; ++j) {
        const int m = 2047 - 8 * cp - j;
        e[j] = T[m < 0 ? 641 : tap_index(m)];
      }
      *(u32x4*)(smem + CV_K + 4576 + cp * 16) = u32x4{e[0] | (e[1] << 16), e[2] | (e[3] << 16), e[4] | (e[5] << 16), e[6] | (e[7] << 16)};
      *(u32x4*)(smem + CV_K + cp * 16) = u32x4{e[1] | (e[2] << 16), e[3] | (e[4] << 16), e[5] | (e[6] << 16), e[7] | (e[8] << 16)};
    }
  }
}

DI void conv_stage_h(char* smem, u32x4 xq0, u32x4 xq1, const float (&mn)[16], const float (&rs)[16], float ca1, float sa, int tid) {
  u32 h[8];
#pragma unroll
  for (int k = 1; k < 8; ++k) {
    const u32 xv = k < 4 ? xq0[k] : xq1[k - 4];
    const u32 c = cvtpk(lo2f(xv) - mn[2 * k], hi2f(xv) - mn[2 * k + 1]);
    const u32 n = cvtpk(lo2f(c) * rs[2 * k], hi2f(c) * rs[2 * k + 1]);
    const u32 p = cvtpk(lo2f(n) * ca1, hi2f(n) * ca1);
    const u32 hv = cvtpk(lo2f(p) + sa, hi2f(p) + sa);
    h[k] = (k >= 4 || tid > 0) ? hv : 0u;
  }
  const int yo = (tid + 3) * 16;
  *(u32x4*)(smem + CV_H0 + yo) = u32x4{h[1], h[2], h[3], h[4]};
  *(u32x4*)(smem + CV_H0 + CV_HS + yo) = u32x4{h[2], h[3], h[4], h[5]};
  *(u32x4*)(smem + CV_H0 + 2 * CV_HS + yo) = u32x4{h[3], h[4], h[5], h[6]};
  *(u32x4*)(smem + CV_H3 + yo) = u32x4{h[4], h[5], h[6], h[7]};
  if (tid < 12) {
    const int cc = tid / 3, yc = tid % 3;
    *(u32x4*)(smem + (cc == 3 ? CV_H3 : CV_H0 + cc * CV_HS) + yc * 16) = u32x4{0u, 0u, 0u, 0u};
  }
}

template <class CB>
DI void conv_mma(const char* smem, int w, int lane, f32x4 (&acc)[8], CB&& side) {
  const int r16 = lane & 15, g = lane >> 4, cpy = r16 & 3;
  const char* hb = smem + (cpy == 3 ? CV_H3 : CV_H0 + cpy * CV_HS) + 16 * ((r16 >> 2) + 4 * w + g);
  const char* kb = smem + CV_K + (r16 & 1) * 4576 + 16 * (252 + 4 * w - 4 * (r16 >> 1) + g) - 256 * 14;
#pragma unroll
  for (int i = 0; i < 8; ++i) acc[i] = f32x4{0.f, 0.f, 0.f, 0.f};
#pragma unroll
  for (int par = 1; par >= 0; --par) {
    bf16x8 Bf[8];
#pragma unroll
    for (int t = 0; t < 8; ++t) Bf[t] = *(const bf16x8*)(hb + 256 * (2 * t + par));
#pragma unroll
    for (int sg = -par; sg <= 14; sg += 2) {
      const bf16x8 Af = *(const bf16x8*)(kb + 256 * (14 - sg));
#pragma unroll
      for (int i = 0; i < 8; ++i) {
        const int tau = 2 * i - sg;
        if (tau >= 0 && tau <= 15) acc[i] = mfma16(Af, Bf[tau >> 1], acc[i]);
      }
      side((1 - par) * 8 + ((sg + par) >> 1));
    }
  }
}

DI void conv_put(char* smem, int w, int lane, const f32x4 (&acc)[8]) {
  char* pb = smem + w * 8192 + (64 * (lane >> 4) + 2 * (lane & 15)) * 4;
#pragma unroll
  for (int i = 0; i < 8; ++i) {
    *(f32x2*)(pb + i * 1024) = f32x2{acc[i][0], acc[i][1]};
    *(f32x2*)(pb + i * 1024 + 128) = f32x2{acc[i][2], acc[i][3]};
  }
}

DI u32x4 conv_fin(const char* smem, int t, u32x4 xv, float dsk, float ga, float c1, u32x4& os) {
  f32x4 y0 = *(const f32x4*)(smem + t * 32), y1 = *(const f32x4*)(smem + t * 32 + 16);
#pragma unroll
  for (int w = 1; w < 4; ++w) { y0 += *(const f32x4*)(smem + w * 8192 + t * 32); y1 += *(const f32x4*)(smem + w * 8192 + t * 32 + 16); }
  const u32x4 hv = *(const u32x4*)(smem + CV_H3 + 48 + t * 16);
  u32x4 o;
#pragma unroll
  for (int k = 0; k < 4; ++k) {
    const u32 yr = k < 2 ? cvtpk(y0[2 * k], y0[2 * k + 1]) : cvtpk(y1[2 * k - 4], y1[2 * k - 3]);
    const u32 hd = cvtpk(lo2f(hv[k]) * dsk, hi2f(hv[k]) * dsk);
    const u32 v = cvtpk(lo2f(yr) + lo2f(hd), hi2f(yr) + hi2f(hd));
    const u32 gv = cvtpk(lo2f(v) * ga, hi2f(v) * ga);
    o[k] = cvtpk(lo2f(xv[k]) + lo2f(gv), hi2f(xv[k]) + hi2f(gv));
    os[k] = cvtpk(lo2f(o[k]) * c1, hi2f(o[k]) * c1);
  }
  return o;
}

template <int NW>
DI void poll_words(const u32* cnt, u32 target, int lane) {
  for (int spins = 0; spins < (1 << 20); ++spins) {
    u32 v = target;
    if (lane < NW) v = __hip_atomic_load(cnt + lane * 64, __ATOMIC_RELAXED, __HIP_MEMORY_SCOPE_AGENT);
    if (__all(v >= target)) break;
    __builtin_amdgcn_s_sleep(1);
  }
}

#define LD_W2 ld_frag1
constexpr int W1_RING = 10;
constexpr int W2_MID = 4;

__global__ __launch_bounds__(512, 2) void k_main(const u16* __restrict__ Dsk, const u16* __restrict__ w2, char* __restrict__ ws,
                                                    u16* __restrict__ out) {
  __shared__ __attribute__((aligned(256))) char smem[MN_SMEM];
  const int tid = threadIdx.x, wv = __builtin_amdgcn_readfirstlane(tid >> 6), lane = tid & 63;
  const int r16 = lane & 15, g = lane >> 4, q = r16 >> 2, p = lane & 3;
  const int b = blockIdx.x, hw = wv >> 2, w4 = wv & 3, t256 = tid & 255;
  const rsrc_t rs = __builtin_amdgcn_make_buffer_rsrc((void*)ws, 0, 0x7fffffff, 0x00020000);
  u32* cnt = (u32*)(ws + WS_CNT);
  const int idx = b >> 3, tt = ((b & 7) >> 1) * 8 + (idx & 7), sl = (b & 1) * 4 + (idx >> 3);
  const int pbase = 7 * sl - (sl >> 2), pcnt = (sl & 3) == 3 ? 6 : 7;
  const bool active = wv < pcnt;
  const int p0 = pbase + (active ? wv : 0);
  TS(1, 0);

  const bool three = b < 128;
  const int d0 = three ? 3 * b : 384 + 2 * (b - 128);
  const int dA = d0 + hw, dC = three ? d0 + 2 : d0;
  bf16x8 af[W1_RING][2];
  const u32 l16 = lane * 16;
  const u32 w1o = (u32)WS_W1F + (u32)(2 * p0) * 20 * 1024;
  {
    const u16* xT = (const u16*)(ws + WS_XT);
    const float* modp = (const float*)(ws + WS_MODP);
    const char* ktA = ws + WS_KT + (size_t)dA * KTAB;
    const u32x4 tA0 = *(const u32x4*)(ktA + lane * 16), tA1 = *(const u32x4*)(ktA + (lane < 17 ? 64 + lane : 0) * 16);
    const int mj = (lane & 15) >> 2;
    float mp = modp[(lane & 3) * (6 * D_) + (mj < 3 ? mj : 4) * D_ + ((lane & 16) ? dC : dA)];
    const int tm1 = t256 > 0 ? t256 - 1 : 0;
    const u32x4 xA0 = *(const u32x4*)(xT + (size_t)dA * L_ + 8 * tm1);
    const u32x4 xA = *(const u32x4*)(xT + (size_t)dA * L_ + 8 * t256);
    u32x4 sq[4];
#pragma unroll
    for (int i = 0; i < 4; ++i) sq[i] = *(const u32x4*)(ws + WS_NT + (i < 2 ? tm1 : t256) * 32 + (i & 1) * 16);
    const char* pc0 = hw == 0 ? ws + WS_KT + (size_t)dC * KTAB + lane * 16 : (const char*)(xT + (size_t)dC * L_ + 8 * tm1);
    const char* pc1 = hw == 0 ? ws + WS_KT + (size_t)dC * KTAB + (lane < 17 ? 64 + lane : 0) * 16 : (const char*)(xT + (size_t)dC * L_ + 8 * t256);
    const u32x4 c0 = *(const u32x4*)pc0, c1 = *(const u32x4*)pc1;
    const u16 dskAr = Dsk[dA], dskCr = Dsk[dC];
    __builtin_amdgcn_sched_barrier(0);
    asm volatile("" ::"v"(xA0), "v"(c0), "v"(sq[0]));
    TS(3, 2);
    const float dskA = bf2f(dskAr), dskC = bf2f(dskCr);
    char* smA = smem + hw * CV_CH;
    char* smC = smem + 2 * CV_CH;
    conv_stage_k(smA, tA0, tA1, w4, lane, t256);
    if (three && hw == 0) conv_stage_k(smC, c0, c1, w4, lane, t256);
    mp += __shfl_xor(mp, 1); mp += __shfl_xor(mp, 2);
    mp = rb(mp);
    auto rl = [&](int l) { return __uint_as_float(__builtin_amdgcn_readlane(__float_as_uint(mp), l)); };
    const float saA = rl(0), caA = rl(4), gaA = rl(8), c1A = rb(1.f + rl(12));
    const float saC = rl(16), caC = rl(20), gaC = rl(24), c1C = rb(1.f + rl(28));
    float mn[16], rsd[16];
#pragma unroll
    for (int i = 2; i < 16; ++i) { mn[i] = lo2f(sq[i >> 2][i & 3]); rsd[i] = __builtin_amdgcn_rcpf(hi2f(sq[i >> 2][i & 3])); }
    mn[0] = mn[1] = rsd[0] = rsd[1] = 0.f;
    conv_stage_h(smA, xA0, xA, mn, rsd, rb(1.f + caA), saA, t256);
    if (three && hw == 1) conv_stage_h(smC, c0, c1, mn, rsd, rb(1.f + caC), saC, t256);
    __builtin_amdgcn_sched_barrier(0);
    TS(3, 3);
    const int t1 = (b - 128) * 4 + wv;
    const bool do1 = !three && wv < 4 && t1 < 432;
    u32x4 rv[5];
    f32x4 accA[8], accC[8];
    if (do1) {
#pragma unroll
      for (int i = 0; i < 5; ++i) rv[i] = __builtin_bit_cast(u32x4, ld_frag(rs, l16, (u32)WS_W1F + (t1 * 5 + i) * 1024));
      const float* mq = modp + 160 * (t1 & 3) + 4 * (lane < 40 ? lane : 0);
#pragma unroll
      for (int k = 0; k < KSM; ++k) { accC[k] = *(const f32x4*)(mq + k * (6 * D_) + 4 * D_); accC[4 + k] = *(const f32x4*)(mq + k * (6 * D_) + 3 * D_); }
    }
    const int t2 = (b - 128) * 3 + wv - 4;
    const bool do2 = !three && wv >= 4 && wv < 7 && t2 < 270;
    if (do2) {
#pragma unroll
      for (int i = 0; i < 8; ++i) accC[i] = *(const f32x4*)(w2 + (size_t)(32 * (t2 / 5) + 4 * i + g) * D_ + 128 * (t2 % 5) + 8 * r16);
    }
    f32x4 gbp;
#pragma unroll
    for (int k = 0; k < KSM; ++k) gbp[k] = modp[k * (6 * D_) + 5 * D_ + sl * 80 + (tid < 80 ? tid : 0)];
    __builtin_amdgcn_sched_barrier(0);
    __syncthreads();
    TS(1, 1);
    conv_mma(smA, w4, lane, accA, [&](int it) {
      if (it < 5 || (it >= 8 && it < 13)) {
        const int ks = it < 5 ? it : it - 3;
#pragma unroll
        for (int fb = 0; fb < 2; ++fb) af[ks][fb] = ld_frag(rs, l16, w1o + (fb * 20 + ks) * 1024);
      }
    });
    if (three && hw == 0) conv_mma(smC, w4, lane, accC, [](int) {});
    if (tid < 80) ((float*)(smem + MN_GB))[tid] = rb((gbp[0] + gbp[1]) + (gbp[2] + gbp[3]));
    if (do2) {
      char* buf = smC + 4096 + (wv - 4) * 8192;
#pragma unroll
      for (int i = 0; i < 8; ++i) {
        const int row = 4 * i + g;
        *(f32x4*)(buf + row * 256 + (((r16 >> 1) ^ rl_swz(row)) << 5) + ((r16 & 1) << 4)) = accC[i];
      }
      wave_sync();
      const int row0 = 4 * g + q, row1 = row0 + 16;
#pragma unroll
      for (int cb = 0; cb < 8; ++cb) {
        const s16x4 lo = tr_read(buf + row0 * 256 + ((cb ^ rl_swz(row0)) << 5) + 8 * p);
        const s16x4 hi = tr_read(buf + row1 * 256 + ((cb ^ rl_swz(row1)) << 5) + 8 * p);
        st_sc1(rs, l16, (u32)WS_W2F + ((8 * (t2 % 5) + cb) * 54 + t2 / 5) * 1024, __builtin_bit_cast(u32x4, cat8(lo, hi)));
      }
    }
    if (do1) {
      char* buf = smC + wv * 1024;
      const f32x4 mc = (accC[0] + accC[1]) + (accC[2] + accC[3]), ms = (accC[4] + accC[5]) + (accC[6] + accC[7]);
      if (lane < 40) {
        *(u32x2*)(buf + lane * 8) = u32x2{cvtpk(1.f + rb(mc[0]), 1.f + rb(mc[1])), cvtpk(1.f + rb(mc[2]), 1.f + rb(mc[3]))};
        *(u32x2*)(buf + 512 + lane * 8) = u32x2{cvtpk(ms[0], ms[1]), cvtpk(ms[2], ms[3])};
      }
      wave_sync();
      float S = 0.f, B = 0.f;
#pragma unroll
      for (int i = 0; i < 5; ++i) {
        const u32x4 c1p = *(const u32x4*)(buf + i * 64 + g * 16), sbp = *(const u32x4*)(buf + 512 + i * 64 + g * 16);
#pragma unroll
        for (int k = 0; k < 4; ++k) { S = dot2bf(rv[i][k], c1p[k], S); B = dot2bf(rv[i][k], sbp[k], B); }
      }
      S += __shfl_xor(S, 16); S += __shfl_xor(S, 32);
      B += __shfl_xor(B, 16); B += __shfl_xor(B, 32);
      if (lane < 32)
        __builtin_amdgcn_raw_buffer_store_b32(__float_as_uint(lane < 16 ? S : B), rs, (g * 4 * F_ + r16) * 4, (u32)WS_SB + ((t1 & 3) * F_ + 16 * (t1 >> 2)) * 4, 16);
    }
    __syncthreads();
    TS(1, 2);
    conv_put(smA, w4, lane, accA);
    if (three && hw == 0) conv_put(smC, w4, lane, accC);
    __syncthreads();
    u32x4 os;
    st_sc1(rs, t256 * 16, (u32)WS_X1T + dA * (L_ * 2), conv_fin(smA, t256, xA, dskA, gaA, c1A, os));
    st_sc1(rs, t256 * 16, (u32)WS_X1S + dA * (L_ * 2), os);
    if (three && hw == 1) {
      st_sc1(rs, t256 * 16, (u32)WS_X1T + dC * (L_ * 2), conv_fin(smC, t256, c1, dskC, gaC, c1C, os));
      st_sc1(rs, t256 * 16, (u32)WS_X1S + dC * (L_ * 2), os);
    }
  }
  asm volatile("s_waitcnt vmcnt(0)" ::: "memory");
  __syncthreads();
  TS(1, 3);
  if (wv == 7) {
    u32 o = 0;
    if (lane == 0) o = __hip_atomic_fetch_add(cnt + (b & 7) * 64, 1u, __ATOMIC_RELAXED, __HIP_MEMORY_SCOPE_AGENT);
    o = __builtin_amdgcn_readfirstlane(o);
    if (o == 31u && lane < 8) __hip_atomic_store(cnt + (8 + lane) * 64 + (b & 7), 1u, __ATOMIC_RELAXED, __HIP_MEMORY_SCOPE_AGENT);
    for (int spins = 0; spins < (1 << 20); ++spins) {
      u32 v = 1u;
      if (lane < 8) v = __hip_atomic_load(cnt + (8 + (b & 7)) * 64 + lane, __ATOMIC_RELAXED, __HIP_MEMORY_SCOPE_AGENT);
      if (__all(v != 0u)) break;
      __builtin_amdgcn_s_sleep(1);
    }
  }
  __syncthreads();
  TS(1, 4);

  const int k0 = 7 * wv - (wv >> 2), nk = (wv & 3) == 3 ? 6 : 7;
  const u32 w2o = (u32)WS_W2F + (u32)((sl * 5) * 54 + k0) * 1024;
  bf16x8 wf[7][5];
  {
    const int l8 = tid & 7, crow = tid >> 3;
    u32x4 xv[12];
    f32x4 sb4;
    f32x4 acc[2][4];
#pragma unroll
    for (int fb = 0; fb < 2; ++fb)
#pragma unroll
      for (int tb = 0; tb < 4; ++tb) acc[fb][tb] = f32x4{0.f, 0.f, 0.f, 0.f};
    if (wv < 7) {
#pragma unroll
      for (int qq = 0; qq < 12; ++qq)
        xv[qq] = ld_sc1(rs, (qq < 11 || crow < 24 ? crow : 0) * (L_ * 2) + l8 * 16, (u32)WS_X1S + qq * (56 * L_ * 2) + tt * 128);
      __builtin_amdgcn_sched_barrier(0);
#pragma unroll
      for (int qq = 0; qq < 12; ++qq) {
        const int c = crow + 56 * qq;
        if (qq < 11 || crow < 24) *(u32x4*)(smem + M1_A + c * 128 + (((l8 >> 1) ^ h2_swz(c)) << 5) + ((l8 & 1) << 4)) = xv[qq];
      }
    } else {
      __builtin_amdgcn_s_setprio(3);
#pragma unroll
      for (int i = 0; i < 10; ++i) af[i >> 1][i & 1] = ld_frag1(rs, (lane >> 3) * (L_ * 2) + (lane & 7) * 16, (u32)WS_X1T + (sl * 80 + 8 * i) * (L_ * 2) + tt * 128);
    }
    __syncthreads();
    TS(1, 5);
    TC(3, 0);
    const int row0 = 8 * g + q, sw = h2_swz(row0);
    const char* rb0 = smem + M1_A + row0 * 128 + 8 * p;
    float* st = (float*)(smem + M1_ST);
    auto ldb = [&](int ks, bf16x8 (&bfr)[4]) {
#pragma unroll
      for (int tb = 0; tb < 4; ++tb) {
        const s16x4 lo = tr_read(rb0 + ks * 4096 + ((tb ^ sw) << 5));
        const s16x4 hi = tr_read(rb0 + ks * 4096 + 512 + ((tb ^ sw) << 5));
        bfr[tb] = cat8(lo, hi);
      }
    };
    bf16x8 bb[2][4];
    ldb(0, bb[0]);
#pragma unroll
    for (int ks = 0; ks < 20; ++ks) {
      if (active) {
        if (ks < 19) ldb(ks + 1, bb[(ks + 1) & 1]);
#pragma unroll
        for (int fb = 0; fb < 2; ++fb)
#pragma unroll
          for (int tb = 0; tb < 4; ++tb) acc[fb][tb] = mfma16(af[ks % W1_RING][fb], bb[ks & 1][tb], acc[fb][tb]);
        if (ks + W1_RING < 20) {
#pragma unroll
          for (int fb = 0; fb < 2; ++fb) af[ks % W1_RING][fb] = ld_frag(rs, l16, w1o + (fb * 20 + ks + W1_RING) * 1024);
        }
      } else if (ks == 0 && wv == 7) {
#pragma unroll
        for (int i = 0; i < 10; ++i) {
          const u32x4 v = __builtin_bit_cast(u32x4, af[i >> 1][i & 1]);
          const f32x4 a0 = {lo2f(v[0]), hi2f(v[0]), lo2f(v[1]), hi2f(v[1])}, a1 = {lo2f(v[2]), hi2f(v[2]), lo2f(v[3]), hi2f(v[3])};
          acc[0][0] += a0; acc[0][1] += a1; acc[0][2] += a0 * a0; acc[0][3] += a1 * a1;
        }
        const bool b5 = lane & 32, b4 = lane & 16, b3 = lane & 8;
        float v4[2][4], v2[2][2], v1[2];
#pragma unroll
        for (int h = 0; h < 2; ++h) {
#pragma unroll
          for (int j = 0; j < 4; ++j) {
            const float lo = acc[0][2 * h][j], hi = acc[0][2 * h + 1][j];
            v4[h][j] = (b5 ? hi : lo) + __shfl_xor(b5 ? lo : hi, 32);
          }
#pragma unroll
          for (int j = 0; j < 2; ++j) v2[h][j] = (b4 ? v4[h][2 + j] : v4[h][j]) + __shfl_xor(b4 ? v4[h][j] : v4[h][2 + j], 16);
          v1[h] = (b3 ? v2[h][1] : v2[h][0]) + __shfl_xor(b3 ? v2[h][0] : v2[h][1], 8);
        }
        const u32 toff = (8 * (lane & 7) + (lane >> 3)) * 8;
        __builtin_amdgcn_raw_buffer_store_b64(u32x2{__float_as_uint(v1[0]), __float_as_uint(v1[1]) | 0x80000000u}, rs, toff,
                                              (u32)WS_ST + (tt * 8 + sl) * 512, 16);
#pragma unroll
        for (int i = 0; i < 10; ++i) {
          const u32x4 v = __builtin_bit_cast(u32x4, af[i >> 1][i & 1]);
          char* dp = smem + M2_X + 8 * (lane & 7) * M2_XS + 2 * (8 * i + (lane >> 3));
#pragma unroll
          for (int k = 0; k < 4; ++k) {
            *(u16*)(dp + (2 * k) * M2_XS) = (u16)(v[k] & 0xffffu);
            *(u16*)(dp + (2 * k + 1) * M2_XS) = (u16)(v[k] >> 16);
          }
        }
        u32x2 gv[8];
        for (int spins = 0; spins < (1 << 20); ++spins) {
          u32 ok = 0x80000000u;
#pragma unroll
          for (int j = 0; j < 8; ++j) { gv[j] = __builtin_amdgcn_raw_buffer_load_b64(rs, lane * 8, (u32)WS_ST + (tt * 8 + j) * 512, 16); ok &= gv[j][1]; }
          if (__all(ok != 0u)) break;
          __builtin_amdgcn_s_sleep(2);
        }
        float sm = 0.f, sq = 0.f;
#pragma unroll
        for (int j = 0; j < 8; ++j) { sm += __uint_as_float(gv[j][0]); sq += __uint_as_float(gv[j][1] & 0x7fffffffu); }
        const float mean = sm * (1.f / D_);
        const float var = fmaxf(sq * (1.f / D_) - mean * mean, 0.f);
        st[lane] = rb(mean);
        st[64 + lane] = 1.f / rb(sqrtf(rb(rb(var) + 1e-5f)));
        __builtin_amdgcn_s_setprio(0);
      }
      if (ks >= 10) {
#pragma unroll
        for (int j = 0; j < 2; ++j) {
          const int f = 2 * (ks - 10) + j;
          wf[f / 5][f % 5] = LD_W2(rs, l16, w2o + ((f % 5) * 54 + f / 5) * 1024);
        }
      }
      if (ks == 9) {
        sb4 = __builtin_bit_cast(f32x4, ld_sc1(rs, ((lane >> 3) * F_ + 4 * (lane & 7)) * 4, (u32)WS_SB + 32 * p0 * 4));
      }
      if (ks == 17) {
#pragma unroll
        for (int e = 0; e < 4; ++e) { sb4[e] += __shfl_xor(sb4[e], 8); sb4[e] += __shfl_xor(sb4[e], 16); }
        if ((lane & 24) == 0) *(f32x4*)(smem + MN_SB + wv * 256 + (lane >> 5) * 128 + (lane & 7) * 16) = sb4;
      }
      __builtin_amdgcn_sched_barrier(0);
    }
    TS(2, 1);
    TC(3, 1);
#pragma unroll
    for (int i = W2_MID; i < 7; ++i)
#pragma unroll
      for (int cb = 0; cb < 5; ++cb) wf[i][cb] = LD_W2(rs, l16, w2o + (cb * 54 + (i < 6 || nk == 7 ? i : 5)) * 1024);
    __builtin_amdgcn_sched_barrier(0);
    __syncthreads();
    TS(2, 2);
    if (active) {
      const float* sbp = (const float*)(smem + MN_SB) + wv * 64 + 4 * g;
      const u32 hoff = (u32)WS_HID + (u32)((tt * 4) * 54 + p0) * 1024;
#pragma unroll
      for (int tb = 0; tb < 4; ++tb) {
        const float mb = st[tb * 16 + r16], rsd = st[64 + tb * 16 + r16];
        u32x4 v;
#pragma unroll
        for (int hh = 0; hh < 2; ++hh) {
          const f32x4 sv = *(const f32x4*)(sbp + 16 * hh), bv = *(const f32x4*)(sbp + 32 + 16 * hh);
          const f32x4 a = (acc[hh][tb] - mb * sv) * rsd + bv;
          v[2 * hh] = cvtpk(fmaxf(a[0], 0.f), fmaxf(a[1], 0.f));
          v[2 * hh + 1] = cvtpk(fmaxf(a[2], 0.f), fmaxf(a[3], 0.f));
        }
        st_sc1(rs, l16, hoff + tb * 54 * 1024, v);
      }
    }
  }
  TS(2, 3);
  asm volatile("s_waitcnt vmcnt(0)" ::: "memory");
  __syncthreads();
  TS(2, 4);
  if (tid == 448) __hip_atomic_store(cnt + (16 + tt * 8 + sl) * 64, 1u, __ATOMIC_RELAXED, __HIP_MEMORY_SCOPE_AGENT);
  __builtin_amdgcn_sched_barrier(0);
  poll_words<1>(cnt + (16 + tt * 8 + wv) * 64, 1u, lane);
  TS(1, 6);

  {
    const u32 hoff = (u32)WS_HID + (u32)((tt * 4) * 54 + k0) * 1024;
    u32x4 hf[2][4];
#pragma unroll
    for (int i = 0; i < 2; ++i)
#pragma unroll
      for (int tb = 0; tb < 4; ++tb) hf[i][tb] = ld_sc1(rs, l16, hoff + (tb * 54 + i) * 1024);
    f32x4 acc[5][4];
#pragma unroll
    for (int cb = 0; cb < 5; ++cb)
#pragma unroll
      for (int tb = 0; tb < 4; ++tb) acc[cb][tb] = f32x4{0.f, 0.f, 0.f, 0.f};
#pragma unroll
    for (int i = 0; i < 7; ++i) {
      if (i < 6 || nk == 7) {
#pragma unroll
        for (int tb = 0; tb < 4; ++tb)
#pragma unroll
          for (int cb = 0; cb < 5; ++cb) acc[cb][tb] = mfma16(wf[i][cb], __builtin_bit_cast(bf16x8, hf[i & 1][tb]), acc[cb][tb]);
      }
      __builtin_amdgcn_sched_barrier(0);
      if (i + 2 < 7) {
        const int kk = (i + 2 < 6 || nk == 7) ? i + 2 : 5;
#pragma unroll
        for (int tb = 0; tb < 4; ++tb) hf[i & 1][tb] = ld_sc1(rs, l16, hoff + (tb * 54 + kk) * 1024);
        __builtin_amdgcn_sched_barrier(0);
      }
    }
    TS(2, 5);
#pragma unroll
    for (int tb = 0; tb < 4; ++tb)
#pragma unroll
      for (int cb = 0; cb < 5; ++cb) {
        const int owner = tb + (cb < 3 ? 0 : 4);
        if (owner != wv) {
          const int slot = wv < owner ? wv : wv - 1;
          *(f32x4*)(smem + M2_RED + (((tb * 5 + cb) * 7 + slot) * 64 + lane) * 16) = acc[cb][tb];
        }
      }
    f32x4 own[3];
#pragma unroll
    for (int o = 0; o < 8; ++o) {
      if (wv == o) {
        asm volatile("" ::: "memory");
#pragma unroll
        for (int j = 0; j < 3; ++j) own[j] = acc[o < 4 ? j : (j < 2 ? 3 + j : 4)][o & 3];
      }
    }
    __syncthreads();
    TS(2, 6);
    const int tok = 16 * w4 + r16, cb0 = wv < 4 ? 0 : 3;
    f32x4 pr[3][7], gb4[3];
    u32x2 xr[3];
#pragma unroll
    for (int j = 0; j < 3; ++j) {
      const int cb = (j < 2 || wv < 4) ? cb0 + j : 4;
#pragma unroll
      for (int s7 = 0; s7 < 7; ++s7) pr[j][s7] = *(const f32x4*)(smem + M2_RED + (((w4 * 5 + cb) * 7 + s7) * 64 + lane) * 16);
      gb4[j] = *(const f32x4*)(smem + MN_GB + (16 * cb + 4 * g) * 4);
      xr[j] = *(const u32x2*)(smem + M2_X + tok * M2_XS + 2 * (16 * cb + 4 * g));
    }
    __builtin_amdgcn_sched_barrier(0);
#pragma unroll
    for (int j = 0; j < 3; ++j) {
      const f32x4 y = ((own[j] + pr[j][0]) + (pr[j][1] + pr[j][2])) + ((pr[j][3] + pr[j][4]) + (pr[j][5] + pr[j][6]));
      const u32 y0 = cvtpk(y[0], y[1]), y1 = cvtpk(y[2], y[3]);
      const u32 v0 = cvtpk(lo2f(y0) * gb4[j][0], hi2f(y0) * gb4[j][1]), v1 = cvtpk(lo2f(y1) * gb4[j][2], hi2f(y1) * gb4[j][3]);
      u32x2 o;
      o[0] = cvtpk(lo2f(xr[j][0]) + lo2f(v0), hi2f(xr[j][0]) + hi2f(v0));
      o[1] = cvtpk(lo2f(xr[j][1]) + lo2f(v1), hi2f(xr[j][1]) + hi2f(v1));
      if (j < 2 || wv < 4) *(u32x2*)(out + (size_t)(tt * 64 + tok) * D_ + sl * 80 + 16 * (cb0 + j) + 4 * g) = o;
    }
  }
  TS(1, 7);
}

extern "C" void kernel_launch(void* const* d_in, const int* in_sizes, int n_in, void* d_out, int out_size, void* d_ws,
                              size_t ws_size, hipStream_t stream) {
  char* ws = (char*)d_ws;
  k_pre<<<NB_LN + NB_KG + NB_MOD + NB_RL, 256, 0, stream>>>(
      (const u16*)d_in[0], (const u16*)d_in[1], (const u16*)d_in[2], (const u16*)d_in[3], (const u16*)d_in[4],
      (const u16*)d_in[5], (const u16*)d_in[6], (const u16*)d_in[7], (const u16*)d_in[10], (const u16*)d_in[11],
      (const u16*)d_in[12], (const u16*)d_in[13], ws);
  k_main<<<256, 512, 0, stream>>>((const u16*)d_in[8], (const u16*)d_in[11], ws, (u16*)d_out);
}
```

```cpp
#define PRETOUCH 2097152
#include <hip/hip_runtime.h>
#include <hip/hip_bf16.h>
#include <stdint.h>

typedef unsigned short u16;
typedef unsigned int u32;
typedef __attribute__((ext_vector_type(8))) short bf16x8;
typedef __attribute__((ext_vector_type(4))) short s16x4;
typedef __attribute__((ext_vector_type(4))) float f32x4;
typedef __attribute__((ext_vector_type(2))) float f32x2;
typedef __attribute__((ext_vector_type(4))) unsigned int u32x4;
typedef __attribute__((ext_vector_type(2))) unsigned int u32x2;
typedef __attribute__((ext_vector_type(3))) unsigned int u32x3;
typedef __bf16 bf16x2_t __attribute__((ext_vector_type(2)));
#define LDS_AS __attribute__((address_space(3)))
#define DI __device__ __forceinline__

constexpr int L_ = 2048, D_ = 640, N_ = 128, F_ = 1728;
constexpr int KSM = 4;

constexpr size_t WS_MODP = 0;
constexpr size_t WS_CNT = 126976;
constexpr size_t WS_NT = 262144;
constexpr size_t WS_XT = WS_NT + (size_t)D_ * L_ * 2;
constexpr size_t WS_X1T = WS_XT + (size_t)D_ * L_ * 2;
constexpr size_t WS_X1S = WS_X1T + (size_t)D_ * L_ * 2;
constexpr int KTAB = 1296;
constexpr size_t WS_KT = WS_X1S + (size_t)D_ * L_ * 2;
constexpr size_t WS_SB = WS_KT + (size_t)D_ * KTAB;
constexpr size_t WS_W1F = WS_SB + 2 * 20 * 1728 * 4;
constexpr size_t WS_W2F = WS_W1F + (size_t)108 * 20 * 1024;
constexpr size_t WS_HID = WS_W2F + (size_t)40 * 54 * 1024;
constexpr size_t WS_ST = WS_HID + (size_t)128 * 54 * 1024;

#define TS(kern, slot) do {} while (0)
#define TC(kern, slot) do {} while (0)

DI void st_wt(void* p, u32x4 v) { asm volatile("global_store_dwordx4 %0, %1, off sc1\n\ts_nop 1" ::"v"(p), "v"(v) : "memory"); }
#define ST_PRE(p, v) (*(p) = (v))
#define ST_PREW(p, v) (*(p) = (v))
DI u32 cvtpk(float lo, float hi) {
  f32x2 v = {lo, hi};
  bf16x2_t r = __builtin_convertvector(v, bf16x2_t);
  return __builtin_bit_cast(u32, r);
}
typedef _Float16 f16x2_t __attribute__((ext_vector_type(2)));
typedef _Float16 f16x8_t __attribute__((ext_vector_type(8)));
DI u32 cvtph(float lo, float hi) {
  f32x2 v = {lo, hi};
  f16x2_t r = __builtin_convertvector(v, f16x2_t);
  return __builtin_bit_cast(u32, r);
}
DI float dot2bf(u32 a, u32 b, float c) {
  return __builtin_amdgcn_fdot2_f32_bf16(__builtin_bit_cast(bf16x2_t, a), __builtin_bit_cast(bf16x2_t, b), c, false);
}
DI float lo2f(u32 v) { return __uint_as_float(v << 16); }
DI float hi2f(u32 v) { return __uint_as_float(v & 0xffff0000u); }
DI float bf2f(u16 v) { return __uint_as_float(((u32)v) << 16); }
DI float rb(float x) { return lo2f(cvtpk(x, x)); }
DI f32x4 mfma16(bf16x8 a, bf16x8 b, f32x4 c) { return __builtin_amdgcn_mfma_f32_16x16x32_bf16(a, b, c, 0, 0, 0); }
DI s16x4 tr_read(const char* p) {
  return __builtin_amdgcn_ds_read_tr16_b64_v4i16((LDS_AS s16x4*)p);
}
DI bf16x8 cat8(s16x4 a, s16x4 b) { return __builtin_shufflevector(a, b, 0, 1, 2, 3, 4, 5, 6, 7); }
DI void wave_sync() {
  __builtin_amdgcn_fence(__ATOMIC_RELEASE, "wavefront");
  __builtin_amdgcn_wave_barrier();
  __builtin_amdgcn_fence(__ATOMIC_ACQUIRE, "wavefront");
}

DI f32x2 cmul2(f32x2 a, f32x2 b) {
  const f32x2 bj = {-b.y, b.x};
  return __builtin_elementwise_fma(a.yy, bj, a.xx * b);
}
struct c32 { float r, i; };
DI c32 cmul(c32 a, c32 b) { return {a.r * b.r - a.i * b.i, a.r * b.i + a.i * b.r}; }
DI c32 cexp_scaled(float ar, float ai, float e) {
  const float mag = __expf(ar * e);
  const float x = ai * e;
  const float C_HI = 0.15915494f, C_LO = 6.4206382e-9f;
  const float r = x * C_HI;
  const float rl = fmaf(x, C_HI, -r) + x * C_LO;
  const float fr = (r - floorf(r)) + rl;
  return {mag * __builtin_amdgcn_cosf(fr), mag * __builtin_amdgcn_sinf(fr)};
}
DI int tap_index(int m) {
  u32 bits = __float_as_uint((float)m);
  bits += 0x7fffu + ((bits >> 16) & 1u);
  const int id2 = (int)(bits >> 16) - (0x4300 - 128);
  return m < 128 ? m : id2;
}

constexpr int NB_LN = 64, NB_KG = 320, NB_MOD = 30 * KSM;
constexpr int NB_RL = 70;
constexpr int PRE_SMEM = 43008;

DI void role_ln1(int b, const u16* __restrict__ x, u32* __restrict__ st1, u16* __restrict__ xT, char* smem) {
  const int tid = threadIdx.x, u = tid >> 4, j = tid & 15;
  const int t0 = b * 32;
  const u32x4* xa = (const u32x4*)(x + (size_t)(t0 + 2 * u) * D_);
  u32x4 A[5], B[5];
#pragma unroll
  for (int q = 0; q < 5; ++q) { A[q] = xa[j + 16 * q]; B[q] = xa[80 + j + 16 * q]; }
  float sa = 0.f, sb = 0.f, qa = 0.f, qb = 0.f;
#pragma unroll
  for (int q = 0; q < 5; ++q)
#pragma unroll
    for (int k = 0; k < 4; ++k) {
      sa = dot2bf(A[q][k], 0x3f803f80u, sa); qa = dot2bf(A[q][k], A[q][k], qa);
      sb = dot2bf(B[q][k], 0x3f803f80u, sb); qb = dot2bf(B[q][k], B[q][k], qb);
    }
#pragma unroll
  for (int o = 1; o < 16; o <<= 1) {
    sa += __shfl_xor(sa, o, 16); sb += __shfl_xor(sb, o, 16); qa += __shfl_xor(qa, o, 16); qb += __shfl_xor(qb, o, 16);
  }
  const float ma = sa * (1.f / D_), mb = sb * (1.f / D_);
  const float va = fmaxf(qa - sa * ma, 0.f), vb = fmaxf(qb - sb * mb, 0.f);
  if (j == 0) {
    const u32 s0 = cvtpk(ma, sqrtf(rb(rb(va * (1.f / D_)) + 1e-5f))), s1 = cvtpk(mb, sqrtf(rb(rb(vb * (1.f / D_)) + 1e-5f)));
    *(u32x2*)(st1 + t0 + 2 * u) = u32x2{s0, s1};
  }
  char* wbase = smem + (8 * j + (j >> 2)) * 64 + ((((u >> 2) ^ (j & 3)) << 2) + (u & 3)) * 4;
#pragma unroll
  for (int q = 0; q < 5; ++q)
#pragma unroll
    for (int k = 0; k < 4; ++k) {
      *(u32*)(wbase + (132 * q + 2 * k) * 64) = (A[q][k] & 0xffffu) | (B[q][k] << 16);
      *(u32*)(wbase + (132 * q + 2 * k + 1) * 64) = (A[q][k] >> 16) | (B[q][k] & 0xffff0000u);
    }
  __syncthreads();
#pragma unroll
  for (int i = 0; i < 10; ++i) {
    const int id = tid + 256 * i, c = id >> 2, o = id & 3;
    const u32x4 v = *(const u32x4*)(smem + (c + (c >> 5)) * 64 + ((o ^ ((c >> 3) & 3)) << 4));
    ST_PRE((u32x4*)(xT + (size_t)c * L_ + t0 + 8 * o), v);
  }
}

DI void role_kgen(int bk, const u16* __restrict__ Are, const u16* __restrict__ Aim, const u16* __restrict__ Bre,
                  const u16* __restrict__ Bim, const u16* __restrict__ Cre, const u16* __restrict__ Cim,
                  const u16* __restrict__ ldt, char* __restrict__ KT, char* smem) {
  const int tid = threadIdx.x, w = tid >> 6, lane = tid & 63, r16 = lane & 15, g = lane >> 4;
  const int d = 2 * bk + (w >> 1), n = (w & 1) * 64 + lane;
  const int gi = d * N_ + n;
  const float dt = rb(expf(bf2f(ldt[d])));
  const float ar = dt * bf2f(Are[gi]), ai = dt * bf2f(Aim[gi]);
  const c32 dB = {dt * bf2f(Bre[gi]), dt * bf2f(Bim[gi])};
  const c32 Cc = {bf2f(Cre[gi]), bf2f(Cim[gi])};
  const c32 W = cmul(Cc, dB);
  const c32 E1 = cexp_scaled(ar, ai, 1.f), E8 = cexp_scaled(ar, ai, 8.f), E128 = cexp_scaled(ar, ai, 128.f);

  float wmax = fmaxf(fabsf(W.r), fabsf(W.i));
#pragma unroll
  for (int o = 1; o < 64; o <<= 1) wmax = fmaxf(wmax, __shfl_xor(wmax, o));
  int ew = (int)((__float_as_uint(wmax) >> 23) & 0xff);
  ew = ew < 20 ? 20 : (ew > 240 ? 240 : ew);
  const float wsc = __uint_as_float((u32)(127 + 10 + 127 - ew - 1) << 23);
  const float unsc = __uint_as_float((u32)(ew + 1 - 20) << 23);

  char* buf = smem + w * 8704;
  char* wr = buf + lane * 4;
  const char* rd = buf + r16 * 272 + g * 16;
  auto put = [&](int row, f32x2 v) { *(u32*)(wr + row * 272) = cvtph(v.x, v.y); };
  {
    const f32x2 e8 = {E8.r, E8.i};
    f32x2 p = {1024.f, 0.f};
#pragma unroll
    for (int jj = 0; jj < 16; ++jj) { put(jj, p); if (jj < 15) p = cmul2(p, e8); }
  }
  wave_sync();
  f16x8_t bh[4];
#pragma unroll
  for (int ks = 0; ks < 4; ++ks) bh[ks] = *(const f16x8_t*)(rd + ks * 64);
  wave_sync();
  const f32x2 e1 = {E1.r, -E1.i}, e128 = {E128.r, -E128.i};
  f32x2 sm[8];
  sm[1] = e1;
#pragma unroll
  for (int i = 2; i < 8; ++i) sm[i] = cmul2(sm[i - 1], e1);
  f32x2 WG[17];
  WG[0] = f32x2{W.r * wsc, -W.i * wsc};
#pragma unroll
  for (int i = 1; i < 17; ++i) WG[i] = cmul2(WG[i - 1], e128);
  f32x4 acc[3];
  auto tile_mma = [&](int t) {
    wave_sync();
    f32x4 a = {0.f, 0.f, 0.f, 0.f};
#pragma unroll
    for (int ks = 0; ks < 4; ++ks) a = __builtin_amdgcn_mfma_f32_16x16x32_f16(*(const f16x8_t*)(rd + ks * 64), bh[ks], a, 0, 0, 0);
    acc[t] = a * unsc;
    wave_sync();
  };
#pragma unroll
  for (int i = 0; i < 2; ++i)
#pragma unroll
    for (int rho = 0; rho < 8; ++rho) put(8 * i + rho, rho ? cmul2(sm[rho], WG[i]) : WG[i]);
  tile_mma(0);
#pragma unroll
  for (int i = 0; i < 2; ++i)
#pragma unroll
    for (int rho = 0; rho < 4; ++rho) put(4 * i + rho, rho ? cmul2(sm[2 * rho], WG[2 + i]) : WG[2 + i]);
#pragma unroll
  for (int i = 0; i < 4; ++i)
#pragma unroll
    for (int rho = 0; rho < 2; ++rho) put(8 + 2 * i + rho, rho ? cmul2(sm[4], WG[4 + i]) : WG[4 + i]);
  tile_mma(1);
#pragma unroll
  for (int i = 0; i < 9; ++i) put(i, WG[8 + i]);
  tile_mma(2);

#pragma unroll
  for (int t = 0; t < 3; ++t) *(f32x4*)(buf + t * 1024 + lane * 16) = acc[t];
  __syncthreads();
  u16* T = (u16*)(smem + (w >> 1) * 2 * 8704 + 4352);
  {
    const char* ob = smem + (w ^ 1) * 8704 + lane * 16;
    if (!(w & 1)) {
      const f32x4 a0 = acc[0] + *(const f32x4*)(ob), a2 = acc[2] + *(const f32x4*)(ob + 2048);
      u16* t0 = T + 128 * (g >> 1) + 4 * (g & 1) + 8 * r16;
      *(u32x2*)t0 = u32x2{cvtpk(a0[0], a0[1]), cvtpk(a0[2], a0[3])};
      if (g < 2) {
#pragma unroll
        for (int rg = 0; rg < 4; ++rg) T[512 + 16 * (4 * g + rg) + r16] = (u16)cvtpk(a2[rg], 0.f);
      } else if (lane == 32) {
        T[640] = (u16)cvtpk(a2[0], 0.f);
      } else if (lane >= 48 && lane < 55) {
        T[641 + lane - 48] = 0;
      }
    } else {
      const f32x4 a1 = acc[1] + *(const f32x4*)(ob + 1024);
      if (g < 2) {
        *(u32x2*)(T + 256 + 64 * g + 4 * r16) = u32x2{cvtpk(a1[0], a1[1]), cvtpk(a1[2], a1[3])};
      } else {
        *(u32*)(T + 384 + 64 * (g - 2) + 2 * r16) = cvtpk(a1[0], a1[1]);
        *(u32*)(T + 384 + 64 * (g - 2) + 32 + 2 * r16) = cvtpk(a1[2], a1[3]);
      }
    }
  }
  __syncthreads();
  const int t128 = tid & 127;
  if (t128 < 81) *(u32x4*)(KT + (size_t)d * KTAB + t128 * 16) = *(const u32x4*)((const char*)T + t128 * 16);
}

DI void role_mod(int bm, const u16* __restrict__ c, const u16* __restrict__ wm, float* __restrict__ modp, char* smem) {
  const int tid = threadIdx.x, cj = tid & 15, rg = tid >> 4, w = tid >> 6;
  const int cbk = bm % 30, ks = bm / 30;
  constexpr int NR = 640 / KSM / 16;
  u32x4 v[NR];
  u16 cr[NR];
#pragma unroll
  for (int i = 0; i < NR; ++i) {
    const int row = (640 / KSM) * ks + rg + 16 * i;
    v[i] = *(const u32x4*)(wm + (size_t)row * (6 * D_) + 128 * cbk + 8 * cj);
    cr[i] = c[row];
  }
  float a[8];
#pragma unroll
  for (int k = 0; k < 8; ++k) a[k] = 0.f;
#pragma unroll
  for (int i = 0; i < NR; ++i) {
    const float cv = bf2f(cr[i]);
    const float sv = rb(cv / (1.f + __expf(-cv)));
#pragma unroll
    for (int k = 0; k < 4; ++k) { a[2 * k] += sv * lo2f(v[i][k]); a[2 * k + 1] += sv * hi2f(v[i][k]); }
  }
#pragma unroll
  for (int k = 0; k < 8; ++k) { a[k] += __shfl_xor(a[k], 16); a[k] += __shfl_xor(a[k], 32); }
  float* red = (float*)smem;
  if ((tid & 63) < 16) {
#pragma unroll
    for (int k = 0; k < 8; ++k) red[w * 128 + 8 * cj + k] = a[k];
  }
  __syncthreads();
  if (tid < 128) modp[ks * (6 * D_) + 128 * cbk + tid] = red[tid] + red[128 + tid] + red[256 + tid] + red[384 + tid];
}

DI int rl_swz(int row) { return (row & 3) | ((((row >> 2) ^ (row >> 3)) & 1) << 2); }

DI void role_relayout(int br, const u16* __restrict__ w1, const u16* __restrict__ w2, char* __restrict__ ws, char* smem) {
  const int tid = threadIdx.x, w = tid >> 6, lane = tid & 63;
  const int tile = br * 4 + w;
  if (tile >= 550) return;
  const bool is1 = tile < 280;
  int ks, nb, ld, nblk;
  const u16* src;
  if (is1) { ks = tile / 14; nb = tile % 14; ld = F_; src = w1; nblk = nb == 13 ? 4 : 8; }
  else { const int t2 = tile - 280; ks = t2 / 5; nb = t2 % 5; ld = D_; src = w2; nblk = 8; }
  char* buf = smem + w * 8192;
  const int lr = lane >> 4, lc = lane & 15;
  u32x4 v[8];
#pragma unroll
  for (int i = 0; i < 8; ++i) v[i] = *(const u32x4*)(src + (size_t)(32 * ks + 4 * i + lr) * ld + 128 * nb + 8 * (lc < 2 * nblk ? lc : 0));
#pragma unroll
  for (int i = 0; i < 8; ++i) {
    const int row = 4 * i + lr;
    *(u32x4*)(buf + row * 256 + (((lc >> 1) ^ rl_swz(row)) << 5) + ((lc & 1) << 4)) = v[i];
  }
  wave_sync();
  const int g = lane >> 4, q = (lane & 15) >> 2, p = lane & 3;
  const int row0 = is1 ? 8 * g + q : 4 * g + q, row1 = is1 ? row0 + 4 : row0 + 16;
  char* dst = is1 ? ws + WS_W1F + ((size_t)(8 * nb) * 20 + ks) * 1024 : ws + WS_W2F + ((size_t)(8 * nb) * 54 + ks) * 1024;
  const int dstride = is1 ? 20 * 1024 : 54 * 1024;
#pragma unroll
  for (int cb = 0; cb < 8; ++cb) {
    const s16x4 lo = tr_read(buf + row0 * 256 + ((cb ^ rl_swz(row0)) << 5) + 8 * p);
    const s16x4 hi = tr_read(buf + row1 * 256 + ((cb ^ rl_swz(row1)) << 5) + 8 * p);
    if (cb < nblk) ST_PREW((u32x4*)(dst + (size_t)cb * dstride + lane * 16), __builtin_bit_cast(u32x4, cat8(lo, hi)));
  }
}

__global__ __launch_bounds__(256) void k_pre(const u16* __restrict__ x, const u16* __restrict__ Are, const u16* __restrict__ Aim,
                                             const u16* __restrict__ Bre, const u16* __restrict__ Bim, const u16* __restrict__ Cre,
                                             const u16* __restrict__ Cim, const u16* __restrict__ ldt, const u16* __restrict__ w1,
                                             const u16* __restrict__ w2, const u16* __restrict__ c, const u16* __restrict__ wm,
                                             char* __restrict__ ws) {
  __shared__ __attribute__((aligned(16))) char smem[PRE_SMEM];
  int b = blockIdx.x;
  b = b < NB_KG ? b + NB_LN : (b < NB_KG + NB_LN ? b - NB_KG : b);
  TS(0, 0);
  if (b < NB_LN) {
    if (b < 34 && threadIdx.x < 64) ((u32*)(ws + WS_CNT))[(b * 8 + (threadIdx.x >> 3)) * 64 + (threadIdx.x & 7)] = 0u;
    *(u32x2*)(ws + WS_ST + (b * 256 + threadIdx.x) * 8) = u32x2{0u, 0u};
    role_ln1(b, x, (u32*)(ws + WS_NT), (u16*)(ws + WS_XT), smem); TS(0, 7); return;
  }
  b -= NB_LN;
  if (b < NB_KG) { role_kgen(b, Are, Aim, Bre, Bim, Cre, Cim, ldt, ws + WS_KT, smem); TS(0, 7); return; }
  b -= NB_KG;
  if (b < NB_MOD) { role_mod(b, c, wm, (float*)(ws + WS_MODP), smem); TS(0, 7); return; }
  b -= NB_MOD;
  role_relayout(b, w1, w2, ws, smem);
  TS(0, 7);
}

constexpr int CV_H0 = 0, CV_HS = 4160, CV_K = 12544, CV_T = 21760, CV_H3 = 32768 + 192, CV_CH = 37376;
static_assert(CV_CH % 256 == 0 && CV_H3 + 4160 <= CV_CH && CV_T + 4 * KTAB <= 32768, "conv lds");
constexpr int M1_A = 0, M1_ST = 81920;
constexpr int MN_SB = 3 * CV_CH;
constexpr int MN_R1 = MN_SB + 2048;
constexpr int M2_RED = 0, M2_X = 143360, M2_XS = 176;
constexpr int MN_GB = M2_X + 64 * M2_XS;
constexpr int MN_SMEM = MN_GB + 320;
static_assert(MN_R1 + 5 * 4864 <= M2_X && M1_ST + 512 <= MN_SB, "mlp lds");

typedef __amdgpu_buffer_rsrc_t rsrc_t;
DI u32x4 ld_sc1(rsrc_t r, u32 voff, u32 soff) { return __builtin_amdgcn_raw_buffer_load_b128(r, voff, soff, 16); }
DI void st_sc1(rsrc_t r, u32 voff, u32 soff, u32x4 v) { __builtin_amdgcn_raw_buffer_store_b128(v, r, voff + soff, 0, 16); }
DI bf16x8 ld_frag1(rsrc_t r, u32 voff, u32 soff) { return __builtin_bit_cast(bf16x8, __builtin_amdgcn_raw_buffer_load_b128(r, voff, soff, 16)); }
DI bf16x8 ld_frag(rsrc_t r, u32 voff, u32 soff) { return __builtin_bit_cast(bf16x8, __builtin_amdgcn_raw_buffer_load_b128(r, voff, soff, 0)); }

DI int h2_swz(int k) { return ((k >> 1) & 1) | (((k >> 3) & 1) << 1); }

DI void conv_stage_k(char* smem, u32x4 tv0, u32x4 tv1, int w, int lane, int tid) {
  char* tb = smem + CV_T + w * KTAB;
  *(u32x4*)(tb + lane * 16) = tv0;
  if (lane < 17) *(u32x4*)(tb + (64 + lane) * 16) = tv1;
  wave_sync();
  const u16* T = (const u16*)tb;
#pragma unroll
  for (int i = 0; i < 2; ++i) {
    const int cp = tid + 256 * i;
    if (cp < 284) {
      u32 e[9];
#pragma unroll
      for (int j = 0; j < 9; ++j) {
        const int m = 2047 - 8 * cp - j;
        e[j] = T[m < 0 ? 641 : tap_index(m)];
      }
      *(u32x4*)(smem + CV_K + 4576 + cp * 16) = u32x4{e[0] | (e[1] << 16), e[2] | (e[3] << 16), e[4] | (e[5] << 16), e[6] | (e[7] << 16)};
      *(u32x4*)(smem + CV_K + cp * 16) = u32x4{e[1] | (e[2] << 16), e[3] | (e[4] << 16), e[5] | (e[6] << 16), e[7] | (e[8] << 16)};
    }
  }
}

DI void conv_stage_h(char* smem, u32x4 xq0, u32x4 xq1, const float (&mn)[16], const float (&rs)[16], float ca1, float sa, int tid) {
  u32 h[8];
#pragma unroll
  for (int k = 1; k < 8; ++k) {
    const u32 xv = k < 4 ? xq0[k] : xq1[k - 4];
    const u32 c = cvtpk(lo2f(xv) - mn[2 * k], hi2f(xv) - mn[2 * k + 1]);
    const u32 n = cvtpk(lo2f(c) * rs[2 * k], hi2f(c) * rs[2 * k + 1]);
    const u32 p = cvtpk(lo2f(n) * ca1, hi2f(n) * ca1);
    const u32 hv = cvtpk(lo2f(p) + sa, hi2f(p) + sa);
    h[k] = (k >= 4 || tid > 0) ? hv : 0u;
  }
  const int yo = (tid + 3) * 16;
  *(u32x4*)(smem + CV_H0 + yo) = u32x4{h[1], h[2], h[3], h[4]};
  *(u32x4*)(smem + CV_H0 + CV_HS + yo) = u32x4{h[2], h[3], h[4], h[5]};
  *(u32x4*)(smem + CV_H0 + 2 * CV_HS + yo) = u32x4{h[3], h[4], h[5], h[6]};
  *(u32x4*)(smem + CV_H3 + yo) = u32x4{h[4], h[5], h[6], h[7]};
  if (tid < 12) {
    const int cc = tid / 3, yc = tid % 3;
    *(u32x4*)(smem + (cc == 3 ? CV_H3 : CV_H0 + cc * CV_HS) + yc * 16) = u32x4{0u, 0u, 0u, 0u};
  }
}

template <class CB>
DI void conv_mma(const char* smem, int w, int lane, f32x4 (&acc)[8], CB&& side) {
  const int r16 = lane & 15, g = lane >> 4, cpy = r16 & 3;
  const char* hb = smem + (cpy == 3 ? CV_H3 : CV_H0 + cpy * CV_HS) + 16 * ((r16 >> 2) + 4 * w + g);
  const char* kb = smem + CV_K + (r16 & 1) * 4576 + 16 * (252 + 4 * w - 4 * (r16 >> 1) + g) - 256 * 14;
#pragma unroll
  for (int i = 0; i < 8; ++i) acc[i] = f32x4{0.f, 0.f, 0.f, 0.f};
#pragma unroll
  for (int par = 1; par >= 0; --par) {
    bf16x8 Bf[8];
#pragma unroll
    for (int t = 0; t < 8; ++t) Bf[t] = *(const bf16x8*)(hb + 256 * (2 * t + par));
#pragma unroll
    for (int sg = -par; sg <= 14; sg += 2) {
      const bf16x8 Af = *(const bf16x8*)(kb + 256 * (14 - sg));
#pragma unroll
      for (int i = 0; i < 8; ++i) {
        const int tau = 2 * i - sg;
        if (tau >= 0 && tau <= 15) acc[i] = mfma16(Af, Bf[tau >> 1], acc[i]);
      }
      side((1 - par) * 8 + ((sg + par) >> 1));
    }
  }
}

DI void conv_put(char* smem, int w, int lane, const f32x4 (&acc)[8]) {
  char* pb = smem + w * 8192 + (64 * (lane >> 4) + 2 * (lane & 15)) * 4;
#pragma unroll
  for (int i = 0; i < 8; ++i) {
    *(f32x2*)(pb + i * 1024) = f32x2{acc[i][0], acc[i][1]};
    *(f32x2*)(pb + i * 1024 + 128) = f32x2{acc[i][2], acc[i][3]};
  }
}

DI u32x4 conv_fin(const char* smem, int t, u32x4 xv, float dsk, float ga, float c1, u32x4& os) {
  f32x4 y0 = *(const f32x4*)(smem + t * 32), y1 = *(const f32x4*)(smem + t * 32 + 16);
#pragma unroll
  for (int w = 1; w < 4; ++w) { y0 += *(const f32x4*)(smem + w * 8192 + t * 32); y1 += *(const f32x4*)(smem + w * 8192 + t * 32 + 16); }
  const u32x4 hv = *(const u32x4*)(smem + CV_H3 + 48 + t * 16);
  u32x4 o;
#pragma unroll
  for (int k = 0; k < 4; ++k) {
    const u32 yr = k < 2 ? cvtpk(y0[2 * k], y0[2 * k + 1]) : cvtpk(y1[2 * k - 4], y1[2 * k - 3]);
    const u32 hd = cvtpk(lo2f(hv[k]) * dsk, hi2f(hv[k]) * dsk);
    const u32 v = cvtpk(lo2f(yr) + lo2f(hd), hi2f(yr) + hi2f(hd));
    const u32 gv = cvtpk(lo2f(v) * ga, hi2f(v) * ga);
    o[k] = cvtpk(lo2f(xv[k]) + lo2f(gv), hi2f(xv[k]) + hi2f(gv));
    os[k] = cvtpk(lo2f(o[k]) * c1, hi2f(o[k]) * c1);
  }
  return o;
}

template <int NW>
DI void poll_words(const u32* cnt, u32 target, int lane) {
  for (int spins = 0; spins < (1 << 20); ++spins) {
    u32 v = target;
    if (lane < NW) v = __hip_atomic_load(cnt + lane * 64, __ATOMIC_RELAXED, __HIP_MEMORY_SCOPE_AGENT);
    if (__all(v >= target)) break;
    __builtin_amdgcn_s_sleep(1);
  }
}

#define LD_W2 ld_frag1
constexpr int W1_RING = 10;
constexpr int W2_MID = 4;

__global__ __launch_bounds__(512, 2) void k_main(const u16* __restrict__ Dsk, const u16* __restrict__ w2, char* __restrict__ ws,
                                                    u16* __restrict__ out) {
  __shared__ __attribute__((aligned(256))) char smem[MN_SMEM];
  const int tid = threadIdx.x, wv = __builtin_amdgcn_readfirstlane(tid >> 6), lane = tid & 63;
  const int r16 = lane & 15, g = lane >> 4, q = r16 >> 2, p = lane & 3;
  const int b = blockIdx.x, hw = wv >> 2, w4 = wv & 3, t256 = tid & 255;
  const rsrc_t rs = __builtin_amdgcn_make_buffer_rsrc((void*)ws, 0, 0x7fffffff, 0x00020000);
  u32* cnt = (u32*)(ws + WS_CNT);
  const int idx = b >> 3, tt = ((b & 7) >> 1) * 8 + (idx & 7), sl = (b & 1) * 4 + (idx >> 3);
  const int pbase = 7 * sl - (sl >> 2), pcnt = (sl & 3) == 3 ? 6 : 7;
  const bool active = wv < pcnt;
  const int p0 = pbase + (active ? wv : 0);
  TS(1, 0);
  u32 touch;
  {
    constexpr u32 STEP = PRETOUCH;
    const u32 nws = (u32)((WS_ST + 131072 + STEP - 1) / STEP);
    const u32 id = (u32)tid;
    const char* tp = id < nws ? ws + (size_t)id * STEP : (const char*)out + (size_t)((id - nws) * STEP < 2621440u ? (id - nws) * STEP : 0);
    touch = *(const volatile u32*)tp;
  }

  const bool three = b < 128;
  const int d0 = three ? 3 * b : 384 + 2 * (b - 128);
  const int dA = d0 + hw, dC = three ? d0 + 2 : d0;
  bf16x8 af[W1_RING][2];
  const u32 l16 = lane * 16;
  const u32 w1o = (u32)WS_W1F + (u32)(2 * p0) * 20 * 1024;
  {
    const u16* xT = (const u16*)(ws + WS_XT);
    const float* modp = (const float*)(ws + WS_MODP);
    const char* ktA = ws + WS_KT + (size_t)dA * KTAB;
    const u32x4 tA0 = *(const u32x4*)(ktA + lane * 16), tA1 = *(const u32x4*)(ktA + (lane < 17 ? 64 + lane : 0) * 16);
    const int mj = (lane & 15) >> 2;
    float mp = modp[(lane & 3) * (6 * D_) + (mj < 3 ? mj : 4) * D_ + ((lane & 16) ? dC : dA)];
    const int tm1 = t256 > 0 ? t256 - 1 : 0;
    const u32x4 xA0 = *(const u32x4*)(xT + (size_t)dA * L_ + 8 * tm1);
    const u32x4 xA = *(const u32x4*)(xT + (size_t)dA * L_ + 8 * t256);
    u32x4 sq[4];
#pragma unroll
    for (int i = 0; i < 4; ++i) sq[i] = *(const u32x4*)(ws + WS_NT + (i < 2 ? tm1 : t256) * 32 + (i & 1) * 16);
    const char* pc0 = hw == 0 ? ws + WS_KT + (size_t)dC * KTAB + lane * 16 : (const char*)(xT + (size_t)dC * L_ + 8 * tm1);
    const char* pc1 = hw == 0 ? ws + WS_KT + (size_t)dC * KTAB + (lane < 17 ? 64 + lane : 0) * 16 : (const char*)(xT + (size_t)dC * L_ + 8 * t256);
    const u32x4 c0 = *(const u32x4*)pc0, c1 = *(const u32x4*)pc1;
    const u16 dskAr = Dsk[dA], dskCr = Dsk[dC];
    __builtin_amdgcn_sched_barrier(0);
    asm volatile("" ::"v"(xA0), "v"(c0), "v"(sq[0]));
    TS(3, 2);
    const float dskA = bf2f(dskAr), dskC = bf2f(dskCr);
    char* smA = smem + hw * CV_CH;
    char* smC = smem + 2 * CV_CH;
    conv_stage_k(smA, tA0, tA1, w4, lane, t256);
    if (three && hw == 0) conv_stage_k(smC, c0, c1, w4, lane, t256);
    mp += __shfl_xor(mp, 1); mp += __shfl_xor(mp, 2);
    mp = rb(mp);
    auto rl = [&](int l) { return __uint_as_float(__builtin_amdgcn_readlane(__float_as_uint(mp), l)); };
    const float saA = rl(0), caA = rl(4), gaA = rl(8), c1A = rb(1.f + rl(12));
    const float saC = rl(16), caC = rl(20), gaC = rl(24), c1C = rb(1.f + rl(28));
    float mn[16], rsd[16];
#pragma unroll
    for (int i = 2; i < 16; ++i) { mn[i] = lo2f(sq[i >> 2][i & 3]); rsd[i] = __builtin_amdgcn_rcpf(hi2f(sq[i >> 2][i & 3])); }
    mn[0] = mn[1] = rsd[0] = rsd[1] = 0.f;
    conv_stage_h(smA, xA0, xA, mn, rsd, rb(1.f + caA), saA, t256);
    if (three && hw == 1) conv_stage_h(smC, c0, c1, mn, rsd, rb(1.f + caC), saC, t256);
    __builtin_amdgcn_sched_barrier(0);
    TS(3, 3);
    const int t1 = (b - 128) * 4 + wv;
    const bool do1 = !three && wv < 4 && t1 < 432;
    u32x4 rv[5];
    f32x4 accA[8], accC[8];
    if (do1) {
#pragma unroll
      for (int i = 0; i < 5; ++i) rv[i] = __builtin_bit_cast(u32x4, ld_frag(rs, l16, (u32)WS_W1F + (t1 * 5 + i) * 1024));
      const float* mq = modp + 160 * (t1 & 3) + 4 * (lane < 40 ? lane : 0);
#pragma unroll
      for (int k = 0; k < KSM; ++k) { accC[k] = *(const f32x4*)(mq + k * (6 * D_) + 4 * D_); accC[4 + k] = *(const f32x4*)(mq + k * (6 * D_) + 3 * D_); }
    }
    const int t2 = (b - 128) * 3 + wv - 4;
    const bool do2 = !three && wv >= 4 && wv < 7 && t2 < 270;
    if (do2) {
#pragma unroll
      for (int i = 0; i < 8; ++i) accC[i] = *(const f32x4*)(w2 + (size_t)(32 * (t2 / 5) + 4 * i + g) * D_ + 128 * (t2 % 5) + 8 * r16);
    }
    f32x4 gbp;
#pragma unroll
    for (int k = 0; k < KSM; ++k) gbp[k] = modp[k * (6 * D_) + 5 * D_ + sl * 80 + (tid < 80 ? tid : 0)];
    __builtin_amdgcn_sched_barrier(0);
    __syncthreads();
    TS(1, 1);
    conv_mma(smA, w4, lane, accA, [&](int it) {
      if (it < 5 || (it >= 8 && it < 13)) {
        const int ks = it < 5 ? it : it - 3;
#pragma unroll
        for (int fb = 0; fb < 2; ++fb) af[ks][fb] = ld_frag(rs, l16, w1o + (fb * 20 + ks) * 1024);
      }
    });
    if (three && hw == 0) conv_mma(smC, w4, lane, accC, [](int) {});
    if (tid < 80) ((float*)(smem + MN_GB))[tid] = rb((gbp[0] + gbp[1]) + (gbp[2] + gbp[3]));
    if (do2) {
      char* buf = smC + 4096 + (wv - 4) * 8192;
#pragma unroll
      for (int i = 0; i < 8; ++i) {
        const int row = 4 * i + g;
        *(f32x4*)(buf + row * 256 + (((r16 >> 1) ^ rl_swz(row)) << 5) + ((r16 & 1) << 4)) = accC[i];
      }
      wave_sync();
      const int row0 = 4 * g + q, row1 = row0 + 16;
#pragma unroll
      for (int cb = 0; cb < 8; ++cb) {
        const s16x4 lo = tr_read(buf + row0 * 256 + ((cb ^ rl_swz(row0)) << 5) + 8 * p);
        const s16x4 hi = tr_read(buf + row1 * 256 + ((cb ^ rl_swz(row1)) << 5) + 8 * p);
        st_sc1(rs, l16, (u32)WS_W2F + ((8 * (t2 % 5) + cb) * 54 + t2 / 5) * 1024, __builtin_bit_cast(u32x4, cat8(lo, hi)));
      }
    }
    if (do1) {
      char* buf = smC + wv * 1024;
      const f32x4 mc = (accC[0] + accC[1]) + (accC[2] + accC[3]), ms = (accC[4] + accC[5]) + (accC[6] + accC[7]);
      if (lane < 40) {
        *(u32x2*)(buf + lane * 8) = u32x2{cvtpk(1.f + rb(mc[0]), 1.f + rb(mc[1])), cvtpk(1.f + rb(mc[2]), 1.f + rb(mc[3]))};
        *(u32x2*)(buf + 512 + lane * 8) = u32x2{cvtpk(ms[0], ms[1]), cvtpk(ms[2], ms[3])};
      }
      wave_sync();
      float S = 0.f, B = 0.f;
#pragma unroll
      for (int i = 0; i < 5; ++i) {
        const u32x4 c1p = *(const u32x4*)(buf + i * 64 + g * 16), sbp = *(const u32x4*)(buf + 512 + i * 64 + g * 16);
#pragma unroll
        for (int k = 0; k < 4; ++k) { S = dot2bf(rv[i][k], c1p[k], S); B = dot2bf(rv[i][k], sbp[k], B); }
      }
      S += __shfl_xor(S, 16); S += __shfl_xor(S, 32);
      B += __shfl_xor(B, 16); B += __shfl_xor(B, 32);
      if (lane < 32)
        __builtin_amdgcn_raw_buffer_store_b32(__float_as_uint(lane < 16 ? S : B), rs, (g * 4 * F_ + r16) * 4, (u32)WS_SB + ((t1 & 3) * F_ + 16 * (t1 >> 2)) * 4, 16);
    }
    __syncthreads();
    TS(1, 2);
    conv_put(smA, w4, lane, accA);
    if (three && hw == 0) conv_put(smC, w4, lane, accC);
    __syncthreads();
    u32x4 os;
    st_sc1(rs, t256 * 16, (u32)WS_X1T + dA * (L_ * 2), conv_fin(smA, t256, xA, dskA, gaA, c1A, os));
    st_sc1(rs, t256 * 16, (u32)WS_X1S + dA * (L_ * 2), os);
    if (three && hw == 1) {
      st_sc1(rs, t256 * 16, (u32)WS_X1T + dC * (L_ * 2), conv_fin(smC, t256, c1, dskC, gaC, c1C, os));
      st_sc1(rs, t256 * 16, (u32)WS_X1S + dC * (L_ * 2), os);
    }
  }
  asm volatile("" ::"v"(touch));
  asm volatile("s_waitcnt vmcnt(0)" ::: "memory");
  __syncthreads();
  TS(1, 3);
  if (wv == 7) {
    u32 o = 0;
    if (lane == 0) o = __hip_atomic_fetch_add(cnt + (b & 7) * 64, 1u, __ATOMIC_RELAXED, __HIP_MEMORY_SCOPE_AGENT);
    o = __builtin_amdgcn_readfirstlane(o);
    if (o == 31u && lane < 8) __hip_atomic_store(cnt + (8 + lane) * 64 + (b & 7), 1u, __ATOMIC_RELAXED, __HIP_MEMORY_SCOPE_AGENT);
    for (int spins = 0; spins < (1 << 20); ++spins) {
      u32 v = 1u;
      if (lane < 8) v = __hip_atomic_load(cnt + (8 + (b & 7)) * 64 + lane, __ATOMIC_RELAXED, __HIP_MEMORY_SCOPE_AGENT);
      if (__all(v != 0u)) break;
      __builtin_amdgcn_s_sleep(1);
    }
  }
  __syncthreads();
  TS(1, 4);

  const int k0 = 7 * wv - (wv >> 2), nk = (wv & 3) == 3 ? 6 : 7;
  const u32 w2o = (u32)WS_W2F + (u32)((sl * 5) * 54 + k0) * 1024;
  bf16x8 wf[7][5];
  {
    const int l8 = tid & 7, crow = tid >> 3;
    u32x4 xv[12];
    f32x4 sb4;
    f32x4 acc[2][4];
#pragma unroll
    for (int fb = 0; fb < 2; ++fb)
#pragma unroll
      for (int tb = 0; tb < 4; ++tb) acc[fb][tb] = f32x4{0.f, 0.f, 0.f, 0.f};
    if (wv < 7) {
#pragma unroll
      for (int qq = 0; qq < 12; ++qq)
        xv[qq] = ld_sc1(rs, (qq < 11 || crow < 24 ? crow : 0) * (L_ * 2) + l8 * 16, (u32)WS_X1S + qq * (56 * L_ * 2) + tt * 128);
      __builtin_amdgcn_sched_barrier(0);
#pragma unroll
      for (int qq = 0; qq < 12; ++qq) {
        const int c = crow + 56 * qq;
        if (qq < 11 || crow < 24) *(u32x4*)(smem + M1_A + c * 128 + (((l8 >> 1) ^ h2_swz(c)) << 5) + ((l8 & 1) << 4)) = xv[qq];
      }
    } else {
      __builtin_amdgcn_s_setprio(3);
#pragma unroll
      for (int i = 0; i < 10; ++i) af[i >> 1][i & 1] = ld_frag1(rs, (lane >> 3) * (L_ * 2) + (lane & 7) * 16, (u32)WS_X1T + (sl * 80 + 8 * i) * (L_ * 2) + tt * 128);
    }
    __syncthreads();
    TS(1, 5);
    TC(3, 0);
    const int row0 = 8 * g + q, sw = h2_swz(row0);
    const char* rb0 = smem + M1_A + row0 * 128 + 8 * p;
    float* st = (float*)(smem + M1_ST);
    auto ldb = [&](int ks, bf16x8 (&bfr)[4]) {
#pragma unroll
      for (int tb = 0; tb < 4; ++tb) {
        const s16x4 lo = tr_read(rb0 + ks * 4096 + ((tb ^ sw) << 5));
        const s16x4 hi = tr_read(rb0 + ks * 4096 + 512 + ((tb ^ sw) << 5));
        bfr[tb] = cat8(lo, hi);
      }
    };
    bf16x8 bb[2][4];
    ldb(0, bb[0]);
#pragma unroll
    for (int ks = 0; ks < 20; ++ks) {
      if (active) {
        if (ks < 19) ldb(ks + 1, bb[(ks + 1) & 1]);
#pragma unroll
        for (int fb = 0; fb < 2; ++fb)
#pragma unroll
          for (int tb = 0; tb < 4; ++tb) acc[fb][tb] = mfma16(af[ks % W1_RING][fb], bb[ks & 1][tb], acc[fb][tb]);
        if (ks + W1_RING < 20) {
#pragma unroll
          for (int fb = 0; fb < 2; ++fb) af[ks % W1_RING][fb] = ld_frag(rs, l16, w1o + (fb * 20 + ks + W1_RING) * 1024);
        }
      } else if (ks == 0 && wv == 7) {
#pragma unroll
        for (int i = 0; i < 10; ++i) {
          const u32x4 v = __builtin_bit_cast(u32x4, af[i >> 1][i & 1]);
          const f32x4 a0 = {lo2f(v[0]), hi2f(v[0]), lo2f(v[1]), hi2f(v[1])}, a1 = {lo2f(v[2]), hi2f(v[2]), lo2f(v[3]), hi2f(v[3])};
          acc[0][0] += a0; acc[0][1] += a1; acc[0][2] += a0 * a0; acc[0][3] += a1 * a1;
        }
        const bool b5 = lane & 32, b4 = lane & 16, b3 = lane & 8;
        float v4[2][4], v2[2][2], v1[2];
#pragma unroll
        for (int h = 0; h < 2; ++h) {
#pragma unroll
          for (int j = 0; j < 4; ++j) {
            const float lo = acc[0][2 * h][j], hi = acc[0][2 * h + 1][j];
            v4[h][j] = (b5 ? hi : lo) + __shfl_xor(b5 ? lo : hi, 32);
          }
#pragma unroll
          for (int j = 0; j < 2; ++j) v2[h][j] = (b4 ? v4[h][2 + j] : v4[h][j]) + __shfl_xor(b4 ? v4[h][j] : v4[h][2 + j], 16);
          v1[h] = (b3 ? v2[h][1] : v2[h][0]) + __shfl_xor(b3 ? v2[h][0] : v2[h][1], 8);
        }
        const u32 toff = (8 * (lane & 7) + (lane >> 3)) * 8;
        __builtin_amdgcn_raw_buffer_store_b64(u32x2{__float_as_uint(v1[0]), __float_as_uint(v1[1]) | 0x80000000u}, rs, toff,
                                              (u32)WS_ST + (tt * 8 + sl) * 512, 16);
#pragma unroll
        for (int i = 0; i < 10; ++i) {
          const u32x4 v = __builtin_bit_cast(u32x4, af[i >> 1][i & 1]);
          char* dp = smem + M2_X + 8 * (lane & 7) * M2_XS + 2 * (8 * i + (lane >> 3));
#pragma unroll
          for (int k = 0; k < 4; ++k) {
            *(u16*)(dp + (2 * k) * M2_XS) = (u16)(v[k] & 0xffffu);
            *(u16*)(dp + (2 * k + 1) * M2_XS) = (u16)(v[k] >> 16);
          }
        }
        u32x2 gv[8];
        for (int spins = 0; spins < (1 << 20); ++spins) {
          u32 ok = 0x80000000u;
#pragma unroll
          for (int j = 0; j < 8; ++j) { gv[j] = __builtin_amdgcn_raw_buffer_load_b64(rs, lane * 8, (u32)WS_ST + (tt * 8 + j) * 512, 16); ok &= gv[j][1]; }
          if (__all(ok != 0u)) break;
          __builtin_amdgcn_s_sleep(2);
        }
        float sm = 0.f, sq = 0.f;
#pragma unroll
        for (int j = 0; j < 8; ++j) { sm += __uint_as_float(gv[j][0]); sq += __uint_as_float(gv[j][1] & 0x7fffffffu); }
        const float mean = sm * (1.f / D_);
        const float var = fmaxf(sq * (1.f / D_) - mean * mean, 0.f);
        st[lane] = rb(mean);
        st[64 + lane] = 1.f / rb(sqrtf(rb(rb(var) + 1e-5f)));
        __builtin_amdgcn_s_setprio(0);
      }
      if (ks >= 10) {
#pragma unroll
        for (int j = 0; j < 2; ++j) {
          const int f = 2 * (ks - 10) + j;
          wf[f / 5][f % 5] = LD_W2(rs, l16, w2o + ((f % 5) * 54 + f / 5) * 1024);
        }
      }
      if (ks == 9) {
        sb4 = __builtin_bit_cast(f32x4, ld_sc1(rs, ((lane >> 3) * F_ + 4 * (lane & 7)) * 4, (u32)WS_SB + 32 * p0 * 4));
      }
      if (ks == 17) {
#pragma unroll
        for (int e = 0; e < 4; ++e) { sb4[e] += __shfl_xor(sb4[e], 8); sb4[e] += __shfl_xor(sb4[e], 16); }
        if ((lane & 24) == 0) *(f32x4*)(smem + MN_SB + wv * 256 + (lane >> 5) * 128 + (lane & 7) * 16) = sb4;
      }
      __builtin_amdgcn_sched_barrier(0);
    }
    TS(2, 1);
    TC(3, 1);
#pragma unroll
    for (int i = W2_MID; i < 7; ++i)
#pragma unroll
      for (int cb = 0; cb < 5; ++cb) wf[i][cb] = LD_W2(rs, l16, w2o + (cb * 54 + (i < 6 || nk == 7 ? i : 5)) * 1024);
    __builtin_amdgcn_sched_barrier(0);
    __syncthreads();
    TS(2, 2);
    if (active) {
      const float* sbp = (const float*)(smem + MN_SB) + wv * 64 + 4 * g;
      const u32 hoff = (u32)WS_HID + (u32)((tt * 4) * 54 + p0) * 1024;
#pragma unroll
      for (int tb = 0; tb < 4; ++tb) {
        const float mb = st[tb * 16 + r16], rsd = st[64 + tb * 16 + r16];
        u32x4 v;
#pragma unroll
        for (int hh = 0; hh < 2; ++hh) {
          const f32x4 sv = *(const f32x4*)(sbp + 16 * hh), bv = *(const f32x4*)(sbp + 32 + 16 * hh);
          const f32x4 a = (acc[hh][tb] - mb * sv) * rsd + bv;
          v[2 * hh] = cvtpk(fmaxf(a[0], 0.f), fmaxf(a[1], 0.f));
          v[2 * hh + 1] = cvtpk(fmaxf(a[2], 0.f), fmaxf(a[3], 0.f));
        }
        st_sc1(rs, l16, hoff + tb * 54 * 1024, v);
      }
    }
  }
  TS(2, 3);
  asm volatile("s_waitcnt vmcnt(0)" ::: "memory");
  __syncthreads();
  TS(2, 4);
  if (tid == 448) __hip_atomic_store(cnt + (16 + tt * 8 + sl) * 64, 1u, __ATOMIC_RELAXED, __HIP_MEMORY_SCOPE_AGENT);
  __builtin_amdgcn_sched_barrier(0);
  poll_words<1>(cnt + (16 + tt * 8 + wv) * 64, 1u, lane);
  TS(1, 6);

  {
    const u32 hoff = (u32)WS_HID + (u32)((tt * 4) * 54 + k0) * 1024;
    u32x4 hf[2][4];
#pragma unroll
    for (int i = 0; i < 2; ++i)
#pragma unroll
      for (int tb = 0; tb < 4; ++tb) hf[i][tb] = ld_sc1(rs, l16, hoff + (tb * 54 + i) * 1024);
    f32x4 acc[5][4];
#pragma unroll
    for (int cb = 0; cb < 5; ++cb)
#pragma unroll
      for (int tb = 0; tb < 4; ++tb) acc[cb][tb] = f32x4{0.f, 0.f, 0.f, 0.f};
#pragma unroll
    for (int i = 0; i < 7; ++i) {
      if (i < 6 || nk == 7) {
#pragma unroll
        for (int tb = 0; tb < 4; ++tb)
#pragma unroll
          for (int cb = 0; cb < 5; ++cb) acc[cb][tb] = mfma16(wf[i][cb], __builtin_bit_cast(bf16x8, hf[i & 1][tb]), acc[cb][tb]);
      }
      __builtin_amdgcn_sched_barrier(0);
      if (i + 2 < 7) {
        const int kk = (i + 2 < 6 || nk == 7) ? i + 2 : 5;
#pragma unroll
        for (int tb = 0; tb < 4; ++tb) hf[i & 1][tb] = ld_sc1(rs, l16, hoff + (tb * 54 + kk) * 1024);
        __builtin_amdgcn_sched_barrier(0);
      }
    }
    TS(2, 5);
#pragma unroll
    for (int tb = 0; tb < 4; ++tb)
#pragma unroll
      for (int cb = 0; cb < 5; ++cb) {
        const int owner = tb + (cb < 3 ? 0 : 4);
        if (owner != wv) {
          const int slot = wv < owner ? wv : wv - 1;
          *(f32x4*)(smem + M2_RED + (((tb * 5 + cb) * 7 + slot) * 64 + lane) * 16) = acc[cb][tb];
        }
      }
    f32x4 own[3];
#pragma unroll
    for (int o = 0; o < 8; ++o) {
      if (wv == o) {
        asm volatile("" ::: "memory");
#pragma unroll
        for (int j = 0; j < 3; ++j) own[j] = acc[o < 4 ? j : (j < 2 ? 3 + j : 4)][o & 3];
      }
    }
    __syncthreads();
    TS(2, 6);
    const int tok = 16 * w4 + r16, cb0 = wv < 4 ? 0 : 3;
    f32x4 pr[3][7], gb4[3];
    u32x2 xr[3];
#pragma unroll
    for (int j = 0; j < 3; ++j) {
      const int cb = (j < 2 || wv < 4) ? cb0 + j : 4;
#pragma unroll
      for (int s7 = 0; s7 < 7; ++s7) pr[j][s7] = *(const f32x4*)(smem + M2_RED + (((w4 * 5 + cb) * 7 + s7) * 64 + lane) * 16);
      gb4[j] = *(const f32x4*)(smem + MN_GB + (16 * cb + 4 * g) * 4);
      xr[j] = *(const u32x2*)(smem + M2_X + tok * M2_XS + 2 * (16 * cb + 4 * g));
    }
    __builtin_amdgcn_sched_barrier(0);
#pragma unroll
    for (int j = 0; j < 3; ++j) {
      const f32x4 y = ((own[j] + pr[j][0]) + (pr[j][1] + pr[j][2])) + ((pr[j][3] + pr[j][4]) + (pr[j][5] + pr[j][6]));
      const u32 y0 = cvtpk(y[0], y[1]), y1 = cvtpk(y[2], y[3]);
      const u32 v0 = cvtpk(lo2f(y0) * gb4[j][0], hi2f(y0) * gb4[j][1]), v1 = cvtpk(lo2f(y1) * gb4[j][2], hi2f(y1) * gb4[j][3]);
      u32x2 o;
      o[0] = cvtpk(lo2f(xr[j][0]) + lo2f(v0), hi2f(xr[j][0]) + hi2f(v0));
      o[1] = cvtpk(lo2f(xr[j][1]) + lo2f(v1), hi2f(xr[j][1]) + hi2f(v1));
      if (j < 2 || wv < 4) *(u32x2*)(out + (size_t)(tt * 64 + tok) * D_ + sl * 80 + 16 * (cb0 + j) + 4 * g) = o;
    }
  }
  TS(1, 7);
}

extern "C" void kernel_launch(void* const* d_in, const int* in_sizes, int n_in, void* d_out, int out_size, void* d_ws,
                              size_t ws_size, hipStream_t stream) {
  char* ws = (char*)d_ws;
  k_pre<<<NB_LN + NB_KG + NB_MOD + NB_RL, 256, 0, stream>>>(
      (const u16*)d_in[0], (const u16*)d_in[1], (const u16*)d_in[2], (const u16*)d_in[3], (const u16*)d_in[4],
      (const u16*)d_in[5], (const u16*)d_in[6], (const u16*)d_in[7], (const u16*)d_in[10], (const u16*)d_in[11],
      (const u16*)d_in[12], (const u16*)d_in[13], ws);
  k_main<<<256, 512, 0, stream>>>((const u16*)d_in[8], (const u16*)d_in[11], ws, (u16*)d_out);
}
```

```cpp
#include <hip/hip_runtime.h>
#include <hip/hip_bf16.h>
#include <stdint.h>

extern "C" __device__ unsigned long long llvm_dispatch_id() __asm("llvm.amdgcn.dispatch.id");
typedef unsigned short u16;
typedef unsigned int u32;
typedef __attribute__((ext_vector_type(8))) short bf16x8;
typedef __attribute__((ext_vector_type(4))) short s16x4;
typedef __attribute__((ext_vector_type(4))) float f32x4;
typedef __attribute__((ext_vector_type(2))) float f32x2;
typedef __attribute__((ext_vector_type(4))) unsigned int u32x4;
typedef __attribute__((ext_vector_type(2))) unsigned int u32x2;
typedef __attribute__((ext_vector_type(3))) unsigned int u32x3;
typedef __bf16 bf16x2_t __attribute__((ext_vector_type(2)));
#define LDS_AS __attribute__((address_space(3)))
#define DI __device__ __forceinline__

constexpr int L_ = 2048, D_ = 640, N_ = 128, F_ = 1728;
constexpr int KSM = 4;

constexpr size_t WS_MODP = 0;
constexpr size_t WS_CNT = 126976;
constexpr size_t WS_NT = 262144;
constexpr size_t WS_XT = WS_NT + (size_t)D_ * L_ * 2;
constexpr size_t WS_X1T = WS_XT + (size_t)D_ * L_ * 2;
constexpr size_t WS_X1S = WS_X1T + (size_t)D_ * L_ * 2;
constexpr int KTAB = 1296;
constexpr size_t WS_KT = WS_X1S + (size_t)D_ * L_ * 2;
constexpr size_t WS_SB = WS_KT + (size_t)D_ * KTAB;
constexpr size_t WS_W1F = WS_SB + 2 * 20 * 1728 * 4;
constexpr size_t WS_W2F = WS_W1F + (size_t)108 * 20 * 1024;
constexpr size_t WS_HID = WS_W2F + (size_t)40 * 54 * 1024;
constexpr size_t WS_ST = WS_HID + (size_t)128 * 54 * 1024;

#define TS(kern, slot) do {} while (0)
#define TC(kern, slot) do {} while (0)

DI void st_wt(void* p, u32x4 v) { asm volatile("global_store_dwordx4 %0, %1, off sc1\n\ts_nop 1" ::"v"(p), "v"(v) : "memory"); }
#define ST_PRE(p, v) (*(p) = (v))
#define ST_PREW(p, v) (*(p) = (v))
DI u32 cvtpk(float lo, float hi) {
  f32x2 v = {lo, hi};
  bf16x2_t r = __builtin_convertvector(v, bf16x2_t);
  return __builtin_bit_cast(u32, r);
}
typedef _Float16 f16x2_t __attribute__((ext_vector_type(2)));
typedef _Float16 f16x8_t __attribute__((ext_vector_type(8)));
DI u32 cvtph(float lo, float hi) {
  f32x2 v = {lo, hi};
  f16x2_t r = __builtin_convertvector(v, f16x2_t);
  return __builtin_bit_cast(u32, r);
}
DI float dot2bf(u32 a, u32 b, float c) {
  return __builtin_amdgcn_fdot2_f32_bf16(__builtin_bit_cast(bf16x2_t, a), __builtin_bit_cast(bf16x2_t, b), c, false);
}
DI float lo2f(u32 v) { return __uint_as_float(v << 16); }
DI float hi2f(u32 v) { return __uint_as_float(v & 0xffff0000u); }
DI float bf2f(u16 v) { return __uint_as_float(((u32)v) << 16); }
DI float rb(float x) { return lo2f(cvtpk(x, x)); }
DI f32x4 mfma16(bf16x8 a, bf16x8 b, f32x4 c) { return __builtin_amdgcn_mfma_f32_16x16x32_bf16(a, b, c, 0, 0, 0); }
DI s16x4 tr_read(const char* p) {
  return __builtin_amdgcn_ds_read_tr16_b64_v4i16((LDS_AS s16x4*)p);
}
DI bf16x8 cat8(s16x4 a, s16x4 b) { return __builtin_shufflevector(a, b, 0, 1, 2, 3, 4, 5, 6, 7); }
DI void wave_sync() {
  __builtin_amdgcn_fence(__ATOMIC_RELEASE, "wavefront");
  __builtin_amdgcn_wave_barrier();
  __builtin_amdgcn_fence(__ATOMIC_ACQUIRE, "wavefront");
}

DI f32x2 cmul2(f32x2 a, f32x2 b) {
  const f32x2 bj = {-b.y, b.x};
  return __builtin_elementwise_fma(a.yy, bj, a.xx * b);
}
struct c32 { float r, i; };
DI c32 cmul(c32 a, c32 b) { return {a.r * b.r - a.i * b.i, a.r * b.i + a.i * b.r}; }
DI c32 cexp_scaled(float ar, float ai, float e) {
  const float mag = __expf(ar * e);
  const float x = ai * e;
  const float C_HI = 0.15915494f, C_LO = 6.4206382e-9f;
  const float r = x * C_HI;
  const float rl = fmaf(x, C_HI, -r) + x * C_LO;
  const float fr = (r - floorf(r)) + rl;
  return {mag * __builtin_amdgcn_cosf(fr), mag * __builtin_amdgcn_sinf(fr)};
}
DI int tap_index(int m) {
  u32 bits = __float_as_uint((float)m);
  bits += 0x7fffu + ((bits >> 16) & 1u);
  const int id2 = (int)(bits >> 16) - (0x4300 - 128);
  return m < 128 ? m : id2;
}

constexpr int NB_LN = 64, NB_KG = 320, NB_MOD = 30 * KSM;
constexpr int NB_RL = 70;
constexpr int PRE_SMEM = 43008;

DI void role_ln1(int b, const u16* __restrict__ x, u32* __restrict__ st1, u16* __restrict__ xT, char* smem) {
  const int tid = threadIdx.x, u = tid >> 4, j = tid & 15;
  const int t0 = b * 32;
  const u32x4* xa = (const u32x4*)(x + (size_t)(t0 + 2 * u) * D_);
  u32x4 A[5], B[5];
#pragma unroll
  for (int q = 0; q < 5; ++q) { A[q] = xa[j + 16 * q]; B[q] = xa[80 + j + 16 * q]; }
  float sa = 0.f, sb = 0.f, qa = 0.f, qb = 0.f;
#pragma unroll
  for (int q = 0; q < 5; ++q)
#pragma unroll
    for (int k = 0; k < 4; ++k) {
      sa = dot2bf(A[q][k], 0x3f803f80u, sa); qa = dot2bf(A[q][k], A[q][k], qa);
      sb = dot2bf(B[q][k], 0x3f803f80u, sb); qb = dot2bf(B[q][k], B[q][k], qb);
    }
#pragma unroll
  for (int o = 1; o < 16; o <<= 1) {
    sa += __shfl_xor(sa, o, 16); sb += __shfl_xor(sb, o, 16); qa += __shfl_xor(qa, o, 16); qb += __shfl_xor(qb, o, 16);
  }
  const float ma = sa * (1.f / D_), mb = sb * (1.f / D_);
  const float va = fmaxf(qa - sa * ma, 0.f), vb = fmaxf(qb - sb * mb, 0.f);
  if (j == 0) {
    const u32 s0 = cvtpk(ma, sqrtf(rb(rb(va * (1.f / D_)) + 1e-5f))), s1 = cvtpk(mb, sqrtf(rb(rb(vb * (1.f / D_)) + 1e-5f)));
    *(u32x2*)(st1 + t0 + 2 * u) = u32x2{s0, s1};
  }
  char* wbase = smem + (8 * j + (j >> 2)) * 64 + ((((u >> 2) ^ (j & 3)) << 2) + (u & 3)) * 4;
#pragma unroll
  for (int q = 0; q < 5; ++q)
#pragma unroll
    for (int k = 0; k < 4; ++k) {
      *(u32*)(wbase + (132 * q + 2 * k) * 64) = (A[q][k] & 0xffffu) | (B[q][k] << 16);
      *(u32*)(wbase + (132 * q + 2 * k + 1) * 64) = (A[q][k] >> 16) | (B[q][k] & 0xffff0000u);
    }
  __syncthreads();
#pragma unroll
  for (int i = 0; i < 10; ++i) {
    const int id = tid + 256 * i, c = id >> 2, o = id & 3;
    const u32x4 v = *(const u32x4*)(smem + (c + (c >> 5)) * 64 + ((o ^ ((c >> 3) & 3)) << 4));
    ST_PRE((u32x4*)(xT + (size_t)c * L_ + t0 + 8 * o), v);
  }
}

DI void role_kgen(int bk, const u16* __restrict__ Are, const u16* __restrict__ Aim, const u16* __restrict__ Bre,
                  const u16* __restrict__ Bim, const u16* __restrict__ Cre, const u16* __restrict__ Cim,
                  const u16* __restrict__ ldt, char* __restrict__ KT, char* smem) {
  const int tid = threadIdx.x, w = tid >> 6, lane = tid & 63, r16 = lane & 15, g = lane >> 4;
  const int d = 2 * bk + (w >> 1), n = (w & 1) * 64 + lane;
  const int gi = d * N_ + n;
  const float dt = rb(expf(bf2f(ldt[d])));
  const float ar = dt * bf2f(Are[gi]), ai = dt * bf2f(Aim[gi]);
  const c32 dB = {dt * bf2f(Bre[gi]), dt * bf2f(Bim[gi])};
  const c32 Cc = {bf2f(Cre[gi]), bf2f(Cim[gi])};
  const c32 W = cmul(Cc, dB);
  const c32 E1 = cexp_scaled(ar, ai, 1.f), E8 = cexp_scaled(ar, ai, 8.f), E128 = cexp_scaled(ar, ai, 128.f);

  float wmax = fmaxf(fabsf(W.r), fabsf(W.i));
#pragma unroll
  for (int o = 1; o < 64; o <<= 1) wmax = fmaxf(wmax, __shfl_xor(wmax, o));
  int ew = (int)((__float_as_uint(wmax) >> 23) & 0xff);
  ew = ew < 20 ? 20 : (ew > 240 ? 240 : ew);
  const float wsc = __uint_as_float((u32)(127 + 10 + 127 - ew - 1) << 23);
  const float unsc = __uint_as_float((u32)(ew + 1 - 20) << 23);

  char* buf = smem + w * 8704;
  char* wr = buf + lane * 4;
  const char* rd = buf + r16 * 272 + g * 16;
  auto put = [&](int row, f32x2 v) { *(u32*)(wr + row * 272) = cvtph(v.x, v.y); };
  {
    const f32x2 e8 = {E8.r, E8.i};
    f32x2 p = {1024.f, 0.f};
#pragma unroll
    for (int jj = 0; jj < 16; ++jj) { put(jj, p); if (jj < 15) p = cmul2(p, e8); }
  }
  wave_sync();
  f16x8_t bh[4];
#pragma unroll
  for (int ks = 0; ks < 4; ++ks) bh[ks] = *(const f16x8_t*)(rd + ks * 64);
  wave_sync();
  const f32x2 e1 = {E1.r, -E1.i}, e128 = {E128.r, -E128.i};
  f32x2 sm[8];
  sm[1] = e1;
#pragma unroll
  for (int i = 2; i < 8; ++i) sm[i] = cmul2(sm[i - 1], e1);
  f32x2 WG[17];
  WG[0] = f32x2{W.r * wsc, -W.i * wsc};
#pragma unroll
  for (int i = 1; i < 17; ++i) WG[i] = cmul2(WG[i - 1], e128);
  f32x4 acc[3];
  auto tile_mma = [&](int t) {
    wave_sync();
    f32x4 a = {0.f, 0.f, 0.f, 0.f};
#pragma unroll
    for (int ks = 0; ks < 4; ++ks) a = __builtin_amdgcn_mfma_f32_16x16x32_f16(*(const f16x8_t*)(rd + ks * 64), bh[ks], a, 0, 0, 0);
    acc[t] = a * unsc;
    wave_sync();
  };
#pragma unroll
  for (int i = 0; i < 2; ++i)
#pragma unroll
    for (int rho = 0; rho < 8; ++rho) put(8 * i + rho, rho ? cmul2(sm[rho], WG[i]) : WG[i]);
  tile_mma(0);
#pragma unroll
  for (int i = 0; i < 2; ++i)
#pragma unroll
    for (int rho = 0; rho < 4; ++rho) put(4 * i + rho, rho ? cmul2(sm[2 * rho], WG[2 + i]) : WG[2 + i]);
#pragma unroll
  for (int i = 0; i < 4; ++i)
#pragma unroll
    for (int rho = 0; rho < 2; ++rho) put(8 + 2 * i + rho, rho ? cmul2(sm[4], WG[4 + i]) : WG[4 + i]);
  tile_mma(1);
#pragma unroll
  for (int i = 0; i < 9; ++i) put(i, WG[8 + i]);
  tile_mma(2);

#pragma unroll
  for (int t = 0; t < 3; ++t) *(f32x4*)(buf + t * 1024 + lane * 16) = acc[t];
  __syncthreads();
  u16* T = (u16*)(smem + (w >> 1) * 2 * 8704 + 4352);
  {
    const char* ob = smem + (w ^ 1) * 8704 + lane * 16;
    if (!(w & 1)) {
      const f32x4 a0 = acc[0] + *(const f32x4*)(ob), a2 = acc[2] + *(const f32x4*)(ob + 2048);
      u16* t0 = T + 128 * (g >> 1) + 4 * (g & 1) + 8 * r16;
      *(u32x2*)t0 = u32x2{cvtpk(a0[0], a0[1]), cvtpk(a0[2], a0[3])};
      if (g < 2) {
#pragma unroll
        for (int rg = 0; rg < 4; ++rg) T[512 + 16 * (4 * g + rg) + r16] = (u16)cvtpk(a2[rg], 0.f);
      } else if (lane == 32) {
        T[640] = (u16)cvtpk(a2[0], 0.f);
      } else if (lane >= 48 && lane < 55) {
        T[641 + lane - 48] = 0;
      }
    } else {
      const f32x4 a1 = acc[1] + *(const f32x4*)(ob + 1024);
      if (g < 2) {
        *(u32x2*)(T + 256 + 64 * g + 4 * r16) = u32x2{cvtpk(a1[0], a1[1]), cvtpk(a1[2], a1[3])};
      } else {
        *(u32*)(T + 384 + 64 * (g - 2) + 2 * r16) = cvtpk(a1[0], a1[1]);
        *(u32*)(T + 384 + 64 * (g - 2) + 32 + 2 * r16) = cvtpk(a1[2], a1[3]);
      }
    }
  }
  __syncthreads();
  const int t128 = tid & 127;
  if (t128 < 81) *(u32x4*)(KT + (size_t)d * KTAB + t128 * 16) = *(const u32x4*)((const char*)T + t128 * 16);
}

DI void role_mod(int bm, const u16* __restrict__ c, const u16* __restrict__ wm, float* __restrict__ modp, char* smem) {
  const int tid = threadIdx.x, cj = tid & 15, rg = tid >> 4, w = tid >> 6;
  const int cbk = bm % 30, ks = bm / 30;
  constexpr int NR = 640 / KSM / 16;
  u32x4 v[NR];
  u16 cr[NR];
#pragma unroll
  for (int i = 0; i < NR; ++i) {
    const int row = (640 / KSM) * ks + rg + 16 * i;
    v[i] = *(const u32x4*)(wm + (size_t)row * (6 * D_) + 128 * cbk + 8 * cj);
    cr[i] = c[row];
  }
  float a[8];
#pragma unroll
  for (int k = 0; k < 8; ++k) a[k] = 0.f;
#pragma unroll
  for (int i = 0; i < NR; ++i) {
    const float cv = bf2f(cr[i]);
    const float sv = rb(cv / (1.f + __expf(-cv)));
#pragma unroll
    for (int k = 0; k < 4; ++k) { a[2 * k] += sv * lo2f(v[i][k]); a[2 * k + 1] += sv * hi2f(v[i][k]); }
  }
#pragma unroll
  for (int k = 0; k < 8; ++k) { a[k] += __shfl_xor(a[k], 16); a[k] += __shfl_xor(a[k], 32); }
  float* red = (float*)smem;
  if ((tid & 63) < 16) {
#pragma unroll
    for (int k = 0; k < 8; ++k) red[w * 128 + 8 * cj + k] = a[k];
  }
  __syncthreads();
  if (tid < 128) modp[ks * (6 * D_) + 128 * cbk + tid] = red[tid] + red[128 + tid] + red[256 + tid] + red[384 + tid];
}

DI int rl_swz(int row) { return (row & 3) | ((((row >> 2) ^ (row >> 3)) & 1) << 2); }

DI void role_relayout(int br, const u16* __restrict__ w1, const u16* __restrict__ w2, char* __restrict__ ws, char* smem) {
  const int tid = threadIdx.x, w = tid >> 6, lane = tid & 63;
  const int tile = br * 4 + w;
  if (tile >= 550) return;
  const bool is1 = tile < 280;
  int ks, nb, ld, nblk;
  const u16* src;
  if (is1) { ks = tile / 14; nb = tile % 14; ld = F_; src = w1; nblk = nb == 13 ? 4 : 8; }
  else { const int t2 = tile - 280; ks = t2 / 5; nb = t2 % 5; ld = D_; src = w2; nblk = 8; }
  char* buf = smem + w * 8192;
  const int lr = lane >> 4, lc = lane & 15;
  u32x4 v[8];
#pragma unroll
  for (int i = 0; i < 8; ++i) v[i] = *(const u32x4*)(src + (size_t)(32 * ks + 4 * i + lr) * ld + 128 * nb + 8 * (lc < 2 * nblk ? lc : 0));
#pragma unroll
  for (int i = 0; i < 8; ++i) {
    const int row = 4 * i + lr;
    *(u32x4*)(buf + row * 256 + (((lc >> 1) ^ rl_swz(row)) << 5) + ((lc & 1) << 4)) = v[i];
  }
  wave_sync();
  const int g = lane >> 4, q = (lane & 15) >> 2, p = lane & 3;
  const int row0 = is1 ? 8 * g + q : 4 * g + q, row1 = is1 ? row0 + 4 : row0 + 16;
  char* dst = is1 ? ws + WS_W1F + ((size_t)(8 * nb) * 20 + ks) * 1024 : ws + WS_W2F + ((size_t)(8 * nb) * 54 + ks) * 1024;
  const int dstride = is1 ? 20 * 1024 : 54 * 1024;
#pragma unroll
  for (int cb = 0; cb < 8; ++cb) {
    const s16x4 lo = tr_read(buf + row0 * 256 + ((cb ^ rl_swz(row0)) << 5) + 8 * p);
    const s16x4 hi = tr_read(buf + row1 * 256 + ((cb ^ rl_swz(row1)) << 5) + 8 * p);
    if (cb < nblk) ST_PREW((u32x4*)(dst + (size_t)cb * dstride + lane * 16), __builtin_bit_cast(u32x4, cat8(lo, hi)));
  }
}

__global__ __launch_bounds__(256) void k_pre(const u16* __restrict__ x, const u16* __restrict__ Are, const u16* __restrict__ Aim,
                                             const u16* __restrict__ Bre, const u16* __restrict__ Bim, const u16* __restrict__ Cre,
                                             const u16* __restrict__ Cim, const u16* __restrict__ ldt, const u16* __restrict__ w1,
                                             const u16* __restrict__ w2, const u16* __restrict__ c, const u16* __restrict__ wm,
                                             char* __restrict__ ws) {
  __shared__ __attribute__((aligned(16))) char smem[PRE_SMEM];
  int b = blockIdx.x;
  b = b < NB_KG ? b + NB_LN : (b < NB_KG + NB_LN ? b - NB_KG : b);
  TS(0, 0);
  if (b < NB_LN) {
    if (b < 34 && threadIdx.x < 64) ((u32*)(ws + WS_CNT))[(b * 8 + (threadIdx.x >> 3)) * 64 + (threadIdx.x & 7)] = 0u;
    *(u32x2*)(ws + WS_ST + (b * 256 + threadIdx.x) * 8) = u32x2{0u, 0u};
    role_ln1(b, x, (u32*)(ws + WS_NT), (u16*)(ws + WS_XT), smem); TS(0, 7); return;
  }
  b -= NB_LN;
  if (b < NB_KG) { role_kgen(b, Are, Aim, Bre, Bim, Cre, Cim, ldt, ws + WS_KT, smem); TS(0, 7); return; }
  b -= NB_KG;
  if (b < NB_MOD) { role_mod(b, c, wm, (float*)(ws + WS_MODP), smem); TS(0, 7); return; }
  b -= NB_MOD;
  role_relayout(b, w1, w2, ws, smem);
  TS(0, 7);
}

constexpr int CV_H0 = 0, CV_HS = 4160, CV_K = 12544, CV_T = 21760, CV_H3 = 32768 + 192, CV_CH = 37376;
static_assert(CV_CH % 256 == 0 && CV_H3 + 4160 <= CV_CH && CV_T + 4 * KTAB <= 32768, "conv lds");
constexpr int M1_A = 0, M1_ST = 81920;
constexpr int MN_SB = 3 * CV_CH;
constexpr int MN_R1 = MN_SB + 2048;
constexpr int M2_RED = 0, M2_X = 143360, M2_XS = 176;
constexpr int MN_GB = M2_X + 64 * M2_XS;
constexpr int MN_SMEM = MN_GB + 320;
static_assert(MN_R1 + 5 * 4864 <= M2_X && M1_ST + 512 <= MN_SB, "mlp lds");

typedef __amdgpu_buffer_rsrc_t rsrc_t;
DI u32x4 ld_sc1(rsrc_t r, u32 voff, u32 soff) { return __builtin_amdgcn_raw_buffer_load_b128(r, voff, soff, 16); }
DI void st_sc1(rsrc_t r, u32 voff, u32 soff, u32x4 v) { __builtin_amdgcn_raw_buffer_store_b128(v, r, voff + soff, 0, 16); }
DI bf16x8 ld_frag1(rsrc_t r, u32 voff, u32 soff) { return __builtin_bit_cast(bf16x8, __builtin_amdgcn_raw_buffer_load_b128(r, voff, soff, 16)); }
DI bf16x8 ld_frag(rsrc_t r, u32 voff, u32 soff) { return __builtin_bit_cast(bf16x8, __builtin_amdgcn_raw_buffer_load_b128(r, voff, soff, 0)); }

DI int h2_swz(int k) { return ((k >> 1) & 1) | (((k >> 3) & 1) << 1); }

DI void conv_stage_k(char* smem, u32x4 tv0, u32x4 tv1, int w, int lane, int tid) {
  char* tb = smem + CV_T + w * KTAB;
  *(u32x4*)(tb + lane * 16) = tv0;
  if (lane < 17) *(u32x4*)(tb + (64 + lane) * 16) = tv1;
  wave_sync();
  const u16* T = (const u16*)tb;
#pragma unroll
  for (int i = 0; i < 2; ++i) {
    const int cp = tid + 256 * i;
    if (cp < 284) {
      u32 e[9];
#pragma unroll
      for (int j = 0; j < 9; ++j) {
        const int m = 2047 - 8 * cp - j;
        e[j] = T[m < 0 ? 641 : tap_index(m)];
      }
      *(u32x4*)(smem + CV_K + 4576 + cp * 16) = u32x4{e[0] | (e[1] << 16), e[2] | (e[3] << 16), e[4] | (e[5] << 16), e[6] | (e[7] << 16)};
      *(u32x4*)(smem + CV_K + cp * 16) = u32x4{e[1] | (e[2] << 16), e[3] | (e[4] << 16), e[5] | (e[6] << 16), e[7] | (e[8] << 16)};
    }
  }
}

DI void conv_stage_h(char* smem, u32x4 xq0, u32x4 xq1, const float (&mn)[16], const float (&rs)[16], float ca1, float sa, int tid) {
  u32 h[8];
#pragma unroll
  for (int k = 1; k < 8; ++k) {
    const u32 xv = k < 4 ? xq0[k] : xq1[k - 4];
    const u32 c = cvtpk(lo2f(xv) - mn[2 * k], hi2f(xv) - mn[2 * k + 1]);
    const u32 n = cvtpk(lo2f(c) * rs[2 * k], hi2f(c) * rs[2 * k + 1]);
    const u32 p = cvtpk(lo2f(n) * ca1, hi2f(n) * ca1);
    const u32 hv = cvtpk(lo2f(p) + sa, hi2f(p) + sa);
    h[k] = (k >= 4 || tid > 0) ? hv : 0u;
  }
  const int yo = (tid + 3) * 16;
  *(u32x4*)(smem + CV_H0 + yo) = u32x4{h[1], h[2], h[3], h[4]};
  *(u32x4*)(smem + CV_H0 + CV_HS + yo) = u32x4{h[2], h[3], h[4], h[5]};
  *(u32x4*)(smem + CV_H0 + 2 * CV_HS + yo) = u32x4{h[3], h[4], h[5], h[6]};
  *(u32x4*)(smem + CV_H3 + yo) = u32x4{h[4], h[5], h[6], h[7]};
  if (tid < 12) {
    const int cc = tid / 3, yc = tid % 3;
    *(u32x4*)(smem + (cc == 3 ? CV_H3 : CV_H0 + cc * CV_HS) + yc * 16) = u32x4{0u, 0u, 0u, 0u};
  }
}

template <class CB>
DI void conv_mma(const char* smem, int w, int lane, f32x4 (&acc)[8], CB&& side) {
  const int r16 = lane & 15, g = lane >> 4, cpy = r16 & 3;
  const char* hb = smem + (cpy == 3 ? CV_H3 : CV_H0 + cpy * CV_HS) + 16 * ((r16 >> 2) + 4 * w + g);
  const char* kb = smem + CV_K + (r16 & 1) * 4576 + 16 * (252 + 4 * w - 4 * (r16 >> 1) + g) - 256 * 14;
#pragma unroll
  for (int i = 0; i < 8; ++i) acc[i] = f32x4{0.f, 0.f, 0.f, 0.f};
#pragma unroll
  for (int par = 1; par >= 0; --par) {
    bf16x8 Bf[8];
#pragma unroll
    for (int t = 0; t < 8; ++t) Bf[t] = *(const bf16x8*)(hb + 256 * (2 * t + par));
#pragma unroll
    for (int sg = -par; sg <= 14; sg += 2) {
      const bf16x8 Af = *(const bf16x8*)(kb + 256 * (14 - sg));
#pragma unroll
      for (int i = 0; i < 8; ++i) {
        const int tau = 2 * i - sg;
        if (tau >= 0 && tau <= 15) acc[i] = mfma16(Af, Bf[tau >> 1], acc[i]);
      }
      side((1 - par) * 8 + ((sg + par) >> 1));
    }
  }
}

DI void conv_put(char* smem, int w, int lane, const f32x4 (&acc)[8]) {
  char* pb = smem + w * 8192 + (64 * (lane >> 4) + 2 * (lane & 15)) * 4;
#pragma unroll
  for (int i = 0; i < 8; ++i) {
    *(f32x2*)(pb + i * 1024) = f32x2{acc[i][0], acc[i][1]};
    *(f32x2*)(pb + i * 1024 + 128) = f32x2{acc[i][2], acc[i][3]};
  }
}

DI u32x4 conv_fin(const char* smem, int t, u32x4 xv, float dsk, float ga, float c1, u32x4& os) {
  f32x4 y0 = *(const f32x4*)(smem + t * 32), y1 = *(const f32x4*)(smem + t * 32 + 16);
#pragma unroll
  for (int w = 1; w < 4; ++w) { y0 += *(const f32x4*)(smem + w * 8192 + t * 32); y1 += *(const f32x4*)(smem + w * 8192 + t * 32 + 16); }
  const u32x4 hv = *(const u32x4*)(smem + CV_H3 + 48 + t * 16);
  u32x4 o;
#pragma unroll
  for (int k = 0; k < 4; ++k) {
    const u32 yr = k < 2 ? cvtpk(y0[2 * k], y0[2 * k + 1]) : cvtpk(y1[2 * k - 4], y1[2 * k - 3]);
    const u32 hd = cvtpk(lo2f(hv[k]) * dsk, hi2f(hv[k]) * dsk);
    const u32 v = cvtpk(lo2f(yr) + lo2f(hd), hi2f(yr) + hi2f(hd));
    const u32 gv = cvtpk(lo2f(v) * ga, hi2f(v) * ga);
    o[k] = cvtpk(lo2f(xv[k]) + lo2f(gv), hi2f(xv[k]) + hi2f(gv));
    os[k] = cvtpk(lo2f(o[k]) * c1, hi2f(o[k]) * c1);
  }
  return o;
}

template <int NW>
DI void poll_words(const u32* cnt, u32 target, int lane) {
  for (int spins = 0; spins < (1 << 20); ++spins) {
    u32 v = target;
    if (lane < NW) v = __hip_atomic_load(cnt + lane * 64, __ATOMIC_RELAXED, __HIP_MEMORY_SCOPE_AGENT);
    if (__all(v >= target)) break;
    __builtin_amdgcn_s_sleep(1);
  }
}

#define LD_W2 ld_frag1
constexpr int W1_RING = 10;
constexpr int W2_MID = 4;

__global__ __launch_bounds__(512, 2) void k_main(const u16* __restrict__ Dsk, const u16* __restrict__ w2, char* __restrict__ ws,
                                                    u16* __restrict__ out) {
  __shared__ __attribute__((aligned(256))) char smem[MN_SMEM];
  const int tid = threadIdx.x, wv = __builtin_amdgcn_readfirstlane(tid >> 6), lane = tid & 63;
  const int r16 = lane & 15, g = lane >> 4, q = r16 >> 2, p = lane & 3;
  const int b = blockIdx.x, hw = wv >> 2, w4 = wv & 3, t256 = tid & 255;
  const rsrc_t rs = __builtin_amdgcn_make_buffer_rsrc((void*)ws, 0, 0x7fffffff, 0x00020000);
  u32* cnt = (u32*)(ws + WS_CNT);
  const int idx = b >> 3, tt = ((b & 7) >> 1) * 8 + (idx & 7), sl = (b & 1) * 4 + (idx >> 3);
  const int pbase = 7 * sl - (sl >> 2), pcnt = (sl & 3) == 3 ? 6 : 7;
  const bool active = wv < pcnt;
  const int p0 = pbase + (active ? wv : 0);
  TS(1, 0);
  if (tid == 0 && b == 0) *(unsigned long long*)(ws + (100u << 20)) = llvm_dispatch_id() ^ (unsigned long long)__builtin_amdgcn_dispatch_ptr();

  const bool three = b < 128;
  const int d0 = three ? 3 * b : 384 + 2 * (b - 128);
  const int dA = d0 + hw, dC = three ? d0 + 2 : d0;
  bf16x8 af[W1_RING][2];
  const u32 l16 = lane * 16;
  const u32 w1o = (u32)WS_W1F + (u32)(2 * p0) * 20 * 1024;
  {
    const u16* xT = (const u16*)(ws + WS_XT);
    const float* modp = (const float*)(ws + WS_MODP);
    const char* ktA = ws + WS_KT + (size_t)dA * KTAB;
    const u32x4 tA0 = *(const u32x4*)(ktA + lane * 16), tA1 = *(const u32x4*)(ktA + (lane < 17 ? 64 + lane : 0) * 16);
    const int mj = (lane & 15) >> 2;
    float mp = modp[(lane & 3) * (6 * D_) + (mj < 3 ? mj : 4) * D_ + ((lane & 16) ? dC : dA)];
    const int tm1 = t256 > 0 ? t256 - 1 : 0;
    const u32x4 xA0 = *(const u32x4*)(xT + (size_t)dA * L_ + 8 * tm1);
    const u32x4 xA = *(const u32x4*)(xT + (size_t)dA * L_ + 8 * t256);
    u32x4 sq[4];
#pragma unroll
    for (int i = 0; i < 4; ++i) sq[i] = *(const u32x4*)(ws + WS_NT + (i < 2 ? tm1 : t256) * 32 + (i & 1) * 16);
    const char* pc0 = hw == 0 ? ws + WS_KT + (size_t)dC * KTAB + lane * 16 : (const char*)(xT + (size_t)dC * L_ + 8 * tm1);
    const char* pc1 = hw == 0 ? ws + WS_KT + (size_t)dC * KTAB + (lane < 17 ? 64 + lane : 0) * 16 : (const char*)(xT + (size_t)dC * L_ + 8 * t256);
    const u32x4 c0 = *(const u32x4*)pc0, c1 = *(const u32x4*)pc1;
    const u16 dskAr = Dsk[dA], dskCr = Dsk[dC];
    __builtin_amdgcn_sched_barrier(0);
    asm volatile("" ::"v"(xA0), "v"(c0), "v"(sq[0]));
    TS(3, 2);
    const float dskA = bf2f(dskAr), dskC = bf2f(dskCr);
    char* smA = smem + hw * CV_CH;
    char* smC = smem + 2 * CV_CH;
    conv_stage_k(smA, tA0, tA1, w4, lane, t256);
    if (three && hw == 0) conv_stage_k(smC, c0, c1, w4, lane, t256);
    mp += __shfl_xor(mp, 1); mp += __shfl_xor(mp, 2);
    mp = rb(mp);
    auto rl = [&](int l) { return __uint_as_float(__builtin_amdgcn_readlane(__float_as_uint(mp), l)); };
    const float saA = rl(0), caA = rl(4), gaA = rl(8), c1A = rb(1.f + rl(12));
    const float saC = rl(16), caC = rl(20), gaC = rl(24), c1C = rb(1.f + rl(28));
    float mn[16], rsd[16];
#pragma unroll
    for (int i = 2; i < 16; ++i) { mn[i] = lo2f(sq[i >> 2][i & 3]); rsd[i] = __builtin_amdgcn_rcpf(hi2f(sq[i >> 2][i & 3])); }
    mn[0] = mn[1] = rsd[0] = rsd[1] = 0.f;
    conv_stage_h(smA, xA0, xA, mn, rsd, rb(1.f + caA), saA, t256);
    if (three && hw == 1) conv_stage_h(smC, c0, c1, mn, rsd, rb(1.f + caC), saC, t256);
    __builtin_amdgcn_sched_barrier(0);
    TS(3, 3);
    const int t1 = (b - 128) * 4 + wv;
    const bool do1 = !three && wv < 4 && t1 < 432;
    u32x4 rv[5];
    f32x4 accA[8], accC[8];
    if (do1) {
#pragma unroll
      for (int i = 0; i < 5; ++i) rv[i] = __builtin_bit_cast(u32x4, ld_frag(rs, l16, (u32)WS_W1F + (t1 * 5 + i) * 1024));
      const float* mq = modp + 160 * (t1 & 3) + 4 * (lane < 40 ? lane : 0);
#pragma unroll
      for (int k = 0; k < KSM; ++k) { accC[k] = *(const f32x4*)(mq + k * (6 * D_) + 4 * D_); accC[4 + k] = *(const f32x4*)(mq + k * (6 * D_) + 3 * D_); }
    }
    const int t2 = (b - 128) * 3 + wv - 4;
    const bool do2 = !three && wv >= 4 && wv < 7 && t2 < 270;
    if (do2) {
#pragma unroll
      for (int i = 0; i < 8; ++i) accC[i] = *(const f32x4*)(w2 + (size_t)(32 * (t2 / 5) + 4 * i + g) * D_ + 128 * (t2 % 5) + 8 * r16);
    }
    f32x4 gbp;
#pragma unroll
    for (int k = 0; k < KSM; ++k) gbp[k] = modp[k * (6 * D_) + 5 * D_ + sl * 80 + (tid < 80 ? tid : 0)];
    __builtin_amdgcn_sched_barrier(0);
    __syncthreads();
    TS(1, 1);
    conv_mma(smA, w4, lane, accA, [&](int it) {
      if (it < 5 || (it >= 8 && it < 13)) {
        const int ks = it < 5 ? it : it - 3;
#pragma unroll
        for (int fb = 0; fb < 2; ++fb) af[ks][fb] = ld_frag(rs, l16, w1o + (fb * 20 + ks) * 1024);
      }
    });
    if (three && hw == 0) conv_mma(smC, w4, lane, accC, [](int) {});
    if (tid < 80) ((float*)(smem + MN_GB))[tid] = rb((gbp[0] + gbp[1]) + (gbp[2] + gbp[3]));
    if (do2) {
      char* buf = smC + 4096 + (wv - 4) * 8192;
#pragma unroll
      for (int i = 0; i < 8; ++i) {
        const int row = 4 * i + g;
        *(f32x4*)(buf + row * 256 + (((r16 >> 1) ^ rl_swz(row)) << 5) + ((r16 & 1) << 4)) = accC[i];
      }
      wave_sync();
      const int row0 = 4 * g + q, row1 = row0 + 16;
#pragma unroll
      for (int cb = 0; cb < 8; ++cb) {
        const s16x4 lo = tr_read(buf + row0 * 256 + ((cb ^ rl_swz(row0)) << 5) + 8 * p);
        const s16x4 hi = tr_read(buf + row1 * 256 + ((cb ^ rl_swz(row1)) << 5) + 8 * p);
        st_sc1(rs, l16, (u32)WS_W2F + ((8 * (t2 % 5) + cb) * 54 + t2 / 5) * 1024, __builtin_bit_cast(u32x4, cat8(lo, hi)));
      }
    }
    if (do1) {
      char* buf = smC + wv * 1024;
      const f32x4 mc = (accC[0] + accC[1]) + (accC[2] + accC[3]), ms = (accC[4] + accC[5]) + (accC[6] + accC[7]);
      if (lane < 40) {
        *(u32x2*)(buf + lane * 8) = u32x2{cvtpk(1.f + rb(mc[0]), 1.f + rb(mc[1])), cvtpk(1.f + rb(mc[2]), 1.f + rb(mc[3]))};
        *(u32x2*)(buf + 512 + lane * 8) = u32x2{cvtpk(ms[0], ms[1]), cvtpk(ms[2], ms[3])};
      }
      wave_sync();
      float S = 0.f, B = 0.f;
#pragma unroll
      for (int i = 0; i < 5; ++i) {
        const u32x4 c1p = *(const u32x4*)(buf + i * 64 + g * 16), sbp = *(const u32x4*)(buf + 512 + i * 64 + g * 16);
#pragma unroll
        for (int k = 0; k < 4; ++k) { S = dot2bf(rv[i][k], c1p[k], S); B = dot2bf(rv[i][k], sbp[k], B); }
      }
      S += __shfl_xor(S, 16); S += __shfl_xor(S, 32);
      B += __shfl_xor(B, 16); B += __shfl_xor(B, 32);
      if (lane < 32)
        __builtin_amdgcn_raw_buffer_store_b32(__float_as_uint(lane < 16 ? S : B), rs, (g * 4 * F_ + r16) * 4, (u32)WS_SB + ((t1 & 3) * F_ + 16 * (t1 >> 2)) * 4, 16);
    }
    __syncthreads();
    TS(1, 2);
    conv_put(smA, w4, lane, accA);
    if (three && hw == 0) conv_put(smC, w4, lane, accC);
    __syncthreads();
    u32x4 os;
    st_sc1(rs, t256 * 16, (u32)WS_X1T + dA * (L_ * 2), conv_fin(smA, t256, xA, dskA, gaA, c1A, os));
    st_sc1(rs, t256 * 16, (u32)WS_X1S + dA * (L_ * 2), os);
    if (three && hw == 1) {
      st_sc1(rs, t256 * 16, (u32)WS_X1T + dC * (L_ * 2), conv_fin(smC, t256, c1, dskC, gaC, c1C, os));
      st_sc1(rs, t256 * 16, (u32)WS_X1S + dC * (L_ * 2), os);
    }
  }
  asm volatile("s_waitcnt vmcnt(0)" ::: "memory");
  __syncthreads();
  TS(1, 3);
  if (wv == 7) {
    u32 o = 0;
    if (lane == 0) o = __hip_atomic_fetch_add(cnt + (b & 7) * 64, 1u, __ATOMIC_RELAXED, __HIP_MEMORY_SCOPE_AGENT);
    o = __builtin_amdgcn_readfirstlane(o);
    if (o == 31u && lane < 8) __hip_atomic_store(cnt + (8 + lane) * 64 + (b & 7), 1u, __ATOMIC_RELAXED, __HIP_MEMORY_SCOPE_AGENT);
    for (int spins = 0; spins < (1 << 20); ++spins) {
      u32 v = 1u;
      if (lane < 8) v = __hip_atomic_load(cnt + (8 + (b & 7)) * 64 + lane, __ATOMIC_RELAXED, __HIP_MEMORY_SCOPE_AGENT);
      if (__all(v != 0u)) break;
      __builtin_amdgcn_s_sleep(1);
    }
  }
  __syncthreads();
  TS(1, 4);

  const int k0 = 7 * wv - (wv >> 2), nk = (wv & 3) == 3 ? 6 : 7;
  const u32 w2o = (u32)WS_W2F + (u32)((sl * 5) * 54 + k0) * 1024;
  bf16x8 wf[7][5];
  {
    const int l8 = tid & 7, crow = tid >> 3;
    u32x4 xv[12];
    f32x4 sb4;
    f32x4 acc[2][4];
#pragma unroll
    for (int fb = 0; fb < 2; ++fb)
#pragma unroll
      for (int tb = 0; tb < 4; ++tb) acc[fb][tb] = f32x4{0.f, 0.f, 0.f, 0.f};
    if (wv < 7) {
#pragma unroll
      for (int qq = 0; qq < 12; ++qq)
        xv[qq] = ld_sc1(rs, (qq < 11 || crow < 24 ? crow : 0) * (L_ * 2) + l8 * 16, (u32)WS_X1S + qq * (56 * L_ * 2) + tt * 128);
      __builtin_amdgcn_sched_barrier(0);
#pragma unroll
      for (int qq = 0; qq < 12; ++qq) {
        const int c = crow + 56 * qq;
        if (qq < 11 || crow < 24) *(u32x4*)(smem + M1_A + c * 128 + (((l8 >> 1) ^ h2_swz(c)) << 5) + ((l8 & 1) << 4)) = xv[qq];
      }
    } else {
      __builtin_amdgcn_s_setprio(3);
#pragma unroll
      for (int i = 0; i < 10; ++i) af[i >> 1][i & 1] = ld_frag1(rs, (lane >> 3) * (L_ * 2) + (lane & 7) * 16, (u32)WS_X1T + (sl * 80 + 8 * i) * (L_ * 2) + tt * 128);
    }
    __syncthreads();
    TS(1, 5);
    TC(3, 0);
    const int row0 = 8 * g + q, sw = h2_swz(row0);
    const char* rb0 = smem + M1_A + row0 * 128 + 8 * p;
    float* st = (float*)(smem + M1_ST);
    auto ldb = [&](int ks, bf16x8 (&bfr)[4]) {
#pragma unroll
      for (int tb = 0; tb < 4; ++tb) {
        const s16x4 lo = tr_read(rb0 + ks * 4096 + ((tb ^ sw) << 5));
        const s16x4 hi = tr_read(rb0 + ks * 4096 + 512 + ((tb ^ sw) << 5));
        bfr[tb] = cat8(lo, hi);
      }
    };
    bf16x8 bb[2][4];
    ldb(0, bb[0]);
#pragma unroll
    for (int ks = 0; ks < 20; ++ks) {
      if (active) {
        if (ks < 19) ldb(ks + 1, bb[(ks + 1) & 1]);
#pragma unroll
        for (int fb = 0; fb < 2; ++fb)
#pragma unroll
          for (int tb = 0; tb < 4; ++tb) acc[fb][tb] = mfma16(af[ks % W1_RING][fb], bb[ks & 1][tb], acc[fb][tb]);
        if (ks + W1_RING < 20) {
#pragma unroll
          for (int fb = 0; fb < 2; ++fb) af[ks % W1_RING][fb] = ld_frag(rs, l16, w1o + (fb * 20 + ks + W1_RING) * 1024);
        }
      } else if (ks == 0 && wv == 7) {
#pragma unroll
        for (int i = 0; i < 10; ++i) {
          const u32x4 v = __builtin_bit_cast(u32x4, af[i >> 1][i & 1]);
          const f32x4 a0 = {lo2f(v[0]), hi2f(v[0]), lo2f(v[1]), hi2f(v[1])}, a1 = {lo2f(v[2]), hi2f(v[2]), lo2f(v[3]), hi2f(v[3])};
          acc[0][0] += a0; acc[0][1] += a1; acc[0][2] += a0 * a0; acc[0][3] += a1 * a1;
        }
        const bool b5 = lane & 32, b4 = lane & 16, b3 = lane & 8;
        float v4[2][4], v2[2][2], v1[2];
#pragma unroll
        for (int h = 0; h < 2; ++h) {
#pragma unroll
          for (int j = 0; j < 4; ++j) {
            const float lo = acc[0][2 * h][j], hi = acc[0][2 * h + 1][j];
            v4[h][j] = (b5 ? hi : lo) + __shfl_xor(b5 ? lo : hi, 32);
          }
#pragma unroll
          for (int j = 0; j < 2; ++j) v2[h][j] = (b4 ? v4[h][2 + j] : v4[h][j]) + __shfl_xor(b4 ? v4[h][j] : v4[h][2 + j], 16);
          v1[h] = (b3 ? v2[h][1] : v2[h][0]) + __shfl_xor(b3 ? v2[h][0] : v2[h][1], 8);
        }
        const u32 toff = (8 * (lane & 7) + (lane >> 3)) * 8;
        __builtin_amdgcn_raw_buffer_store_b64(u32x2{__float_as_uint(v1[0]), __float_as_uint(v1[1]) | 0x80000000u}, rs, toff,
                                              (u32)WS_ST + (tt * 8 + sl) * 512, 16);
#pragma unroll
        for (int i = 0; i < 10; ++i) {
          const u32x4 v = __builtin_bit_cast(u32x4, af[i >> 1][i & 1]);
          char* dp = smem + M2_X + 8 * (lane & 7) * M2_XS + 2 * (8 * i + (lane >> 3));
#pragma unroll
          for (int k = 0; k < 4; ++k) {
            *(u16*)(dp + (2 * k) * M2_XS) = (u16)(v[k] & 0xffffu);
            *(u16*)(dp + (2 * k + 1) * M2_XS) = (u16)(v[k] >> 16);
          }
        }
        u32x2 gv[8];
        for (int spins = 0; spins < (1 << 20); ++spins) {
          u32 ok = 0x80000000u;
#pragma unroll
          for (int j = 0; j < 8; ++j) { gv[j] = __builtin_amdgcn_raw_buffer_load_b64(rs, lane * 8, (u32)WS_ST + (tt * 8 + j) * 512, 16); ok &= gv[j][1]; }
          if (__all(ok != 0u)) break;
          __builtin_amdgcn_s_sleep(2);
        }
        float sm = 0.f, sq = 0.f;
#pragma unroll
        for (int j = 0; j < 8; ++j) { sm += __uint_as_float(gv[j][0]); sq += __uint_as_float(gv[j][1] & 0x7fffffffu); }
        const float mean = sm * (1.f / D_);
        const float var = fmaxf(sq * (1.f / D_) - mean * mean, 0.f);
        st[lane] = rb(mean);
        st[64 + lane] = 1.f / rb(sqrtf(rb(rb(var) + 1e-5f)));
        __builtin_amdgcn_s_setprio(0);
      }
      if (ks >= 10) {
#pragma unroll
        for (int j = 0; j < 2; ++j) {
          const int f = 2 * (ks - 10) + j;
          wf[f / 5][f % 5] = LD_W2(rs, l16, w2o + ((f % 5) * 54 + f / 5) * 1024);
        }
      }
      if (ks == 9) {
        sb4 = __builtin_bit_cast(f32x4, ld_sc1(rs, ((lane >> 3) * F_ + 4 * (lane & 7)) * 4, (u32)WS_SB + 32 * p0 * 4));
      }
      if (ks == 17) {
#pragma unroll
        for (int e = 0; e < 4; ++e) { sb4[e] += __shfl_xor(sb4[e], 8); sb4[e] += __shfl_xor(sb4[e], 16); }
        if ((lane & 24) == 0) *(f32x4*)(smem + MN_SB + wv * 256 + (lane >> 5) * 128 + (lane & 7) * 16) = sb4;
      }
      __builtin_amdgcn_sched_barrier(0);
    }
    TS(2, 1);
    TC(3, 1);
#pragma unroll
    for (int i = W2_MID; i < 7; ++i)
#pragma unroll
      for (int cb = 0; cb < 5; ++cb) wf[i][cb] = LD_W2(rs, l16, w2o + (cb * 54 + (i < 6 || nk == 7 ? i : 5)) * 1024);
    __builtin_amdgcn_sched_barrier(0);
    __syncthreads();
    TS(2, 2);
    if (active) {
      const float* sbp = (const float*)(smem + MN_SB) + wv * 64 + 4 * g;
      const u32 hoff = (u32)WS_HID + (u32)((tt * 4) * 54 + p0) * 1024;
#pragma unroll
      for (int tb = 0; tb < 4; ++tb) {
        const float mb = st[tb * 16 + r16], rsd = st[64 + tb * 16 + r16];
        u32x4 v;
#pragma unroll
        for (int hh = 0; hh < 2; ++hh) {
          const f32x4 sv = *(const f32x4*)(sbp + 16 * hh), bv = *(const f32x4*)(sbp + 32 + 16 * hh);
          const f32x4 a = (acc[hh][tb] - mb * sv) * rsd + bv;
          v[2 * hh] = cvtpk(fmaxf(a[0], 0.f), fmaxf(a[1], 0.f));
          v[2 * hh + 1] = cvtpk(fmaxf(a[2], 0.f), fmaxf(a[3], 0.f));
        }
        st_sc1(rs, l16, hoff + tb * 54 * 1024, v);
      }
    }
  }
  TS(2, 3);
  asm volatile("s_waitcnt vmcnt(0)" ::: "memory");
  __syncthreads();
  TS(2, 4);
  if (tid == 448) __hip_atomic_store(cnt + (16 + tt * 8 + sl) * 64, 1u, __ATOMIC_RELAXED, __HIP_MEMORY_SCOPE_AGENT);
  __builtin_amdgcn_sched_barrier(0);
  poll_words<1>(cnt + (16 + tt * 8 + wv) * 64, 1u, lane);
  TS(1, 6);

  {
    const u32 hoff = (u32)WS_HID + (u32)((tt * 4) * 54 + k0) * 1024;
    u32x4 hf[2][4];
#pragma unroll
    for (int i = 0; i < 2; ++i)
#pragma unroll
      for (int tb = 0; tb < 4; ++tb) hf[i][tb] = ld_sc1(rs, l16, hoff + (tb * 54 + i) * 1024);
    f32x4 acc[5][4];
#pragma unroll
    for (int cb = 0; cb < 5; ++cb)
#pragma unroll
      for (int tb = 0; tb < 4; ++tb) acc[cb][tb] = f32x4{0.f, 0.f, 0.f, 0.f};
#pragma unroll
    for (int i = 0; i < 7; ++i) {
      if (i < 6 || nk == 7) {
#pragma unroll
        for (int tb = 0; tb < 4; ++tb)
#pragma unroll
          for (int cb = 0; cb < 5; ++cb) acc[cb][tb] = mfma16(wf[i][cb], __builtin_bit_cast(bf16x8, hf[i & 1][tb]), acc[cb][tb]);
      }
      __builtin_amdgcn_sched_barrier(0);
      if (i + 2 < 7) {
        const int kk = (i + 2 < 6 || nk == 7) ? i + 2 : 5;
#pragma unroll
        for (int tb = 0; tb < 4; ++tb) hf[i & 1][tb] = ld_sc1(rs, l16, hoff + (tb * 54 + kk) * 1024);
        __builtin_amdgcn_sched_barrier(0);
      }
    }
    TS(2, 5);
#pragma unroll
    for (int tb = 0; tb < 4; ++tb)
#pragma unroll
      for (int cb = 0; cb < 5; ++cb) {
        const int owner = tb + (cb < 3 ? 0 : 4);
        if (owner != wv) {
          const int slot = wv < owner ? wv : wv - 1;
          *(f32x4*)(smem + M2_RED + (((tb * 5 + cb) * 7 + slot) * 64 + lane) * 16) = acc[cb][tb];
        }
      }
    f32x4 own[3];
#pragma unroll
    for (int o = 0; o < 8; ++o) {
      if (wv == o) {
        asm volatile("" ::: "memory");
#pragma unroll
        for (int j = 0; j < 3; ++j) own[j] = acc[o < 4 ? j : (j < 2 ? 3 + j : 4)][o & 3];
      }
    }
    __syncthreads();
    TS(2, 6);
    const int tok = 16 * w4 + r16, cb0 = wv < 4 ? 0 : 3;
    f32x4 pr[3][7], gb4[3];
    u32x2 xr[3];
#pragma unroll
    for (int j = 0; j < 3; ++j) {
      const int cb = (j < 2 || wv < 4) ? cb0 + j : 4;
#pragma unroll
      for (int s7 = 0; s7 < 7; ++s7) pr[j][s7] = *(const f32x4*)(smem + M2_RED + (((w4 * 5 + cb) * 7 + s7) * 64 + lane) * 16);
      gb4[j] = *(const f32x4*)(smem + MN_GB + (16 * cb + 4 * g) * 4);
      xr[j] = *(const u32x2*)(smem + M2_X + tok * M2_XS + 2 * (16 * cb + 4 * g));
    }
    __builtin_amdgcn_sched_barrier(0);
#pragma unroll
    for (int j = 0; j < 3; ++j) {
      const f32x4 y = ((own[j] + pr[j][0]) + (pr[j][1] + pr[j][2])) + ((pr[j][3] + pr[j][4]) + (pr[j][5] + pr[j][6]));
      const u32 y0 = cvtpk(y[0], y[1]), y1 = cvtpk(y[2], y[3]);
      const u32 v0 = cvtpk(lo2f(y0) * gb4[j][0], hi2f(y0) * gb4[j][1]), v1 = cvtpk(lo2f(y1) * gb4[j][2], hi2f(y1) * gb4[j][3]);
      u32x2 o;
      o[0] = cvtpk(lo2f(xr[j][0]) + lo2f(v0), hi2f(xr[j][0]) + hi2f(v0));
      o[1] = cvtpk(lo2f(xr[j][1]) + lo2f(v1), hi2f(xr[j][1]) + hi2f(v1));
      if (j < 2 || wv < 4) *(u32x2*)(out + (size_t)(tt * 64 + tok) * D_ + sl * 80 + 16 * (cb0 + j) + 4 * g) = o;
    }
  }
  TS(1, 7);
}

extern "C" void kernel_launch(void* const* d_in, const int* in_sizes, int n_in, void* d_out, int out_size, void* d_ws,
                              size_t ws_size, hipStream_t stream) {
  char* ws = (char*)d_ws;
  k_pre<<<NB_LN + NB_KG + NB_MOD + NB_RL, 256, 0, stream>>>(
      (const u16*)d_in[0], (const u16*)d_in[1], (const u16*)d_in[2], (const u16*)d_in[3], (const u16*)d_in[4],
      (const u16*)d_in[5], (const u16*)d_in[6], (const u16*)d_in[7], (const u16*)d_in[10], (const u16*)d_in[11],
      (const u16*)d_in[12], (const u16*)d_in[13], ws);
  k_main<<<256, 512, 0, stream>>>((const u16*)d_in[8], (const u16*)d_in[11], ws, (u16*)d_out);
}
```

```cpp
#include <hip/hip_runtime.h>
#include <hip/hip_bf16.h>
#include <stdint.h>

extern "C" __device__ unsigned long long llvm_dispatch_id() __asm("llvm.amdgcn.dispatch.id");
typedef unsigned short u16;
typedef unsigned int u32;
typedef __attribute__((ext_vector_type(8))) short bf16x8;
typedef __attribute__((ext_vector_type(4))) short s16x4;
typedef __attribute__((ext_vector_type(4))) float f32x4;
typedef __attribute__((ext_vector_type(2))) float f32x2;
typedef __attribute__((ext_vector_type(4))) unsigned int u32x4;
typedef __attribute__((ext_vector_type(2))) unsigned int u32x2;
typedef __attribute__((ext_vector_type(3))) unsigned int u32x3;
typedef __bf16 bf16x2_t __attribute__((ext_vector_type(2)));
#define LDS_AS __attribute__((address_space(3)))
#define DI __device__ __forceinline__

constexpr int L_ = 2048, D_ = 640, N_ = 128, F_ = 1728;
constexpr int KSM = 4;

constexpr size_t WS_MODP = 0;
constexpr size_t WS_CNT = 126976;
constexpr size_t WS_FL = 196608;
constexpr size_t WS_NT = 262144;
constexpr size_t WS_XT = WS_NT + (size_t)D_ * L_ * 2;
constexpr size_t WS_X1T = WS_XT + (size_t)D_ * L_ * 2;
constexpr size_t WS_X1S = WS_X1T + (size_t)D_ * L_ * 2;
constexpr int KTAB = 1296;
constexpr size_t WS_KT = WS_X1S + (size_t)D_ * L_ * 2;
constexpr size_t WS_SB = WS_KT + (size_t)D_ * KTAB;
constexpr size_t WS_W1F = WS_SB + 2 * 20 * 1728 * 4;
constexpr size_t WS_W2F = WS_W1F + (size_t)108 * 20 * 1024;
constexpr size_t WS_HID = WS_W2F + (size_t)40 * 54 * 1024;
constexpr size_t WS_ST = WS_HID + (size_t)128 * 54 * 1024;

#define TS(kern, slot) do {} while (0)
#define TC(kern, slot) do {} while (0)

DI void st_wt(void* p, u32x4 v) { asm volatile("global_store_dwordx4 %0, %1, off sc1\n\ts_nop 1" ::"v"(p), "v"(v) : "memory"); }
#define ST_PRE(p, v) (*(p) = (v))
#define ST_PREW(p, v) (*(p) = (v))
DI u32 cvtpk(float lo, float hi) {
  f32x2 v = {lo, hi};
  bf16x2_t r = __builtin_convertvector(v, bf16x2_t);
  return __builtin_bit_cast(u32, r);
}
typedef _Float16 f16x2_t __attribute__((ext_vector_type(2)));
typedef _Float16 f16x8_t __attribute__((ext_vector_type(8)));
DI u32 cvtph(float lo, float hi) {
  f32x2 v = {lo, hi};
  f16x2_t r = __builtin_convertvector(v, f16x2_t);
  return __builtin_bit_cast(u32, r);
}
DI float dot2bf(u32 a, u32 b, float c) {
  return __builtin_amdgcn_fdot2_f32_bf16(__builtin_bit_cast(bf16x2_t, a), __builtin_bit_cast(bf16x2_t, b), c, false);
}
DI float lo2f(u32 v) { return __uint_as_float(v << 16); }
DI float hi2f(u32 v) { return __uint_as_float(v & 0xffff0000u); }
DI float bf2f(u16 v) { return __uint_as_float(((u32)v) << 16); }
DI float rb(float x) { return lo2f(cvtpk(x, x)); }
DI f32x4 mfma16(bf16x8 a, bf16x8 b, f32x4 c) { return __builtin_amdgcn_mfma_f32_16x16x32_bf16(a, b, c, 0, 0, 0); }
DI s16x4 tr_read(const char* p) {
  return __builtin_amdgcn_ds_read_tr16_b64_v4i16((LDS_AS s16x4*)p);
}
DI bf16x8 cat8(s16x4 a, s16x4 b) { return __builtin_shufflevector(a, b, 0, 1, 2, 3, 4, 5, 6, 7); }
DI void wave_sync() {
  __builtin_amdgcn_fence(__ATOMIC_RELEASE, "wavefront");
  __builtin_amdgcn_wave_barrier();
  __builtin_amdgcn_fence(__ATOMIC_ACQUIRE, "wavefront");
}

DI f32x2 cmul2(f32x2 a, f32x2 b) {
  const f32x2 bj = {-b.y, b.x};
  return __builtin_elementwise_fma(a.yy, bj, a.xx * b);
}
struct c32 { float r, i; };
DI c32 cmul(c32 a, c32 b) { return {a.r * b.r - a.i * b.i, a.r * b.i + a.i * b.r}; }
DI c32 cexp_scaled(float ar, float ai, float e) {
  const float mag = __expf(ar * e);
  const float x = ai * e;
  const float C_HI = 0.15915494f, C_LO = 6.4206382e-9f;
  const float r = x * C_HI;
  const float rl = fmaf(x, C_HI, -r) + x * C_LO;
  const float fr = (r - floorf(r)) + rl;
  return {mag * __builtin_amdgcn_cosf(fr), mag * __builtin_amdgcn_sinf(fr)};
}
DI int tap_index(int m) {
  u32 bits = __float_as_uint((float)m);
  bits += 0x7fffu + ((bits >> 16) & 1u);
  const int id2 = (int)(bits >> 16) - (0x4300 - 128);
  return m < 128 ? m : id2;
}

DI int rl_swz(int row) { return (row & 3) | ((((row >> 2) ^ (row >> 3)) & 1) << 2); }

constexpr int CV_H0 = 0, CV_HS = 4160, CV_K = 12544, CV_T = 21760, CV_H3 = 32768 + 192, CV_CH = 37376;
static_assert(CV_CH % 256 == 0 && CV_H3 + 4160 <= CV_CH && CV_T + 4 * KTAB <= 32768, "conv lds");
constexpr int M1_A = 0, M1_ST = 81920;
constexpr int MN_SB = 3 * CV_CH;
constexpr int MN_R1 = MN_SB + 2048;
constexpr int M2_RED = 0, M2_X = 143360, M2_XS = 176;
constexpr int MN_GB = M2_X + 64 * M2_XS;
constexpr int MN_PC = MN_GB + 320;
constexpr int MN_SMEM = MN_PC + 16;
static_assert(MN_R1 + 5 * 4864 <= M2_X && M1_ST + 512 <= MN_SB, "mlp lds");

typedef __amdgpu_buffer_rsrc_t rsrc_t;
DI u32x4 ld_sc1(rsrc_t r, u32 voff, u32 soff) { return __builtin_amdgcn_raw_buffer_load_b128(r, voff, soff, 16); }
DI void st_sc1(rsrc_t r, u32 voff, u32 soff, u32x4 v) { __builtin_amdgcn_raw_buffer_store_b128(v, r, voff + soff, 0, 16); }
DI bf16x8 ld_frag1(rsrc_t r, u32 voff, u32 soff) { return __builtin_bit_cast(bf16x8, __builtin_amdgcn_raw_buffer_load_b128(r, voff, soff, 16)); }
DI bf16x8 ld_frag(rsrc_t r, u32 voff, u32 soff) { return __builtin_bit_cast(bf16x8, __builtin_amdgcn_raw_buffer_load_b128(r, voff, soff, 0)); }

DI int h2_swz(int k) { return ((k >> 1) & 1) | (((k >> 3) & 1) << 1); }

DI void pair_sync(u32* c, u32 target) {
  __builtin_amdgcn_fence(__ATOMIC_RELEASE, "workgroup");
  if ((threadIdx.x & 63) == 0) __hip_atomic_fetch_add(c, 1u, __ATOMIC_RELAXED, __HIP_MEMORY_SCOPE_WORKGROUP);
  for (int spins = 0; spins < (1 << 22); ++spins) {
    if (__hip_atomic_load(c, __ATOMIC_RELAXED, __HIP_MEMORY_SCOPE_WORKGROUP) >= target) break;
    __builtin_amdgcn_s_sleep(1);
  }
  __builtin_amdgcn_fence(__ATOMIC_ACQUIRE, "workgroup");
}

DI void kgen_pair(int d, int hi, int lane, const u16* __restrict__ Are, const u16* __restrict__ Aim, const u16* __restrict__ Bre,
                  const u16* __restrict__ Bim, const u16* __restrict__ Cre, const u16* __restrict__ Cim,
                  const u16* __restrict__ ldt, char* smem, u32* pc) {
  const int r16 = lane & 15, g = lane >> 4, n = hi * 64 + lane;
  const int gi = d * N_ + n;
  const float dt = rb(expf(bf2f(ldt[d])));
  const float ar = dt * bf2f(Are[gi]), ai = dt * bf2f(Aim[gi]);
  const c32 dB = {dt * bf2f(Bre[gi]), dt * bf2f(Bim[gi])};
  const c32 Cc = {bf2f(Cre[gi]), bf2f(Cim[gi])};
  const c32 W = cmul(Cc, dB);
  const c32 E1 = cexp_scaled(ar, ai, 1.f), E8 = cexp_scaled(ar, ai, 8.f), E128 = cexp_scaled(ar, ai, 128.f);

  float wmax = fmaxf(fabsf(W.r), fabsf(W.i));
#pragma unroll
  for (int o = 1; o < 64; o <<= 1) wmax = fmaxf(wmax, __shfl_xor(wmax, o));
  int ew = (int)((__float_as_uint(wmax) >> 23) & 0xff);
  ew = ew < 20 ? 20 : (ew > 240 ? 240 : ew);
  const float wsc = __uint_as_float((u32)(127 + 10 + 127 - ew - 1) << 23);
  const float unsc = __uint_as_float((u32)(ew + 1 - 20) << 23);

  char* buf = smem + hi * 6144;
  char* wr = buf + lane * 4;
  const char* rd = buf + r16 * 272 + g * 16;
  auto put = [&](int row, f32x2 v) { *(u32*)(wr + row * 272) = cvtph(v.x, v.y); };
  {
    const f32x2 e8 = {E8.r, E8.i};
    f32x2 p = {1024.f, 0.f};
#pragma unroll
    for (int jj = 0; jj < 16; ++jj) { put(jj, p); if (jj < 15) p = cmul2(p, e8); }
  }
  wave_sync();
  f16x8_t bh[4];
#pragma unroll
  for (int ks = 0; ks < 4; ++ks) bh[ks] = *(const f16x8_t*)(rd + ks * 64);
  wave_sync();
  const f32x2 e1 = {E1.r, -E1.i}, e128 = {E128.r, -E128.i};
  f32x2 sm[8];
  sm[1] = e1;
#pragma unroll
  for (int i = 2; i < 8; ++i) sm[i] = cmul2(sm[i - 1], e1);
  f32x2 WG[17];
  WG[0] = f32x2{W.r * wsc, -W.i * wsc};
#pragma unroll
  for (int i = 1; i < 17; ++i) WG[i] = cmul2(WG[i - 1], e128);
  f32x4 acc[3];
  auto tile_mma = [&](int t) {
    wave_sync();
    f32x4 a = {0.f, 0.f, 0.f, 0.f};
#pragma unroll
    for (int ks = 0; ks < 4; ++ks) a = __builtin_amdgcn_mfma_f32_16x16x32_f16(*(const f16x8_t*)(rd + ks * 64), bh[ks], a, 0, 0, 0);
    acc[t] = a * unsc;
    wave_sync();
  };
#pragma unroll
  for (int i = 0; i < 2; ++i)
#pragma unroll
    for (int rho = 0; rho < 8; ++rho) put(8 * i + rho, rho ? cmul2(sm[rho], WG[i]) : WG[i]);
  tile_mma(0);
#pragma unroll
  for (int i = 0; i < 2; ++i)
#pragma unroll
    for (int rho = 0; rho < 4; ++rho) put(4 * i + rho, rho ? cmul2(sm[2 * rho], WG[2 + i]) : WG[2 + i]);
#pragma unroll
  for (int i = 0; i < 4; ++i)
#pragma unroll
    for (int rho = 0; rho < 2; ++rho) put(8 + 2 * i + rho, rho ? cmul2(sm[4], WG[4 + i]) : WG[4 + i]);
  tile_mma(1);
#pragma unroll
  for (int i = 0; i < 9; ++i) put(i, WG[8 + i]);
  tile_mma(2);

#pragma unroll
  for (int t = 0; t < 3; ++t) *(f32x4*)(buf + t * 1024 + lane * 16) = acc[t];
  pair_sync(pc, 2u);
  u16* T = (u16*)(smem + CV_T);
  {
    const char* ob = smem + (hi ^ 1) * 6144 + lane * 16;
    if (!hi) {
      const f32x4 a0 = acc[0] + *(const f32x4*)(ob), a2 = acc[2] + *(const f32x4*)(ob + 2048);
      u16* t0 = T + 128 * (g >> 1) + 4 * (g & 1) + 8 * r16;
      *(u32x2*)t0 = u32x2{cvtpk(a0[0], a0[1]), cvtpk(a0[2], a0[3])};
      if (g < 2) {
#pragma unroll
        for (int rg = 0; rg < 4; ++rg) T[512 + 16 * (4 * g + rg) + r16] = (u16)cvtpk(a2[rg], 0.f);
      } else if (lane == 32) {
        T[640] = (u16)cvtpk(a2[0], 0.f);
      } else if (lane >= 48 && lane < 55) {
        T[641 + lane - 48] = 0;
      }
    } else {
      const f32x4 a1 = acc[1] + *(const f32x4*)(ob + 1024);
      if (g < 2) {
        *(u32x2*)(T + 256 + 64 * g + 4 * r16) = u32x2{cvtpk(a1[0], a1[1]), cvtpk(a1[2], a1[3])};
      } else {
        *(u32*)(T + 384 + 64 * (g - 2) + 2 * r16) = cvtpk(a1[0], a1[1]);
        *(u32*)(T + 384 + 64 * (g - 2) + 32 + 2 * r16) = cvtpk(a1[2], a1[3]);
      }
    }
  }
  pair_sync(pc, 4u);
#pragma unroll
  for (int i = 0; i < 3; ++i) {
    const int cp = n + 128 * i;
    if (cp < 284) {
      u32 e[9];
#pragma unroll
      for (int j = 0; j < 9; ++j) {
        const int m = 2047 - 8 * cp - j;
        e[j] = T[m < 0 ? 641 : tap_index(m)];
      }
      *(u32x4*)(smem + CV_K + 4576 + cp * 16) = u32x4{e[0] | (e[1] << 16), e[2] | (e[3] << 16), e[4] | (e[5] << 16), e[6] | (e[7] << 16)};
      *(u32x4*)(smem + CV_K + cp * 16) = u32x4{e[1] | (e[2] << 16), e[3] | (e[4] << 16), e[5] | (e[6] << 16), e[7] | (e[8] << 16)};
    }
  }
}

DI void ln1_pair(int t0, int t, const u16* __restrict__ x, rsrc_t rs, char* buf, u32* pc) {
  const int u = t >> 4, j = t & 15;
  const u32x4* xa = (const u32x4*)(x + (size_t)(t0 + 2 * u) * D_);
  u32x4 A[5], B[5];
#pragma unroll
  for (int q = 0; q < 5; ++q) { A[q] = xa[j + 16 * q]; B[q] = xa[80 + j + 16 * q]; }
  char* wbase = buf + 9 * j * 32 + ((((u >> 2) ^ ((j >> 2) & 1)) << 2) + (u & 3)) * 4;
#pragma unroll
  for (int q = 0; q < 5; ++q)
#pragma unroll
    for (int k = 0; k < 4; ++k) {
      *(u32*)(wbase + (144 * q + 2 * k) * 32) = (A[q][k] & 0xffffu) | (B[q][k] << 16);
      *(u32*)(wbase + (144 * q + 2 * k + 1) * 32) = (A[q][k] >> 16) | (B[q][k] & 0xffff0000u);
    }
  float sa = 0.f, sb = 0.f, qa = 0.f, qb = 0.f;
#pragma unroll
  for (int q = 0; q < 5; ++q)
#pragma unroll
    for (int k = 0; k < 4; ++k) {
      sa = dot2bf(A[q][k], 0x3f803f80u, sa); qa = dot2bf(A[q][k], A[q][k], qa);
      sb = dot2bf(B[q][k], 0x3f803f80u, sb); qb = dot2bf(B[q][k], B[q][k], qb);
    }
#pragma unroll
  for (int o = 1; o < 16; o <<= 1) {
    sa += __shfl_xor(sa, o, 16); sb += __shfl_xor(sb, o, 16); qa += __shfl_xor(qa, o, 16); qb += __shfl_xor(qb, o, 16);
  }
  const float ma = sa * (1.f / D_), mb = sb * (1.f / D_);
  const float va = fmaxf(qa - sa * ma, 0.f), vb = fmaxf(qb - sb * mb, 0.f);
  if (j == 0) {
    const u32 s0 = cvtpk(ma, sqrtf(rb(rb(va * (1.f / D_)) + 1e-5f))), s1 = cvtpk(mb, sqrtf(rb(rb(vb * (1.f / D_)) + 1e-5f)));
    __builtin_amdgcn_raw_buffer_store_b64(u32x2{s0, s1}, rs, (u32)WS_NT + (t0 + 2 * u) * 4, 0, 16);
  }
  pair_sync(pc, 2u);
#pragma unroll
  for (int i = 0; i < 10; ++i) {
    const int id = t + 128 * i, c = id >> 1, o = id & 1;
    const u32x4 v = *(const u32x4*)(buf + (c + (c >> 3)) * 32 + ((o ^ ((c >> 5) & 1)) << 4));
    st_sc1(rs, c * (L_ * 2) + o * 16, (u32)WS_XT + t0 * 2, v);
  }
}

DI void mod_unit(int un, int lane, const u16* __restrict__ c, const u16* __restrict__ wm, rsrc_t rs) {
  const int cbk = un % 60, ks = un / 60, cj = lane & 7, rg = lane >> 3;
  u32x4 v[20];
  u16 cr[20];
#pragma unroll
  for (int i = 0; i < 20; ++i) {
    const int row = 160 * ks + rg + 8 * i;
    v[i] = *(const u32x4*)(wm + (size_t)row * (6 * D_) + 64 * cbk + 8 * cj);
    cr[i] = c[row];
  }
  float a[8];
#pragma unroll
  for (int k = 0; k < 8; ++k) a[k] = 0.f;
#pragma unroll
  for (int i = 0; i < 20; ++i) {
    const float cv = bf2f(cr[i]);
    const float sv = rb(cv / (1.f + __expf(-cv)));
#pragma unroll
    for (int k = 0; k < 4; ++k) { a[2 * k] += sv * lo2f(v[i][k]); a[2 * k + 1] += sv * hi2f(v[i][k]); }
  }
#pragma unroll
  for (int k = 0; k < 8; ++k) { a[k] += __shfl_xor(a[k], 8); a[k] += __shfl_xor(a[k], 16); a[k] += __shfl_xor(a[k], 32); }
  if (lane < 16) {
    const f32x4 o = rg ? f32x4{a[4], a[5], a[6], a[7]} : f32x4{a[0], a[1], a[2], a[3]};
    st_sc1(rs, (8 * cj + 4 * rg) * 4, (u32)WS_MODP + (ks * (6 * D_) + 64 * cbk) * 4, __builtin_bit_cast(u32x4, o));
  }
}

DI void w1_tile(int tile, int lane, const u16* __restrict__ w1, rsrc_t rs, char* buf) {
  const int ks = tile / 14, nb = tile % 14, nblk = nb == 13 ? 4 : 8;
  const int g = lane >> 4, lc = lane & 15, q = lc >> 2, p = lane & 3;
  u32x4 v[8];
#pragma unroll
  for (int i = 0; i < 8; ++i) v[i] = *(const u32x4*)(w1 + (size_t)(32 * ks + 4 * i + g) * F_ + 128 * nb + 8 * (lc < 2 * nblk ? lc : 0));
#pragma unroll
  for (int i = 0; i < 8; ++i) {
    const int row = 4 * i + g;
    *(u32x4*)(buf + row * 256 + (((lc >> 1) ^ rl_swz(row)) << 5) + ((lc & 1) << 4)) = v[i];
  }
  wave_sync();
  const int row0 = 8 * g + q, row1 = row0 + 4;
#pragma unroll
  for (int cb = 0; cb < 8; ++cb) {
    const s16x4 lo = tr_read(buf + row0 * 256 + ((cb ^ rl_swz(row0)) << 5) + 8 * p);
    const s16x4 hi = tr_read(buf + row1 * 256 + ((cb ^ rl_swz(row1)) << 5) + 8 * p);
    if (cb < nblk) st_sc1(rs, lane * 16, (u32)WS_W1F + ((8 * nb + cb) * 20 + ks) * 1024, __builtin_bit_cast(u32x4, cat8(lo, hi)));
  }
  wave_sync();
}

DI void conv_stage_h(char* smem, u32x4 xq0, u32x4 xq1, const float (&mn)[16], const float (&rs)[16], float ca1, float sa, int tid) {
  u32 h[8];
#pragma unroll
  for (int k = 1; k < 8; ++k) {
    const u32 xv = k < 4 ? xq0[k] : xq1[k - 4];
    const u32 c = cvtpk(lo2f(xv) - mn[2 * k], hi2f(xv) - mn[2 * k + 1]);
    const u32 n = cvtpk(lo2f(c) * rs[2 * k], hi2f(c) * rs[2 * k + 1]);
    const u32 p = cvtpk(lo2f(n) * ca1, hi2f(n) * ca1);
    const u32 hv = cvtpk(lo2f(p) + sa, hi2f(p) + sa);
    h[k] = (k >= 4 || tid > 0) ? hv : 0u;
  }
  const int yo = (tid + 3) * 16;
  *(u32x4*)(smem + CV_H0 + yo) = u32x4{h[1], h[2], h[3], h[4]};
  *(u32x4*)(smem + CV_H0 + CV_HS + yo) = u32x4{h[2], h[3], h[4], h[5]};
  *(u32x4*)(smem + CV_H0 + 2 * CV_HS + yo) = u32x4{h[3], h[4], h[5], h[6]};
  *(u32x4*)(smem + CV_H3 + yo) = u32x4{h[4], h[5], h[6], h[7]};
  if (tid < 12) {
    const int cc = tid / 3, yc = tid % 3;
    *(u32x4*)(smem + (cc == 3 ? CV_H3 : CV_H0 + cc * CV_HS) + yc * 16) = u32x4{0u, 0u, 0u, 0u};
  }
}

template <class CB>
DI void conv_mma(const char* smem, int w, int lane, f32x4 (&acc)[8], CB&& side) {
  const int r16 = lane & 15, g = lane >> 4, cpy = r16 & 3;
  const char* hb = smem + (cpy == 3 ? CV_H3 : CV_H0 + cpy * CV_HS) + 16 * ((r16 >> 2) + 4 * w + g);
  const char* kb = smem + CV_K + (r16 & 1) * 4576 + 16 * (252 + 4 * w - 4 * (r16 >> 1) + g) - 256 * 14;
#pragma unroll
  for (int i = 0; i < 8; ++i) acc[i] = f32x4{0.f, 0.f, 0.f, 0.f};
#pragma unroll
  for (int par = 1; par >= 0; --par) {
    bf16x8 Bf[8];
#pragma unroll
    for (int t = 0; t < 8; ++t) Bf[t] = *(const bf16x8*)(hb + 256 * (2 * t + par));
#pragma unroll
    for (int sg = -par; sg <= 14; sg += 2) {
      const bf16x8 Af = *(const bf16x8*)(kb + 256 * (14 - sg));
#pragma unroll
      for (int i = 0; i < 8; ++i) {
        const int tau = 2 * i - sg;
        if (tau >= 0 && tau <= 15) acc[i] = mfma16(Af, Bf[tau >> 1], acc[i]);
      }
      side((1 - par) * 8 + ((sg + par) >> 1));
    }
  }
}

DI void conv_put(char* smem, int w, int lane, const f32x4 (&acc)[8]) {
  char* pb = smem + w * 8192 + (64 * (lane >> 4) + 2 * (lane & 15)) * 4;
#pragma unroll
  for (int i = 0; i < 8; ++i) {
    *(f32x2*)(pb + i * 1024) = f32x2{acc[i][0], acc[i][1]};
    *(f32x2*)(pb + i * 1024 + 128) = f32x2{acc[i][2], acc[i][3]};
  }
}

DI u32x4 conv_fin(const char* smem, int t, u32x4 xv, float dsk, float ga, float c1, u32x4& os) {
  f32x4 y0 = *(const f32x4*)(smem + t * 32), y1 = *(const f32x4*)(smem + t * 32 + 16);
#pragma unroll
  for (int w = 1; w < 4; ++w) { y0 += *(const f32x4*)(smem + w * 8192 + t * 32); y1 += *(const f32x4*)(smem + w * 8192 + t * 32 + 16); }
  const u32x4 hv = *(const u32x4*)(smem + CV_H3 + 48 + t * 16);
  u32x4 o;
#pragma unroll
  for (int k = 0; k < 4; ++k) {
    const u32 yr = k < 2 ? cvtpk(y0[2 * k], y0[2 * k + 1]) : cvtpk(y1[2 * k - 4], y1[2 * k - 3]);
    const u32 hd = cvtpk(lo2f(hv[k]) * dsk, hi2f(hv[k]) * dsk);
    const u32 v = cvtpk(lo2f(yr) + lo2f(hd), hi2f(yr) + hi2f(hd));
    const u32 gv = cvtpk(lo2f(v) * ga, hi2f(v) * ga);
    o[k] = cvtpk(lo2f(xv[k]) + lo2f(gv), hi2f(xv[k]) + hi2f(gv));
    os[k] = cvtpk(lo2f(o[k]) * c1, hi2f(o[k]) * c1);
  }
  return o;
}

template <int NW>
DI void poll_words(const u32* cnt, u32 target, int lane) {
  for (int spins = 0; spins < (1 << 20); ++spins) {
    u32 v = target;
    if (lane < NW) v = __hip_atomic_load(cnt + lane * 64, __ATOMIC_RELAXED, __HIP_MEMORY_SCOPE_AGENT);
    if (__all(v >= target)) break;
    __builtin_amdgcn_s_sleep(1);
  }
}

#define LD_W2 ld_frag1
constexpr int W1_RING = 10;
constexpr int W2_MID = 4;

__global__ __launch_bounds__(512, 2) void k_main(const u16* __restrict__ x, const u16* __restrict__ Are, const u16* __restrict__ Aim,
                                                    const u16* __restrict__ Bre, const u16* __restrict__ Bim, const u16* __restrict__ Cre,
                                                    const u16* __restrict__ Cim, const u16* __restrict__ ldt, const u16* __restrict__ Dsk,
                                                    const u16* __restrict__ w1, const u16* __restrict__ w2, const u16* __restrict__ cvec,
                                                    const u16* __restrict__ wm, char* __restrict__ ws, u16* __restrict__ out) {
  __shared__ __attribute__((aligned(256))) char smem[MN_SMEM];
  const int tid = threadIdx.x, wv = __builtin_amdgcn_readfirstlane(tid >> 6), lane = tid & 63;
  const int r16 = lane & 15, g = lane >> 4, q = r16 >> 2, p = lane & 3;
  const int b = blockIdx.x, hw = wv >> 2, w4 = wv & 3, t256 = tid & 255;
  const rsrc_t rs = __builtin_amdgcn_make_buffer_rsrc((void*)ws, 0, 0x7fffffff, 0x00020000);
  u32* cnt = (u32*)(ws + WS_CNT);
  const int idx = b >> 3, tt = ((b & 7) >> 1) * 8 + (idx & 7), sl = (b & 1) * 4 + (idx >> 3);
  const int pbase = 7 * sl - (sl >> 2), pcnt = (sl & 3) == 3 ? 6 : 7;
  const bool active = wv < pcnt;
  const int p0 = pbase + (active ? wv : 0);
  TS(1, 0);

  const bool three = b < 128;
  const int d0 = three ? 3 * b : 384 + 2 * (b - 128);
  const u32 l16 = lane * 16;

  {
    u32* pcs = (u32*)(smem + MN_PC);
    if (tid < 4) pcs[tid] = 0u;
    u32* fl = (u32*)(ws + WS_FL);
    u32 tag = 0;
    if (wv >= 4) {
      const u32 ep = __hip_atomic_load(fl + 8192, __ATOMIC_RELAXED, __HIP_MEMORY_SCOPE_AGENT);
      const unsigned long long h = (llvm_dispatch_id() * 0x9E3779B97F4A7C15ull) ^ ((unsigned long long)__builtin_amdgcn_dispatch_ptr() * 0xC2B2AE3D27D4EB4Full);
      tag = ep + 1u + ((u32)(h >> 33) & 0x3fffffffu);
    }
    __syncthreads();
    const int nkg = three ? 6 : 4;
    if (wv < nkg) {
      kgen_pair(d0 + (wv >> 1), wv & 1, lane, Are, Aim, Bre, Bim, Cre, Cim, ldt, smem + (wv >> 1) * CV_CH, pcs + (wv >> 1));
    } else {
      if (three) {
        const int un = b * 2 + wv - 6;
        if (un < 240) mod_unit(un, lane, cvec, wm, rs);
      } else if (wv < 6) {
        ln1_pair((b - 128) * 16, tid - 256, x, rs, smem + 2 * CV_CH, pcs + 2);
      } else {
        const int tl = (b - 128) * 2 + wv - 6;
        w1_tile(tl, lane, w1, rs, smem + MN_R1 + (wv - 6) * 8192);
        if (tl + 256 < 280) w1_tile(tl + 256, lane, w1, rs, smem + MN_R1 + (wv - 6) * 8192);
      }
      if (wv == 7) {
        __builtin_amdgcn_raw_buffer_store_b64(u32x2{0u, 0u}, rs, lane * 8, (u32)WS_ST + (tt * 8 + sl) * 512, 16);
        if (lane == 0) __hip_atomic_store(cnt + (16 + tt * 8 + sl) * 64, 0u, __ATOMIC_RELAXED, __HIP_MEMORY_SCOPE_AGENT);
        if (b < 16 && lane < 8) __hip_atomic_store(cnt + b * 64 + lane, 0u, __ATOMIC_RELAXED, __HIP_MEMORY_SCOPE_AGENT);
      }
      asm volatile("s_waitcnt vmcnt(0)" ::: "memory");
      u32* mine = fl + ((b & 7) * 32 + idx) * 4;
      if (three) { if (lane < 2) __hip_atomic_store(mine + 2 * (wv - 6) + lane, tag, __ATOMIC_RELAXED, __HIP_MEMORY_SCOPE_AGENT); }
      else if (lane == 0) __hip_atomic_store(mine + wv - 4, tag, __ATOMIC_RELAXED, __HIP_MEMORY_SCOPE_AGENT);
      if (wv == 6 && b < 8) {
        for (int spins = 0; spins < (1 << 20); ++spins) {
          const u32x2 v = __builtin_amdgcn_raw_buffer_load_b64(rs, lane * 8, (u32)WS_FL + b * 512, 16);
          if (__all(v[0] == tag && v[1] == tag)) break;
          __builtin_amdgcn_s_sleep(1);
        }
        if (lane < 8) __hip_atomic_store(fl + 4096 + lane * 64 + b, tag, __ATOMIC_RELAXED, __HIP_MEMORY_SCOPE_AGENT);
      }
      if (wv == 7) {
        for (int spins = 0; spins < (1 << 20); ++spins) {
          u32 v = tag;
          if (lane < 8) v = __hip_atomic_load(fl + 4096 + (b & 7) * 64 + lane, __ATOMIC_RELAXED, __HIP_MEMORY_SCOPE_AGENT);
          if (__all(v == tag)) break;
          __builtin_amdgcn_s_sleep(1);
        }
        if (b == 0 && lane == 0) __hip_atomic_store(fl + 8192, tag, __ATOMIC_RELAXED, __HIP_MEMORY_SCOPE_AGENT);
      }
    }
    __syncthreads();
  }
  TS(3, 6);

  const int dA = d0 + hw, dC = three ? d0 + 2 : d0;
  bf16x8 af[W1_RING][2];
  const u32 w1o = (u32)WS_W1F + (u32)(2 * p0) * 20 * 1024;
  {
    auto ldf = [&](u32 off) { return __uint_as_float(__builtin_amdgcn_raw_buffer_load_b32(rs, off, (u32)WS_MODP, 16)); };
    const int mj = (lane & 15) >> 2;
    float mp = ldf(((lane & 3) * (6 * D_) + (mj < 3 ? mj : 4) * D_ + ((lane & 16) ? dC : dA)) * 4);
    const int tm1 = t256 > 0 ? t256 - 1 : 0;
    const u32x4 xA0 = ld_sc1(rs, tm1 * 16, (u32)WS_XT + dA * (L_ * 2));
    const u32x4 xA = ld_sc1(rs, t256 * 16, (u32)WS_XT + dA * (L_ * 2));
    u32x4 sq[4];
#pragma unroll
    for (int i = 0; i < 4; ++i) sq[i] = ld_sc1(rs, (i < 2 ? tm1 : t256) * 32 + (i & 1) * 16, (u32)WS_NT);
    const u32x4 c0 = ld_sc1(rs, tm1 * 16, (u32)WS_XT + dC * (L_ * 2)), c1 = ld_sc1(rs, t256 * 16, (u32)WS_XT + dC * (L_ * 2));
    const u16 dskAr = Dsk[dA], dskCr = Dsk[dC];
    __builtin_amdgcn_sched_barrier(0);
    asm volatile("" ::"v"(xA0), "v"(c0), "v"(sq[0]));
    TS(3, 2);
    const float dskA = bf2f(dskAr), dskC = bf2f(dskCr);
    char* smA = smem + hw * CV_CH;
    char* smC = smem + 2 * CV_CH;
    mp += __shfl_xor(mp, 1); mp += __shfl_xor(mp, 2);
    mp = rb(mp);
    auto rl = [&](int l) { return __uint_as_float(__builtin_amdgcn_readlane(__float_as_uint(mp), l)); };
    const float saA = rl(0), caA = rl(4), gaA = rl(8), c1A = rb(1.f + rl(12));
    const float saC = rl(16), caC = rl(20), gaC = rl(24), c1C = rb(1.f + rl(28));
    float mn[16], rsd[16];
#pragma unroll
    for (int i = 2; i < 16; ++i) { mn[i] = lo2f(sq[i >> 2][i & 3]); rsd[i] = __builtin_amdgcn_rcpf(hi2f(sq[i >> 2][i & 3])); }
    mn[0] = mn[1] = rsd[0] = rsd[1] = 0.f;
    conv_stage_h(smA, xA0, xA, mn, rsd, rb(1.f + caA), saA, t256);
    if (three && hw == 1) conv_stage_h(smC, c0, c1, mn, rsd, rb(1.f + caC), saC, t256);
    __builtin_amdgcn_sched_barrier(0);
    TS(3, 3);
    const int t1 = (b - 128) * 4 + wv;
    const bool do1 = !three && wv < 4 && t1 < 432;
    u32x4 rv[5];
    f32x4 accA[8], accC[8];
    if (do1) {
#pragma unroll
      for (int i = 0; i < 5; ++i) rv[i] = ld_sc1(rs, l16, (u32)WS_W1F + (t1 * 5 + i) * 1024);
      const u32 mq = (160 * (t1 & 3) + 4 * (lane < 40 ? lane : 0)) * 4;
#pragma unroll
      for (int k = 0; k < KSM; ++k) {
        accC[k] = __builtin_bit_cast(f32x4, ld_sc1(rs, mq, (u32)WS_MODP + (k * (6 * D_) + 4 * D_) * 4));
        accC[4 + k] = __builtin_bit_cast(f32x4, ld_sc1(rs, mq, (u32)WS_MODP + (k * (6 * D_) + 3 * D_) * 4));
      }
    }
    const int t2 = (b - 128) * 3 + wv - 4;
    const bool do2 = !three && wv >= 4 && wv < 7 && t2 < 270;
    if (do2) {
#pragma unroll
      for (int i = 0; i < 8; ++i) accC[i] = *(const f32x4*)(w2 + (size_t)(32 * (t2 / 5) + 4 * i + g) * D_ + 128 * (t2 % 5) + 8 * r16);
    }
    f32x4 gbp;
#pragma unroll
    for (int k = 0; k < KSM; ++k) gbp[k] = ldf((k * (6 * D_) + 5 * D_ + sl * 80 + (tid < 80 ? tid : 0)) * 4);
    __builtin_amdgcn_sched_barrier(0);
    __syncthreads();
    TS(1, 1);
    conv_mma(smA, w4, lane, accA, [&](int it) {
      if (it < 5 || (it >= 8 && it < 13)) {
        const int ks = it < 5 ? it : it - 3;
#pragma unroll
        for (int fb = 0; fb < 2; ++fb) af[ks][fb] = ld_frag1(rs, l16, w1o + (fb * 20 + ks) * 1024);
      }
    });
    if (three && hw == 0) conv_mma(smC, w4, lane, accC, [](int) {});
    if (tid < 80) ((float*)(smem + MN_GB))[tid] = rb((gbp[0] + gbp[1]) + (gbp[2] + gbp[3]));
    if (do2) {
      char* buf = smC + 4096 + (wv - 4) * 8192;
#pragma unroll
      for (int i = 0; i < 8; ++i) {
        const int row = 4 * i + g;
        *(f32x4*)(buf + row * 256 + (((r16 >> 1) ^ rl_swz(row)) << 5) + ((r16 & 1) << 4)) = accC[i];
      }
      wave_sync();
      const int row0 = 4 * g + q, row1 = row0 + 16;
#pragma unroll
      for (int cb = 0; cb < 8; ++cb) {
        const s16x4 lo = tr_read(buf + row0 * 256 + ((cb ^ rl_swz(row0)) << 5) + 8 * p);
        const s16x4 hi = tr_read(buf + row1 * 256 + ((cb ^ rl_swz(row1)) << 5) + 8 * p);
        st_sc1(rs, l16, (u32)WS_W2F + ((8 * (t2 % 5) + cb) * 54 + t2 / 5) * 1024, __builtin_bit_cast(u32x4, cat8(lo, hi)));
      }
    }
    if (do1) {
      char* buf = smC + wv * 1024;
      const f32x4 mc = (accC[0] + accC[1]) + (accC[2] + accC[3]), ms = (accC[4] + accC[5]) + (accC[6] + accC[7]);
      if (lane < 40) {
        *(u32x2*)(buf + lane * 8) = u32x2{cvtpk(1.f + rb(mc[0]), 1.f + rb(mc[1])), cvtpk(1.f + rb(mc[2]), 1.f + rb(mc[3]))};
        *(u32x2*)(buf + 512 + lane * 8) = u32x2{cvtpk(ms[0], ms[1]), cvtpk(ms[2], ms[3])};
      }
      wave_sync();
      float S = 0.f, B = 0.f;
#pragma unroll
      for (int i = 0; i < 5; ++i) {
        const u32x4 c1p = *(const u32x4*)(buf + i * 64 + g * 16), sbp = *(const u32x4*)(buf + 512 + i * 64 + g * 16);
#pragma unroll
        for (int k = 0; k < 4; ++k) { S = dot2bf(rv[i][k], c1p[k], S); B = dot2bf(rv[i][k], sbp[k], B); }
      }
      S += __shfl_xor(S, 16); S += __shfl_xor(S, 32);
      B += __shfl_xor(B, 16); B += __shfl_xor(B, 32);
      if (lane < 32)
        __builtin_amdgcn_raw_buffer_store_b32(__float_as_uint(lane < 16 ? S : B), rs, (g * 4 * F_ + r16) * 4, (u32)WS_SB + ((t1 & 3) * F_ + 16 * (t1 >> 2)) * 4, 16);
    }
    __syncthreads();
    TS(1, 2);
    conv_put(smA, w4, lane, accA);
    if (three && hw == 0) conv_put(smC, w4, lane, accC);
    __syncthreads();
    u32x4 os;
    st_sc1(rs, t256 * 16, (u32)WS_X1T + dA * (L_ * 2), conv_fin(smA, t256, xA, dskA, gaA, c1A, os));
    st_sc1(rs, t256 * 16, (u32)WS_X1S + dA * (L_ * 2), os);
    if (three && hw == 1) {
      st_sc1(rs, t256 * 16, (u32)WS_X1T + dC * (L_ * 2), conv_fin(smC, t256, c1, dskC, gaC, c1C, os));
      st_sc1(rs, t256 * 16, (u32)WS_X1S + dC * (L_ * 2), os);
    }
  }
  asm volatile("s_waitcnt vmcnt(0)" ::: "memory");
  __syncthreads();
  TS(1, 3);
  if (wv == 7) {
    u32 o = 0;
    if (lane == 0) o = __hip_atomic_fetch_add(cnt + (b & 7) * 64, 1u, __ATOMIC_RELAXED, __HIP_MEMORY_SCOPE_AGENT);
    o = __builtin_amdgcn_readfirstlane(o);
    if (o == 31u && lane < 8) __hip_atomic_store(cnt + (8 + lane) * 64 + (b & 7), 1u, __ATOMIC_RELAXED, __HIP_MEMORY_SCOPE_AGENT);
    for (int spins = 0; spins < (1 << 20); ++spins) {
      u32 v = 1u;
      if (lane < 8) v = __hip_atomic_load(cnt + (8 + (b & 7)) * 64 + lane, __ATOMIC_RELAXED, __HIP_MEMORY_SCOPE_AGENT);
      if (__all(v != 0u)) break;
      __builtin_amdgcn_s_sleep(1);
    }
  }
  __syncthreads();
  TS(1, 4);

  const int k0 = 7 * wv - (wv >> 2), nk = (wv & 3) == 3 ? 6 : 7;
  const u32 w2o = (u32)WS_W2F + (u32)((sl * 5) * 54 + k0) * 1024;
  bf16x8 wf[7][5];
  {
    const int l8 = tid & 7, crow = tid >> 3;
    u32x4 xv[12];
    f32x4 sb4;
    f32x4 acc[2][4];
#pragma unroll
    for (int fb = 0; fb < 2; ++fb)
#pragma unroll
      for (int tb = 0; tb < 4; ++tb) acc[fb][tb] = f32x4{0.f, 0.f, 0.f, 0.f};
    if (wv < 7) {
#pragma unroll
      for (int qq = 0; qq < 12; ++qq)
        xv[qq] = ld_sc1(rs, (qq < 11 || crow < 24 ? crow : 0) * (L_ * 2) + l8 * 16, (u32)WS_X1S + qq * (56 * L_ * 2) + tt * 128);
      __builtin_amdgcn_sched_barrier(0);
#pragma unroll
      for (int qq = 0; qq < 12; ++qq) {
        const int c = crow + 56 * qq;
        if (qq < 11 || crow < 24) *(u32x4*)(smem + M1_A + c * 128 + (((l8 >> 1) ^ h2_swz(c)) << 5) + ((l8 & 1) << 4)) = xv[qq];
      }
    } else {
      __builtin_amdgcn_s_setprio(3);
#pragma unroll
      for (int i = 0; i < 10; ++i) af[i >> 1][i & 1] = ld_frag1(rs, (lane >> 3) * (L_ * 2) + (lane & 7) * 16, (u32)WS_X1T + (sl * 80 + 8 * i) * (L_ * 2) + tt * 128);
    }
    __syncthreads();
    TS(1, 5);
    TC(3, 0);
    const int row0 = 8 * g + q, sw = h2_swz(row0);
    const char* rb0 = smem + M1_A + row0 * 128 + 8 * p;
    float* st = (float*)(smem + M1_ST);
    auto ldb = [&](int ks, bf16x8 (&bfr)[4]) {
#pragma unroll
      for (int tb = 0; tb < 4; ++tb) {
        const s16x4 lo = tr_read(rb0 + ks * 4096 + ((tb ^ sw) << 5));
        const s16x4 hi = tr_read(rb0 + ks * 4096 + 512 + ((tb ^ sw) << 5));
        bfr[tb] = cat8(lo, hi);
      }
    };
    bf16x8 bb[2][4];
    ldb(0, bb[0]);
#pragma unroll
    for (int ks = 0; ks < 20; ++ks) {
      if (active) {
        if (ks < 19) ldb(ks + 1, bb[(ks + 1) & 1]);
#pragma unroll
        for (int fb = 0; fb < 2; ++fb)
#pragma unroll
          for (int tb = 0; tb < 4; ++tb) acc[fb][tb] = mfma16(af[ks % W1_RING][fb], bb[ks & 1][tb], acc[fb][tb]);
        if (ks + W1_RING < 20) {
#pragma unroll
          for (int fb = 0; fb < 2; ++fb) af[ks % W1_RING][fb] = ld_frag1(rs, l16, w1o + (fb * 20 + ks + W1_RING) * 1024);
        }
      } else if (ks == 0 && wv == 7) {
#pragma unroll
        for (int i = 0; i < 10; ++i) {
          const u32x4 v = __builtin_bit_cast(u32x4, af[i >> 1][i & 1]);
          const f32x4 a0 = {lo2f(v[0]), hi2f(v[0]), lo2f(v[1]), hi2f(v[1])}, a1 = {lo2f(v[2]), hi2f(v[2]), lo2f(v[3]), hi2f(v[3])};
          acc[0][0] += a0; acc[0][1] += a1; acc[0][2] += a0 * a0; acc[0][3] += a1 * a1;
        }
        const bool b5 = lane & 32, b4 = lane & 16, b3 = lane & 8;
        float v4[2][4], v2[2][2], v1[2];
#pragma unroll
        for (int h = 0; h < 2; ++h) {
#pragma unroll
          for (int j = 0; j < 4; ++j) {
            const float lo = acc[0][2 * h][j], hi = acc[0][2 * h + 1][j];
            v4[h][j] = (b5 ? hi : lo) + __shfl_xor(b5 ? lo : hi, 32);
          }
#pragma unroll
          for (int j = 0; j < 2; ++j) v2[h][j] = (b4 ? v4[h][2 + j] : v4[h][j]) + __shfl_xor(b4 ? v4[h][j] : v4[h][2 + j], 16);
          v1[h] = (b3 ? v2[h][1] : v2[h][0]) + __shfl_xor(b3 ? v2[h][0] : v2[h][1], 8);
        }
        const u32 toff = (8 * (lane & 7) + (lane >> 3)) * 8;
        __builtin_amdgcn_raw_buffer_store_b64(u32x2{__float_as_uint(v1[0]), __float_as_uint(v1[1]) | 0x80000000u}, rs, toff,
                                              (u32)WS_ST + (tt * 8 + sl) * 512, 16);
#pragma unroll
        for (int i = 0; i < 10; ++i) {
          const u32x4 v = __builtin_bit_cast(u32x4, af[i >> 1][i & 1]);
          char* dp = smem + M2_X + 8 * (lane & 7) * M2_XS + 2 * (8 * i + (lane >> 3));
#pragma unroll
          for (int k = 0; k < 4; ++k) {
            *(u16*)(dp + (2 * k) * M2_XS) = (u16)(v[k] & 0xffffu);
            *(u16*)(dp + (2 * k + 1) * M2_XS) = (u16)(v[k] >> 16);
          }
        }
        u32x2 gv[8];
        for (int spins = 0; spins < (1 << 20); ++spins) {
          u32 ok = 0x80000000u;
#pragma unroll
          for (int j = 0; j < 8; ++j) { gv[j] = __builtin_amdgcn_raw_buffer_load_b64(rs, lane * 8, (u32)WS_ST + (tt * 8 + j) * 512, 16); ok &= gv[j][1]; }
          if (__all(ok != 0u)) break;
          __builtin_amdgcn_s_sleep(2);
        }
        float sm = 0.f, sq = 0.f;
#pragma unroll
        for (int j = 0; j < 8; ++j) { sm += __uint_as_float(gv[j][0]); sq += __uint_as_float(gv[j][1] & 0x7fffffffu); }
        const float mean = sm * (1.f / D_);
        const float var = fmaxf(sq * (1.f / D_) - mean * mean, 0.f);
        st[lane] = rb(mean);
        st[64 + lane] = 1.f / rb(sqrtf(rb(rb(var) + 1e-5f)));
        __builtin_amdgcn_s_setprio(0);
      }
      if (ks >= 10) {
#pragma unroll
        for (int j = 0; j < 2; ++j) {
          const int f = 2 * (ks - 10) + j;
          wf[f / 5][f % 5] = LD_W2(rs, l16, w2o + ((f % 5) * 54 + f / 5) * 1024);
        }
      }
      if (ks == 9) {
        sb4 = __builtin_bit_cast(f32x4, ld_sc1(rs, ((lane >> 3) * F_ + 4 * (lane & 7)) * 4, (u32)WS_SB + 32 * p0 * 4));
      }
      if (ks == 17) {
#pragma unroll
        for (int e = 0; e < 4; ++e) { sb4[e] += __shfl_xor(sb4[e], 8); sb4[e] += __shfl_xor(sb4[e], 16); }
        if ((lane & 24) == 0) *(f32x4*)(smem + MN_SB + wv * 256 + (lane >> 5) * 128 + (lane & 7) * 16) = sb4;
      }
      __builtin_amdgcn_sched_barrier(0);
    }
    TS(2, 1);
    TC(3, 1);
#pragma unroll
    for (int i = W2_MID; i < 7; ++i)
#pragma unroll
      for (int cb = 0; cb < 5; ++cb) wf[i][cb] = LD_W2(rs, l16, w2o + (cb * 54 + (i < 6 || nk == 7 ? i : 5)) * 1024);
    __builtin_amdgcn_sched_barrier(0);
    __syncthreads();
    TS(2, 2);
    if (active) {
      const float* sbp = (const float*)(smem + MN_SB) + wv * 64 + 4 * g;
      const u32 hoff = (u32)WS_HID + (u32)((tt * 4) * 54 + p0) * 1024;
#pragma unroll
      for (int tb = 0; tb < 4; ++tb) {
        const float mb = st[tb * 16 + r16], rsd = st[64 + tb * 16 + r16];
        u32x4 v;
#pragma unroll
        for (int hh = 0; hh < 2; ++hh) {
          const f32x4 sv = *(const f32x4*)(sbp + 16 * hh), bv = *(const f32x4*)(sbp + 32 + 16 * hh);
          const f32x4 a = (acc[hh][tb] - mb * sv) * rsd + bv;
          v[2 * hh] = cvtpk(fmaxf(a[0], 0.f), fmaxf(a[1], 0.f));
          v[2 * hh + 1] = cvtpk(fmaxf(a[2], 0.f), fmaxf(a[3], 0.f));
        }
        st_sc1(rs, l16, hoff + tb * 54 * 1024, v);
      }
    }
  }
  TS(2, 3);
  asm volatile("s_waitcnt vmcnt(0)" ::: "memory");
  __syncthreads();
  TS(2, 4);
  if (tid == 448) __hip_atomic_store(cnt + (16 + tt * 8 + sl) * 64, 1u, __ATOMIC_RELAXED, __HIP_MEMORY_SCOPE_AGENT);
  __builtin_amdgcn_sched_barrier(0);
  poll_words<1>(cnt + (16 + tt * 8 + wv) * 64, 1u, lane);
  TS(1, 6);

  {
    const u32 hoff = (u32)WS_HID + (u32)((tt * 4) * 54 + k0) * 1024;
    u32x4 hf[2][4];
#pragma unroll
    for (int i = 0; i < 2; ++i)
#pragma unroll
      for (int tb = 0; tb < 4; ++tb) hf[i][tb] = ld_sc1(rs, l16, hoff + (tb * 54 + i) * 1024);
    f32x4 acc[5][4];
#pragma unroll
    for (int cb = 0; cb < 5; ++cb)
#pragma unroll
      for (int tb = 0; tb < 4; ++tb) acc[cb][tb] = f32x4{0.f, 0.f, 0.f, 0.f};
#pragma unroll
    for (int i = 0; i < 7; ++i) {
      if (i < 6 || nk == 7) {
#pragma unroll
        for (int tb = 0; tb < 4; ++tb)
#pragma unroll
          for (int cb = 0; cb < 5; ++cb) acc[cb][tb] = mfma16(wf[i][cb], __builtin_bit_cast(bf16x8, hf[i & 1][tb]), acc[cb][tb]);
      }
      __builtin_amdgcn_sched_barrier(0);
      if (i + 2 < 7) {
        const int kk = (i + 2 < 6 || nk == 7) ? i + 2 : 5;
#pragma unroll
        for (int tb = 0; tb < 4; ++tb) hf[i & 1][tb] = ld_sc1(rs, l16, hoff + (tb * 54 + kk) * 1024);
        __builtin_amdgcn_sched_barrier(0);
      }
    }
    TS(2, 5);
#pragma unroll
    for (int tb = 0; tb < 4; ++tb)
#pragma unroll
      for (int cb = 0; cb < 5; ++cb) {
        const int owner = tb + (cb < 3 ? 0 : 4);
        if (owner != wv) {
          const int slot = wv < owner ? wv : wv - 1;
          *(f32x4*)(smem + M2_RED + (((tb * 5 + cb) * 7 + slot) * 64 + lane) * 16) = acc[cb][tb];
        }
      }
    f32x4 own[3];
#pragma unroll
    for (int o = 0; o < 8; ++o) {
      if (wv == o) {
        asm volatile("" ::: "memory");
#pragma unroll
        for (int j = 0; j < 3; ++j) own[j] = acc[o < 4 ? j : (j < 2 ? 3 + j : 4)][o & 3];
      }
    }
    __syncthreads();
    TS(2, 6);
    const int tok = 16 * w4 + r16, cb0 = wv < 4 ? 0 : 3;
    f32x4 pr[3][7], gb4[3];
    u32x2 xr[3];
#pragma unroll
    for (int j = 0; j < 3; ++j) {
      const int cb = (j < 2 || wv < 4) ? cb0 + j : 4;
#pragma unroll
      for (int s7 = 0; s7 < 7; ++s7) pr[j][s7] = *(const f32x4*)(smem + M2_RED + (((w4 * 5 + cb) * 7 + s7) * 64 + lane) * 16);
      gb4[j] = *(const f32x4*)(smem + MN_GB + (16 * cb + 4 * g) * 4);
      xr[j] = *(const u32x2*)(smem + M2_X + tok * M2_XS + 2 * (16 * cb + 4 * g));
    }
    __builtin_amdgcn_sched_barrier(0);
#pragma unroll
    for (int j = 0; j < 3; ++j) {
      const f32x4 y = ((own[j] + pr[j][0]) + (pr[j][1] + pr[j][2])) + ((pr[j][3] + pr[j][4]) + (pr[j][5] + pr[j][6]));
      const u32 y0 = cvtpk(y[0], y[1]), y1 = cvtpk(y[2], y[3]);
      const u32 v0 = cvtpk(lo2f(y0) * gb4[j][0], hi2f(y0) * gb4[j][1]), v1 = cvtpk(lo2f(y1) * gb4[j][2], hi2f(y1) * gb4[j][3]);
      u32x2 o;
      o[0] = cvtpk(lo2f(xr[j][0]) + lo2f(v0), hi2f(xr[j][0]) + hi2f(v0));
      o[1] = cvtpk(lo2f(xr[j][1]) + lo2f(v1), hi2f(xr[j][1]) + hi2f(v1));
      if (j < 2 || wv < 4) *(u32x2*)(out + (size_t)(tt * 64 + tok) * D_ + sl * 80 + 16 * (cb0 + j) + 4 * g) = o;
    }
  }
  TS(1, 7);
}

extern "C" void kernel_launch(void* const* d_in, const int* in_sizes, int n_in, void* d_out, int out_size, void* d_ws,
                              size_t ws_size, hipStream_t stream) {
  char* ws = (char*)d_ws;
  k_main<<<256, 512, 0, stream>>>(
      (const u16*)d_in[0], (const u16*)d_in[1], (const u16*)d_in[2], (const u16*)d_in[3], (const u16*)d_in[4],
      (const u16*)d_in[5], (const u16*)d_in[6], (const u16*)d_in[7], (const u16*)d_in[8], (const u16*)d_in[10],
      (const u16*)d_in[11], (const u16*)d_in[12], (const u16*)d_in[13], ws, (u16*)d_out);
}
```

```cpp
#define AUX 16
#define WAIT_PT 17
#define WAIT_T 5000
#include <hip/hip_runtime.h>
#include <hip/hip_bf16.h>
#include <stdint.h>

extern "C" __device__ unsigned long long llvm_dispatch_id() __asm("llvm.amdgcn.dispatch.id");
typedef unsigned short u16;
typedef unsigned int u32;
typedef __attribute__((ext_vector_type(8))) short bf16x8;
typedef __attribute__((ext_vector_type(4))) short s16x4;
typedef __attribute__((ext_vector_type(4))) float f32x4;
typedef __attribute__((ext_vector_type(2))) float f32x2;
typedef __attribute__((ext_vector_type(4))) unsigned int u32x4;
typedef __attribute__((ext_vector_type(2))) unsigned int u32x2;
typedef __attribute__((ext_vector_type(3))) unsigned int u32x3;
typedef __bf16 bf16x2_t __attribute__((ext_vector_type(2)));
#define LDS_AS __attribute__((address_space(3)))
#define DI __device__ __forceinline__

constexpr int L_ = 2048, D_ = 640, N_ = 128, F_ = 1728;
constexpr int KSM = 4;

constexpr size_t WS_MODP = 0;
constexpr size_t WS_CNT = 126976;
constexpr size_t WS_FL = 196608;
constexpr size_t WS_NT = 262144;
constexpr size_t WS_XT = WS_NT + (size_t)D_ * L_ * 2;
constexpr size_t WS_X1T = WS_XT + (size_t)D_ * L_ * 2;
constexpr size_t WS_X1S = WS_X1T + (size_t)D_ * L_ * 2;
constexpr int KTAB = 1296;
constexpr size_t WS_KT = WS_X1S + (size_t)D_ * L_ * 2;
constexpr size_t WS_SB = WS_KT + (size_t)D_ * KTAB;
constexpr size_t WS_W1F = WS_SB + 2 * 20 * 1728 * 4;
constexpr size_t WS_W2F = WS_W1F + (size_t)108 * 20 * 1024;
constexpr size_t WS_HID = WS_W2F + (size_t)40 * 54 * 1024;
constexpr size_t WS_ST = WS_HID + (size_t)128 * 54 * 1024;

#define TS(kern, slot) do { if ((kern) * 10 + (slot) == 10) t_start = __builtin_amdgcn_s_memrealtime(); if ((kern) * 10 + (slot) == WAIT_PT && blockIdx.x == 0 && threadIdx.x < 64) { while (__builtin_amdgcn_s_memrealtime() - t_start < WAIT_T) __builtin_amdgcn_s_sleep(1); } } while (0)
#define TC(kern, slot) do {} while (0)

DI void st_wt(void* p, u32x4 v) { asm volatile("global_store_dwordx4 %0, %1, off sc1\n\ts_nop 1" ::"v"(p), "v"(v) : "memory"); }
#define ST_PRE(p, v) (*(p) = (v))
#define ST_PREW(p, v) (*(p) = (v))
DI u32 cvtpk(float lo, float hi) {
  f32x2 v = {lo, hi};
  bf16x2_t r = __builtin_convertvector(v, bf16x2_t);
  return __builtin_bit_cast(u32, r);
}
typedef _Float16 f16x2_t __attribute__((ext_vector_type(2)));
typedef _Float16 f16x8_t __attribute__((ext_vector_type(8)));
DI u32 cvtph(float lo, float hi) {
  f32x2 v = {lo, hi};
  f16x2_t r = __builtin_convertvector(v, f16x2_t);
  return __builtin_bit_cast(u32, r);
}
DI float dot2bf(u32 a, u32 b, float c) {
  return __builtin_amdgcn_fdot2_f32_bf16(__builtin_bit_cast(bf16x2_t, a), __builtin_bit_cast(bf16x2_t, b), c, false);
}
DI float lo2f(u32 v) { return __uint_as_float(v << 16); }
DI float hi2f(u32 v) { return __uint_as_float(v & 0xffff0000u); }
DI float bf2f(u16 v) { return __uint_as_float(((u32)v) << 16); }
DI float rb(float x) { return lo2f(cvtpk(x, x)); }
DI f32x4 mfma16(bf16x8 a, bf16x8 b, f32x4 c) { return __builtin_amdgcn_mfma_f32_16x16x32_bf16(a, b, c, 0, 0, 0); }
DI s16x4 tr_read(const char* p) {
  return __builtin_amdgcn_ds_read_tr16_b64_v4i16((LDS_AS s16x4*)p);
}
DI bf16x8 cat8(s16x4 a, s16x4 b) { return __builtin_shufflevector(a, b, 0, 1, 2, 3, 4, 5, 6, 7); }
DI void wave_sync() {
  __builtin_amdgcn_fence(__ATOMIC_RELEASE, "wavefront");
  __builtin_amdgcn_wave_barrier();
  __builtin_amdgcn_fence(__ATOMIC_ACQUIRE, "wavefront");
}

DI f32x2 cmul2(f32x2 a, f32x2 b) {
  const f32x2 bj = {-b.y, b.x};
  return __builtin_elementwise_fma(a.yy, bj, a.xx * b);
}
struct c32 { float r, i; };
DI c32 cmul(c32 a, c32 b) { return {a.r * b.r - a.i * b.i, a.r * b.i + a.i * b.r}; }
DI c32 cexp_scaled(float ar, float ai, float e) {
  const float mag = __expf(ar * e);
  const float x = ai * e;
  const float C_HI = 0.15915494f, C_LO = 6.4206382e-9f;
  const float r = x * C_HI;
  const float rl = fmaf(x, C_HI, -r) + x * C_LO;
  const float fr = (r - floorf(r)) + rl;
  return {mag * __builtin_amdgcn_cosf(fr), mag * __builtin_amdgcn_sinf(fr)};
}
DI int tap_index(int m) {
  u32 bits = __float_as_uint((float)m);
  bits += 0x7fffu + ((bits >> 16) & 1u);
  const int id2 = (int)(bits >> 16) - (0x4300 - 128);
  return m < 128 ? m : id2;
}

DI int rl_swz(int row) { return (row & 3) | ((((row >> 2) ^ (row >> 3)) & 1) << 2); }

constexpr int CV_H0 = 0, CV_HS = 4160, CV_K = 12544, CV_T = 21760, CV_H3 = 32768 + 192, CV_CH = 37376;
static_assert(CV_CH % 256 == 0 && CV_H3 + 4160 <= CV_CH && CV_T + 4 * KTAB <= 32768, "conv lds");
constexpr int M1_A = 0, M1_ST = 81920;
constexpr int MN_SB = 3 * CV_CH;
constexpr int MN_R1 = MN_SB + 2048;
constexpr int M2_RED = 0, M2_X = 143360, M2_XS = 176;
constexpr int MN_GB = M2_X + 64 * M2_XS;
constexpr int MN_PC = MN_GB + 320;
constexpr int MN_SMEM = MN_PC + 16;
static_assert(MN_R1 + 5 * 4864 <= M2_X && M1_ST + 512 <= MN_SB, "mlp lds");

typedef __amdgpu_buffer_rsrc_t rsrc_t;
DI u32x4 ld_sc1(rsrc_t r, u32 voff, u32 soff) { return __builtin_amdgcn_raw_buffer_load_b128(r, voff, soff, 16); }
DI void st_sc1(rsrc_t r, u32 voff, u32 soff, u32x4 v) { __builtin_amdgcn_raw_buffer_store_b128(v, r, voff + soff, 0, 16); }
DI bf16x8 ld_frag1(rsrc_t r, u32 voff, u32 soff) { return __builtin_bit_cast(bf16x8, __builtin_amdgcn_raw_buffer_load_b128(r, voff, soff, 16)); }
DI bf16x8 ld_frag(rsrc_t r, u32 voff, u32 soff) { return __builtin_bit_cast(bf16x8, __builtin_amdgcn_raw_buffer_load_b128(r, voff, soff, 0)); }

DI int h2_swz(int k) { return ((k >> 1) & 1) | (((k >> 3) & 1) << 1); }

DI void pair_sync(u32* c, u32 target) {
  __builtin_amdgcn_fence(__ATOMIC_RELEASE, "workgroup");
  if ((threadIdx.x & 63) == 0) __hip_atomic_fetch_add(c, 1u, __ATOMIC_RELAXED, __HIP_MEMORY_SCOPE_WORKGROUP);
  for (int spins = 0; spins < (1 << 22); ++spins) {
    if (__hip_atomic_load(c, __ATOMIC_RELAXED, __HIP_MEMORY_SCOPE_WORKGROUP) >= target) break;
    __builtin_amdgcn_s_sleep(1);
  }
  __builtin_amdgcn_fence(__ATOMIC_ACQUIRE, "workgroup");
}

DI void kgen_pair(int d, int hi, int lane, const u16* __restrict__ Are, const u16* __restrict__ Aim, const u16* __restrict__ Bre,
                  const u16* __restrict__ Bim, const u16* __restrict__ Cre, const u16* __restrict__ Cim,
                  const u16* __restrict__ ldt, char* smem, u32* pc) {
  const int r16 = lane & 15, g = lane >> 4, n = hi * 64 + lane;
  const int gi = d * N_ + n;
  const float dt = rb(expf(bf2f(ldt[d])));
  const float ar = dt * bf2f(Are[gi]), ai = dt * bf2f(Aim[gi]);
  const c32 dB = {dt * bf2f(Bre[gi]), dt * bf2f(Bim[gi])};
  const c32 Cc = {bf2f(Cre[gi]), bf2f(Cim[gi])};
  const c32 W = cmul(Cc, dB);
  const c32 E1 = cexp_scaled(ar, ai, 1.f), E8 = cexp_scaled(ar, ai, 8.f), E128 = cexp_scaled(ar, ai, 128.f);

  float wmax = fmaxf(fabsf(W.r), fabsf(W.i));
#pragma unroll
  for (int o = 1; o < 64; o <<= 1) wmax = fmaxf(wmax, __shfl_xor(wmax, o));
  int ew = (int)((__float_as_uint(wmax) >> 23) & 0xff);
  ew = ew < 20 ? 20 : (ew > 240 ? 240 : ew);
  const float wsc = __uint_as_float((u32)(127 + 10 + 127 - ew - 1) << 23);
  const float unsc = __uint_as_float((u32)(ew + 1 - 20) << 23);

  char* buf = smem + hi * 6144;
  char* wr = buf + lane * 4;
  const char* rd = buf + r16 * 272 + g * 16;
  auto put = [&](int row, f32x2 v) { *(u32*)(wr + row * 272) = cvtph(v.x, v.y); };
  {
    const f32x2 e8 = {E8.r, E8.i};
    f32x2 p = {1024.f, 0.f};
#pragma unroll
    for (int jj = 0; jj < 16; ++jj) { put(jj, p); if (jj < 15) p = cmul2(p, e8); }
  }
  wave_sync();
  f16x8_t bh[4];
#pragma unroll
  for (int ks = 0; ks < 4; ++ks) bh[ks] = *(const f16x8_t*)(rd + ks * 64);
  wave_sync();
  const f32x2 e1 = {E1.r, -E1.i}, e128 = {E128.r, -E128.i};
  f32x2 sm[8];
  sm[1] = e1;
#pragma unroll
  for (int i = 2; i < 8; ++i) sm[i] = cmul2(sm[i - 1], e1);
  f32x2 WG[17];
  WG[0] = f32x2{W.r * wsc, -W.i * wsc};
#pragma unroll
  for (int i = 1; i < 17; ++i) WG[i] = cmul2(WG[i - 1], e128);
  f32x4 acc[3];
  auto tile_mma = [&](int t) {
    wave_sync();
    f32x4 a = {0.f, 0.f, 0.f, 0.f};
#pragma unroll
    for (int ks = 0; ks < 4; ++ks) a = __builtin_amdgcn_mfma_f32_16x16x32_f16(*(const f16x8_t*)(rd + ks * 64), bh[ks], a, 0, 0, 0);
    acc[t] = a * unsc;
    wave_sync();
  };
#pragma unroll
  for (int i = 0; i < 2; ++i)
#pragma unroll
    for (int rho = 0; rho < 8; ++rho) put(8 * i + rho, rho ? cmul2(sm[rho], WG[i]) : WG[i]);
  tile_mma(0);
#pragma unroll
  for (int i = 0; i < 2; ++i)
#pragma unroll
    for (int rho = 0; rho < 4; ++rho) put(4 * i + rho, rho ? cmul2(sm[2 * rho], WG[2 + i]) : WG[2 + i]);
#pragma unroll
  for (int i = 0; i < 4; ++i)
#pragma unroll
    for (int rho = 0; rho < 2; ++rho) put(8 + 2 * i + rho, rho ? cmul2(sm[4], WG[4 + i]) : WG[4 + i]);
  tile_mma(1);
#pragma unroll
  for (int i = 0; i < 9; ++i) put(i, WG[8 + i]);
  tile_mma(2);

#pragma unroll
  for (int t = 0; t < 3; ++t) *(f32x4*)(buf + t * 1024 + lane * 16) = acc[t];
  pair_sync(pc, 2u);
  u16* T = (u16*)(smem + CV_T);
  {
    const char* ob = smem + (hi ^ 1) * 6144 + lane * 16;
    if (!hi) {
      const f32x4 a0 = acc[0] + *(const f32x4*)(ob), a2 = acc[2] + *(const f32x4*)(ob + 2048);
      u16* t0 = T + 128 * (g >> 1) + 4 * (g & 1) + 8 * r16;
      *(u32x2*)t0 = u32x2{cvtpk(a0[0], a0[1]), cvtpk(a0[2], a0[3])};
      if (g < 2) {
#pragma unroll
        for (int rg = 0; rg < 4; ++rg) T[512 + 16 * (4 * g + rg) + r16] = (u16)cvtpk(a2[rg], 0.f);
      } else if (lane == 32) {
        T[640] = (u16)cvtpk(a2[0], 0.f);
      } else if (lane >= 48 && lane < 55) {
        T[641 + lane - 48] = 0;
      }
    } else {
      const f32x4 a1 = acc[1] + *(const f32x4*)(ob + 1024);
      if (g < 2) {
        *(u32x2*)(T + 256 + 64 * g + 4 * r16) = u32x2{cvtpk(a1[0], a1[1]), cvtpk(a1[2], a1[3])};
      } else {
        *(u32*)(T + 384 + 64 * (g - 2) + 2 * r16) = cvtpk(a1[0], a1[1]);
        *(u32*)(T + 384 + 64 * (g - 2) + 32 + 2 * r16) = cvtpk(a1[2], a1[3]);
      }
    }
  }
  pair_sync(pc, 4u);
#pragma unroll
  for (int i = 0; i < 3; ++i) {
    const int cp = n + 128 * i;
    if (cp < 284) {
      u32 e[9];
#pragma unroll
      for (int j = 0; j < 9; ++j) {
        const int m = 2047 - 8 * cp - j;
        e[j] = T[m < 0 ? 641 : tap_index(m)];
      }
      *(u32x4*)(smem + CV_K + 4576 + cp * 16) = u32x4{e[0] | (e[1] << 16), e[2] | (e[3] << 16), e[4] | (e[5] << 16), e[6] | (e[7] << 16)};
      *(u32x4*)(smem + CV_K + cp * 16) = u32x4{e[1] | (e[2] << 16), e[3] | (e[4] << 16), e[5] | (e[6] << 16), e[7] | (e[8] << 16)};
    }
  }
}

DI void ln1_pair(int t0, int t, const u16* __restrict__ x, rsrc_t rs, char* buf, u32* pc) {
  const int u = t >> 4, j = t & 15;
  const u32x4* xa = (const u32x4*)(x + (size_t)(t0 + 2 * u) * D_);
  u32x4 A[5], B[5];
#pragma unroll
  for (int q = 0; q < 5; ++q) { A[q] = xa[j + 16 * q]; B[q] = xa[80 + j + 16 * q]; }
  char* wbase = buf + 9 * j * 32 + ((((u >> 2) ^ ((j >> 2) & 1)) << 2) + (u & 3)) * 4;
#pragma unroll
  for (int q = 0; q < 5; ++q)
#pragma unroll
    for (int k = 0; k < 4; ++k) {
      *(u32*)(wbase + (144 * q + 2 * k) * 32) = (A[q][k] & 0xffffu) | (B[q][k] << 16);
      *(u32*)(wbase + (144 * q + 2 * k + 1) * 32) = (A[q][k] >> 16) | (B[q][k] & 0xffff0000u);
    }
  float sa = 0.f, sb = 0.f, qa = 0.f, qb = 0.f;
#pragma unroll
  for (int q = 0; q < 5; ++q)
#pragma unroll
    for (int k = 0; k < 4; ++k) {
      sa = dot2bf(A[q][k], 0x3f803f80u, sa); qa = dot2bf(A[q][k], A[q][k], qa);
      sb = dot2bf(B[q][k], 0x3f803f80u, sb); qb = dot2bf(B[q][k], B[q][k], qb);
    }
#pragma unroll
  for (int o = 1; o < 16; o <<= 1) {
    sa += __shfl_xor(sa, o, 16); sb += __shfl_xor(sb, o, 16); qa += __shfl_xor(qa, o, 16); qb += __shfl_xor(qb, o, 16);
  }
  const float ma = sa * (1.f / D_), mb = sb * (1.f / D_);
  const float va = fmaxf(qa - sa * ma, 0.f), vb = fmaxf(qb - sb * mb, 0.f);
  if (j == 0) {
    const u32 s0 = cvtpk(ma, sqrtf(rb(rb(va * (1.f / D_)) + 1e-5f))), s1 = cvtpk(mb, sqrtf(rb(rb(vb * (1.f / D_)) + 1e-5f)));
    __builtin_amdgcn_raw_buffer_store_b64(u32x2{s0, s1}, rs, (u32)WS_NT + (t0 + 2 * u) * 4, 0, 16);
  }
  pair_sync(pc, 2u);
#pragma unroll
  for (int i = 0; i < 10; ++i) {
    const int id = t + 128 * i, c = id >> 1, o = id & 1;
    const u32x4 v = *(const u32x4*)(buf + (c + (c >> 3)) * 32 + ((o ^ ((c >> 5) & 1)) << 4));
    st_sc1(rs, c * (L_ * 2) + o * 16, (u32)WS_XT + t0 * 2, v);
  }
}

DI void mod_unit(int un, int lane, const u16* __restrict__ c, const u16* __restrict__ wm, rsrc_t rs) {
  const int cbk = un % 60, ks = un / 60, cj = lane & 7, rg = lane >> 3;
  u32x4 v[20];
  u16 cr[20];
#pragma unroll
  for (int i = 0; i < 20; ++i) {
    const int row = 160 * ks + rg + 8 * i;
    v[i] = *(const u32x4*)(wm + (size_t)row * (6 * D_) + 64 * cbk + 8 * cj);
    cr[i] = c[row];
  }
  float a[8];
#pragma unroll
  for (int k = 0; k < 8; ++k) a[k] = 0.f;
#pragma unroll
  for (int i = 0; i < 20; ++i) {
    const float cv = bf2f(cr[i]);
    const float sv = rb(cv / (1.f + __expf(-cv)));
#pragma unroll
    for (int k = 0; k < 4; ++k) { a[2 * k] += sv * lo2f(v[i][k]); a[2 * k + 1] += sv * hi2f(v[i][k]); }
  }
#pragma unroll
  for (int k = 0; k < 8; ++k) { a[k] += __shfl_xor(a[k], 8); a[k] += __shfl_xor(a[k], 16); a[k] += __shfl_xor(a[k], 32); }
  if (lane < 16) {
    const f32x4 o = rg ? f32x4{a[4], a[5], a[6], a[7]} : f32x4{a[0], a[1], a[2], a[3]};
    st_sc1(rs, (8 * cj + 4 * rg) * 4, (u32)WS_MODP + (ks * (6 * D_) + 64 * cbk) * 4, __builtin_bit_cast(u32x4, o));
  }
}

DI void w1_tile(int tile, int lane, const u16* __restrict__ w1, rsrc_t rs, char* buf) {
  const int ks = tile / 14, nb = tile % 14, nblk = nb == 13 ? 4 : 8;
  const int g = lane >> 4, lc = lane & 15, q = lc >> 2, p = lane & 3;
  u32x4 v[8];
#pragma unroll
  for (int i = 0; i < 8; ++i) v[i] = *(const u32x4*)(w1 + (size_t)(32 * ks + 4 * i + g) * F_ + 128 * nb + 8 * (lc < 2 * nblk ? lc : 0));
#pragma unroll
  for (int i = 0; i < 8; ++i) {
    const int row = 4 * i + g;
    *(u32x4*)(buf + row * 256 + (((lc >> 1) ^ rl_swz(row)) << 5) + ((lc & 1) << 4)) = v[i];
  }
  wave_sync();
  const int row0 = 8 * g + q, row1 = row0 + 4;
#pragma unroll
  for (int cb = 0; cb < 8; ++cb) {
    const s16x4 lo = tr_read(buf + row0 * 256 + ((cb ^ rl_swz(row0)) << 5) + 8 * p);
    const s16x4 hi = tr_read(buf + row1 * 256 + ((cb ^ rl_swz(row1)) << 5) + 8 * p);
    if (cb < nblk) st_sc1(rs, lane * 16, (u32)WS_W1F + ((8 * nb + cb) * 20 + ks) * 1024, __builtin_bit_cast(u32x4, cat8(lo, hi)));
  }
  wave_sync();
}

DI void conv_stage_h(char* smem, u32x4 xq0, u32x4 xq1, const float (&mn)[16], const float (&rs)[16], float ca1, float sa, int tid) {
  u32 h[8];
#pragma unroll
  for (int k = 1; k < 8; ++k) {
    const u32 xv = k < 4 ? xq0[k] : xq1[k - 4];
    const u32 c = cvtpk(lo2f(xv) - mn[2 * k], hi2f(xv) - mn[2 * k + 1]);
    const u32 n = cvtpk(lo2f(c) * rs[2 * k], hi2f(c) * rs[2 * k + 1]);
    const u32 p = cvtpk(lo2f(n) * ca1, hi2f(n) * ca1);
    const u32 hv = cvtpk(lo2f(p) + sa, hi2f(p) + sa);
    h[k] = (k >= 4 || tid > 0) ? hv : 0u;
  }
  const int yo = (tid + 3) * 16;
  *(u32x4*)(smem + CV_H0 + yo) = u32x4{h[1], h[2], h[3], h[4]};
  *(u32x4*)(smem + CV_H0 + CV_HS + yo) = u32x4{h[2], h[3], h[4], h[5]};
  *(u32x4*)(smem + CV_H0 + 2 * CV_HS + yo) = u32x4{h[3], h[4], h[5], h[6]};
  *(u32x4*)(smem + CV_H3 + yo) = u32x4{h[4], h[5], h[6], h[7]};
  if (tid < 12) {
    const int cc = tid / 3, yc = tid % 3;
    *(u32x4*)(smem + (cc == 3 ? CV_H3 : CV_H0 + cc * CV_HS) + yc * 16) = u32x4{0u, 0u, 0u, 0u};
  }
}

template <class CB>
DI void conv_mma(const char* smem, int w, int lane, f32x4 (&acc)[8], CB&& side) {
  const int r16 = lane & 15, g = lane >> 4, cpy = r16 & 3;
  const char* hb = smem + (cpy == 3 ? CV_H3 : CV_H0 + cpy * CV_HS) + 16 * ((r16 >> 2) + 4 * w + g);
  const char* kb = smem + CV_K + (r16 & 1) * 4576 + 16 * (252 + 4 * w - 4 * (r16 >> 1) + g) - 256 * 14;
#pragma unroll
  for (int i = 0; i < 8; ++i) acc[i] = f32x4{0.f, 0.f, 0.f, 0.f};
#pragma unroll
  for (int par = 1; par >= 0; --par) {
    bf16x8 Bf[8];
#pragma unroll
    for (int t = 0; t < 8; ++t) Bf[t] = *(const bf16x8*)(hb + 256 * (2 * t + par));
#pragma unroll
    for (int sg = -par; sg <= 14; sg += 2) {
      const bf16x8 Af = *(const bf16x8*)(kb + 256 * (14 - sg));
#pragma unroll
      for (int i = 0; i < 8; ++i) {
        const int tau = 2 * i - sg;
        if (tau >= 0 && tau <= 15) acc[i] = mfma16(Af, Bf[tau >> 1], acc[i]);
      }
      side((1 - par) * 8 + ((sg + par) >> 1));
    }
  }
}

DI void conv_put(char* smem, int w, int lane, const f32x4 (&acc)[8]) {
  char* pb = smem + w * 8192 + (64 * (lane >> 4) + 2 * (lane & 15)) * 4;
#pragma unroll
  for (int i = 0; i < 8; ++i) {
    *(f32x2*)(pb + i * 1024) = f32x2{acc[i][0], acc[i][1]};
    *(f32x2*)(pb + i * 1024 + 128) = f32x2{acc[i][2], acc[i][3]};
  }
}

DI u32x4 conv_fin(const char* smem, int t, u32x4 xv, float dsk, float ga, float c1, u32x4& os) {
  f32x4 y0 = *(const f32x4*)(smem + t * 32), y1 = *(const f32x4*)(smem + t * 32 + 16);
#pragma unroll
  for (int w = 1; w < 4; ++w) { y0 += *(const f32x4*)(smem + w * 8192 + t * 32); y1 += *(const f32x4*)(smem + w * 8192 + t * 32 + 16); }
  const u32x4 hv = *(const u32x4*)(smem + CV_H3 + 48 + t * 16);
  u32x4 o;
#pragma unroll
  for (int k = 0; k < 4; ++k) {
    const u32 yr = k < 2 ? cvtpk(y0[2 * k], y0[2 * k + 1]) : cvtpk(y1[2 * k - 4], y1[2 * k - 3]);
    const u32 hd = cvtpk(lo2f(hv[k]) * dsk, hi2f(hv[k]) * dsk);
    const u32 v = cvtpk(lo2f(yr) + lo2f(hd), hi2f(yr) + hi2f(hd));
    const u32 gv = cvtpk(lo2f(v) * ga, hi2f(v) * ga);
    o[k] = cvtpk(lo2f(xv[k]) + lo2f(gv), hi2f(xv[k]) + hi2f(gv));
    os[k] = cvtpk(lo2f(o[k]) * c1, hi2f(o[k]) * c1);
  }
  return o;
}

template <int NW>
DI void poll_words(const u32* cnt, u32 target, int lane) {
  for (int spins = 0; spins < (1 << 20); ++spins) {
    u32 v = target;
    if (lane < NW) v = __hip_atomic_load(cnt + lane * 64, __ATOMIC_RELAXED, __HIP_MEMORY_SCOPE_AGENT);
    if (__all(v >= target)) break;
    __builtin_amdgcn_s_sleep(1);
  }
}

#define LD_W2 ld_frag1
constexpr int W1_RING = 10;
constexpr int W2_MID = 4;

__global__ __launch_bounds__(512, 2) void k_main(const u16* __restrict__ x, const u16* __restrict__ Are, const u16* __restrict__ Aim,
                                                    const u16* __restrict__ Bre, const u16* __restrict__ Bim, const u16* __restrict__ Cre,
                                                    const u16* __restrict__ Cim, const u16* __restrict__ ldt, const u16* __restrict__ Dsk,
                                                    const u16* __restrict__ w1, const u16* __restrict__ w2, const u16* __restrict__ cvec,
                                                    const u16* __restrict__ wm, char* __restrict__ ws, u16* __restrict__ out) {
  __shared__ __attribute__((aligned(256))) char smem[MN_SMEM];
  const int tid = threadIdx.x, wv = __builtin_amdgcn_readfirstlane(tid >> 6), lane = tid & 63;
  const int r16 = lane & 15, g = lane >> 4, q = r16 >> 2, p = lane & 3;
  const int b = blockIdx.x, hw = wv >> 2, w4 = wv & 3, t256 = tid & 255;
  const rsrc_t rs = __builtin_amdgcn_make_buffer_rsrc((void*)ws, 0, 0x7fffffff, 0x00020000);
  u32* cnt = (u32*)(ws + WS_CNT);
  const int idx = b >> 3, tt = ((b & 7) >> 1) * 8 + (idx & 7), sl = (b & 1) * 4 + (idx >> 3);
  const int pbase = 7 * sl - (sl >> 2), pcnt = (sl & 3) == 3 ? 6 : 7;
  const bool active = wv < pcnt;
  const int p0 = pbase + (active ? wv : 0);
  unsigned long long t_start;
  TS(1, 0);

  const bool three = b < 128;
  const int d0 = three ? 3 * b : 384 + 2 * (b - 128);
  const u32 l16 = lane * 16;

  {
    u32* pcs = (u32*)(smem + MN_PC);
    if (tid < 4) pcs[tid] = 0u;
    u32* fl = (u32*)(ws + WS_FL);
    u32 tag = 0;
    if (wv >= 4) {
      const u32 ep = __hip_atomic_load(fl + 8192, __ATOMIC_RELAXED, __HIP_MEMORY_SCOPE_AGENT);
      const unsigned long long h = (llvm_dispatch_id() * 0x9E3779B97F4A7C15ull) ^ ((unsigned long long)__builtin_amdgcn_dispatch_ptr() * 0xC2B2AE3D27D4EB4Full);
      tag = ep + 1u + ((u32)(h >> 33) & 0x3fffffffu);
    }
    __syncthreads();
    const int nkg = three ? 6 : 4;
    if (wv < nkg) {
      kgen_pair(d0 + (wv >> 1), wv & 1, lane, Are, Aim, Bre, Bim, Cre, Cim, ldt, smem + (wv >> 1) * CV_CH, pcs + (wv >> 1));
    } else {
      if (three) {
        const int un = b * 2 + wv - 6;
        if (un < 240) mod_unit(un, lane, cvec, wm, rs);
      } else if (wv < 6) {
        ln1_pair((b - 128) * 16, tid - 256, x, rs, smem + 2 * CV_CH, pcs + 2);
      } else {
        const int tl = (b - 128) * 2 + wv - 6;
        w1_tile(tl, lane, w1, rs, smem + MN_R1 + (wv - 6) * 8192);
        if (tl + 256 < 280) w1_tile(tl + 256, lane, w1, rs, smem + MN_R1 + (wv - 6) * 8192);
      }
      if (wv == 7) {
        __builtin_amdgcn_raw_buffer_store_b64(u32x2{0u, 0u}, rs, lane * 8, (u32)WS_ST + (tt * 8 + sl) * 512, 16);
        if (lane == 0) __hip_atomic_store(cnt + (16 + tt * 8 + sl) * 64, 0u, __ATOMIC_RELAXED, __HIP_MEMORY_SCOPE_AGENT);
        if (b < 16 && lane < 8) __hip_atomic_store(cnt + b * 64 + lane, 0u, __ATOMIC_RELAXED, __HIP_MEMORY_SCOPE_AGENT);
      }
      asm volatile("s_waitcnt vmcnt(0)" ::: "memory");
      u32* mine = fl + ((b & 7) * 32 + idx) * 4;
      if (three) { if (lane < 2) __hip_atomic_store(mine + 2 * (wv - 6) + lane, tag, __ATOMIC_RELAXED, __HIP_MEMORY_SCOPE_AGENT); }
      else if (lane == 0) __hip_atomic_store(mine + wv - 4, tag, __ATOMIC_RELAXED, __HIP_MEMORY_SCOPE_AGENT);
      if (wv == 6 && b < 8) {
        for (int spins = 0; spins < (1 << 20); ++spins) {
          const u32x2 v = __builtin_amdgcn_raw_buffer_load_b64(rs, lane * 8, (u32)WS_FL + b * 512, 16);
          if (__all(v[0] == tag && v[1] == tag)) break;
          __builtin_amdgcn_s_sleep(1);
        }
        if (lane < 8) __hip_atomic_store(fl + 4096 + lane * 64 + b, tag, __ATOMIC_RELAXED, __HIP_MEMORY_SCOPE_AGENT);
      }
      if (wv == 7) {
        for (int spins = 0; spins < (1 << 20); ++spins) {
          u32 v = tag;
          if (lane < 8) v = __hip_atomic_load(fl + 4096 + (b & 7) * 64 + lane, __ATOMIC_RELAXED, __HIP_MEMORY_SCOPE_AGENT);
          if (__all(v == tag)) break;
          __builtin_amdgcn_s_sleep(1);
        }
        if (b == 0 && lane == 0) __hip_atomic_store(fl + 8192, tag, __ATOMIC_RELAXED, __HIP_MEMORY_SCOPE_AGENT);
      }
    }
    __syncthreads();
  }
  TS(3, 6);

  const int dA = d0 + hw, dC = three ? d0 + 2 : d0;
  bf16x8 af[W1_RING][2];
  const u32 w1o = (u32)WS_W1F + (u32)(2 * p0) * 20 * 1024;
  {
    auto ldf = [&](u32 off) { return __uint_as_float(__builtin_amdgcn_raw_buffer_load_b32(rs, off, (u32)WS_MODP, 16)); };
    const int mj = (lane & 15) >> 2;
    float mp = ldf(((lane & 3) * (6 * D_) + (mj < 3 ? mj : 4) * D_ + ((lane & 16) ? dC : dA)) * 4);
    const int tm1 = t256 > 0 ? t256 - 1 : 0;
    const u32x4 xA0 = ld_sc1(rs, tm1 * 16, (u32)WS_XT + dA * (L_ * 2));
    const u32x4 xA = ld_sc1(rs, t256 * 16, (u32)WS_XT + dA * (L_ * 2));
    u32x4 sq[4];
#pragma unroll
    for (int i = 0; i < 4; ++i) sq[i] = ld_sc1(rs, (i < 2 ? tm1 : t256) * 32 + (i & 1) * 16, (u32)WS_NT);
    const u32x4 c0 = ld_sc1(rs, tm1 * 16, (u32)WS_XT + dC * (L_ * 2)), c1 = ld_sc1(rs, t256 * 16, (u32)WS_XT + dC * (L_ * 2));
    const u16 dskAr = Dsk[dA], dskCr = Dsk[dC];
    __builtin_amdgcn_sched_barrier(0);
    asm volatile("" ::"v"(xA0), "v"(c0), "v"(sq[0]));
    TS(3, 2);
    const float dskA = bf2f(dskAr), dskC = bf2f(dskCr);
    char* smA = smem + hw * CV_CH;
    char* smC = smem + 2 * CV_CH;
    mp += __shfl_xor(mp, 1); mp += __shfl_xor(mp, 2);
    mp = rb(mp);
    auto rl = [&](int l) { return __uint_as_float(__builtin_amdgcn_readlane(__float_as_uint(mp), l)); };
    const float saA = rl(0), caA = rl(4), gaA = rl(8), c1A = rb(1.f + rl(12));
    const float saC = rl(16), caC = rl(20), gaC = rl(24), c1C = rb(1.f + rl(28));
    float mn[16], rsd[16];
#pragma unroll
    for (int i = 2; i < 16; ++i) { mn[i] = lo2f(sq[i >> 2][i & 3]); rsd[i] = __builtin_amdgcn_rcpf(hi2f(sq[i >> 2][i & 3])); }
    mn[0] = mn[1] = rsd[0] = rsd[1] = 0.f;
    conv_stage_h(smA, xA0, xA, mn, rsd, rb(1.f + caA), saA, t256);
    if (three && hw == 1) conv_stage_h(smC, c0, c1, mn, rsd, rb(1.f + caC), saC, t256);
    __builtin_amdgcn_sched_barrier(0);
    TS(3, 3);
    const int t1 = (b - 128) * 4 + wv;
    const bool do1 = !three && wv < 4 && t1 < 432;
    u32x4 rv[5];
    f32x4 accA[8], accC[8];
    if (do1) {
#pragma unroll
      for (int i = 0; i < 5; ++i) rv[i] = ld_sc1(rs, l16, (u32)WS_W1F + (t1 * 5 + i) * 1024);
      const u32 mq = (160 * (t1 & 3) + 4 * (lane < 40 ? lane : 0)) * 4;
#pragma unroll
      for (int k = 0; k < KSM; ++k) {
        accC[k] = __builtin_bit_cast(f32x4, ld_sc1(rs, mq, (u32)WS_MODP + (k * (6 * D_) + 4 * D_) * 4));
        accC[4 + k] = __builtin_bit_cast(f32x4, ld_sc1(rs, mq, (u32)WS_MODP + (k * (6 * D_) + 3 * D_) * 4));
      }
    }
    const int t2 = (b - 128) * 3 + wv - 4;
    const bool do2 = !three && wv >= 4 && wv < 7 && t2 < 270;
    if (do2) {
#pragma unroll
      for (int i = 0; i < 8; ++i) accC[i] = *(const f32x4*)(w2 + (size_t)(32 * (t2 / 5) + 4 * i + g) * D_ + 128 * (t2 % 5) + 8 * r16);
    }
    f32x4 gbp;
#pragma unroll
    for (int k = 0; k < KSM; ++k) gbp[k] = ldf((k * (6 * D_) + 5 * D_ + sl * 80 + (tid < 80 ? tid : 0)) * 4);
    __builtin_amdgcn_sched_barrier(0);
    __syncthreads();
    TS(1, 1);
    conv_mma(smA, w4, lane, accA, [&](int it) {
      if (it < 5 || (it >= 8 && it < 13)) {
        const int ks = it < 5 ? it : it - 3;
#pragma unroll
        for (int fb = 0; fb < 2; ++fb) af[ks][fb] = ld_frag1(rs, l16, w1o + (fb * 20 + ks) * 1024);
      }
    });
    if (three && hw == 0) conv_mma(smC, w4, lane, accC, [](int) {});
    if (tid < 80) ((float*)(smem + MN_GB))[tid] = rb((gbp[0] + gbp[1]) + (gbp[2] + gbp[3]));
    if (do2) {
      char* buf = smC + 4096 + (wv - 4) * 8192;
#pragma unroll
      for (int i = 0; i < 8; ++i) {
        const int row = 4 * i + g;
        *(f32x4*)(buf + row * 256 + (((r16 >> 1) ^ rl_swz(row)) << 5) + ((r16 & 1) << 4)) = accC[i];
      }
      wave_sync();
      const int row0 = 4 * g + q, row1 = row0 + 16;
#pragma unroll
      for (int cb = 0; cb < 8; ++cb) {
        const s16x4 lo = tr_read(buf + row0 * 256 + ((cb ^ rl_swz(row0)) << 5) + 8 * p);
        const s16x4 hi = tr_read(buf + row1 * 256 + ((cb ^ rl_swz(row1)) << 5) + 8 * p);
        st_sc1(rs, l16, (u32)WS_W2F + ((8 * (t2 % 5) + cb) * 54 + t2 / 5) * 1024, __builtin_bit_cast(u32x4, cat8(lo, hi)));
      }
    }
    if (do1) {
      char* buf = smC + wv * 1024;
      const f32x4 mc = (accC[0] + accC[1]) + (accC[2] + accC[3]), ms = (accC[4] + accC[5]) + (accC[6] + accC[7]);
      if (lane < 40) {
        *(u32x2*)(buf + lane * 8) = u32x2{cvtpk(1.f + rb(mc[0]), 1.f + rb(mc[1])), cvtpk(1.f + rb(mc[2]), 1.f + rb(mc[3]))};
        *(u32x2*)(buf + 512 + lane * 8) = u32x2{cvtpk(ms[0], ms[1]), cvtpk(ms[2], ms[3])};
      }
      wave_sync();
      float S = 0.f, B = 0.f;
#pragma unroll
      for (int i = 0; i < 5; ++i) {
        const u32x4 c1p = *(const u32x4*)(buf + i * 64 + g * 16), sbp = *(const u32x4*)(buf + 512 + i * 64 + g * 16);
#pragma unroll
        for (int k = 0; k < 4; ++k) { S = dot2bf(rv[i][k], c1p[k], S); B = dot2bf(rv[i][k], sbp[k], B); }
      }
      S += __shfl_xor(S, 16); S += __shfl_xor(S, 32);
      B += __shfl_xor(B, 16); B += __shfl_xor(B, 32);
      if (lane < 32)
        __builtin_amdgcn_raw_buffer_store_b32(__float_as_uint(lane < 16 ? S : B), rs, (g * 4 * F_ + r16) * 4, (u32)WS_SB + ((t1 & 3) * F_ + 16 * (t1 >> 2)) * 4, 16);
    }
    __syncthreads();
    TS(1, 2);
    conv_put(smA, w4, lane, accA);
    if (three && hw == 0) conv_put(smC, w4, lane, accC);
    __syncthreads();
    u32x4 os;
    st_sc1(rs, t256 * 16, (u32)WS_X1T + dA * (L_ * 2), conv_fin(smA, t256, xA, dskA, gaA, c1A, os));
    st_sc1(rs, t256 * 16, (u32)WS_X1S + dA * (L_ * 2), os);
    if (three && hw == 1) {
      st_sc1(rs, t256 * 16, (u32)WS_X1T + dC * (L_ * 2), conv_fin(smC, t256, c1, dskC, gaC, c1C, os));
      st_sc1(rs, t256 * 16, (u32)WS_X1S + dC * (L_ * 2), os);
    }
  }
  asm volatile("s_waitcnt vmcnt(0)" ::: "memory");
  __syncthreads();
  TS(1, 3);
  if (wv == 7) {
    u32 o = 0;
    if (lane == 0) o = __hip_atomic_fetch_add(cnt + (b & 7) * 64, 1u, __ATOMIC_RELAXED, __HIP_MEMORY_SCOPE_AGENT);
    o = __builtin_amdgcn_readfirstlane(o);
    if (o == 31u && lane < 8) __hip_atomic_store(cnt + (8 + lane) * 64 + (b & 7), 1u, __ATOMIC_RELAXED, __HIP_MEMORY_SCOPE_AGENT);
    for (int spins = 0; spins < (1 << 20); ++spins) {
      u32 v = 1u;
      if (lane < 8) v = __hip_atomic_load(cnt + (8 + (b & 7)) * 64 + lane, __ATOMIC_RELAXED, __HIP_MEMORY_SCOPE_AGENT);
      if (__all(v != 0u)) break;
      __builtin_amdgcn_s_sleep(1);
    }
  }
  __syncthreads();
  TS(1, 4);

  const int k0 = 7 * wv - (wv >> 2), nk = (wv & 3) == 3 ? 6 : 7;
  const u32 w2o = (u32)WS_W2F + (u32)((sl * 5) * 54 + k0) * 1024;
  bf16x8 wf[7][5];
  {
    const int l8 = tid & 7, crow = tid >> 3;
    u32x4 xv[12];
    f32x4 sb4;
    f32x4 acc[2][4];
#pragma unroll
    for (int fb = 0; fb < 2; ++fb)
#pragma unroll
      for (int tb = 0; tb < 4; ++tb) acc[fb][tb] = f32x4{0.f, 0.f, 0.f, 0.f};
    if (wv < 7) {
#pragma unroll
      for (int qq = 0; qq < 12; ++qq)
        xv[qq] = ld_sc1(rs, (qq < 11 || crow < 24 ? crow : 0) * (L_ * 2) + l8 * 16, (u32)WS_X1S + qq * (56 * L_ * 2) + tt * 128);
      __builtin_amdgcn_sched_barrier(0);
#pragma unroll
      for (int qq = 0; qq < 12; ++qq) {
        const int c = crow + 56 * qq;
        if (qq < 11 || crow < 24) *(u32x4*)(smem + M1_A + c * 128 + (((l8 >> 1) ^ h2_swz(c)) << 5) + ((l8 & 1) << 4)) = xv[qq];
      }
    } else {
      __builtin_amdgcn_s_setprio(3);
#pragma unroll
      for (int i = 0; i < 10; ++i) af[i >> 1][i & 1] = ld_frag1(rs, (lane >> 3) * (L_ * 2) + (lane & 7) * 16, (u32)WS_X1T + (sl * 80 + 8 * i) * (L_ * 2) + tt * 128);
    }
    __syncthreads();
    TS(1, 5);
    TC(3, 0);
    const int row0 = 8 * g + q, sw = h2_swz(row0);
    const char* rb0 = smem + M1_A + row0 * 128 + 8 * p;
    float* st = (float*)(smem + M1_ST);
    auto ldb = [&](int ks, bf16x8 (&bfr)[4]) {
#pragma unroll
      for (int tb = 0; tb < 4; ++tb) {
        const s16x4 lo = tr_read(rb0 + ks * 4096 + ((tb ^ sw) << 5));
        const s16x4 hi = tr_read(rb0 + ks * 4096 + 512 + ((tb ^ sw) << 5));
        bfr[tb] = cat8(lo, hi);
      }
    };
    bf16x8 bb[2][4];
    ldb(0, bb[0]);
#pragma unroll
    for (int ks = 0; ks < 20; ++ks) {
      if (active) {
        if (ks < 19) ldb(ks + 1, bb[(ks + 1) & 1]);
#pragma unroll
        for (int fb = 0; fb < 2; ++fb)
#pragma unroll
          for (int tb = 0; tb < 4; ++tb) acc[fb][tb] = mfma16(af[ks % W1_RING][fb], bb[ks & 1][tb], acc[fb][tb]);
        if (ks + W1_RING < 20) {
#pragma unroll
          for (int fb = 0; fb < 2; ++fb) af[ks % W1_RING][fb] = ld_frag1(rs, l16, w1o + (fb * 20 + ks + W1_RING) * 1024);
        }
      } else if (ks == 0 && wv == 7) {
#pragma unroll
        for (int i = 0; i < 10; ++i) {
          const u32x4 v = __builtin_bit_cast(u32x4, af[i >> 1][i & 1]);
          const f32x4 a0 = {lo2f(v[0]), hi2f(v[0]), lo2f(v[1]), hi2f(v[1])}, a1 = {lo2f(v[2]), hi2f(v[2]), lo2f(v[3]), hi2f(v[3])};
          acc[0][0] += a0; acc[0][1] += a1; acc[0][2] += a0 * a0; acc[0][3] += a1 * a1;
        }
        const bool b5 = lane & 32, b4 = lane & 16, b3 = lane & 8;
        float v4[2][4], v2[2][2], v1[2];
#pragma unroll
        for (int h = 0; h < 2; ++h) {
#pragma unroll
          for (int j = 0; j < 4; ++j) {
            const float lo = acc[0][2 * h][j], hi = acc[0][2 * h + 1][j];
            v4[h][j] = (b5 ? hi : lo) + __shfl_xor(b5 ? lo : hi, 32);
          }
#pragma unroll
          for (int j = 0; j < 2; ++j) v2[h][j] = (b4 ? v4[h][2 + j] : v4[h][j]) + __shfl_xor(b4 ? v4[h][j] : v4[h][2 + j], 16);
          v1[h] = (b3 ? v2[h][1] : v2[h][0]) + __shfl_xor(b3 ? v2[h][0] : v2[h][1], 8);
        }
        const u32 toff = (8 * (lane & 7) + (lane >> 3)) * 8;
        __builtin_amdgcn_raw_buffer_store_b64(u32x2{__float_as_uint(v1[0]), __float_as_uint(v1[1]) | 0x80000000u}, rs, toff,
                                              (u32)WS_ST + (tt * 8 + sl) * 512, 16);
#pragma unroll
        for (int i = 0; i < 10; ++i) {
          const u32x4 v = __builtin_bit_cast(u32x4, af[i >> 1][i & 1]);
          char* dp = smem + M2_X + 8 * (lane & 7) * M2_XS + 2 * (8 * i + (lane >> 3));
#pragma unroll
          for (int k = 0; k < 4; ++k) {
            *(u16*)(dp + (2 * k) * M2_XS) = (u16)(v[k] & 0xffffu);
            *(u16*)(dp + (2 * k + 1) * M2_XS) = (u16)(v[k] >> 16);
          }
        }
        u32x2 gv[8];
        for (int spins = 0; spins < (1 << 20); ++spins) {
          u32 ok = 0x80000000u;
#pragma unroll
          for (int j = 0; j < 8; ++j) { gv[j] = __builtin_amdgcn_raw_buffer_load_b64(rs, lane * 8, (u32)WS_ST + (tt * 8 + j) * 512, 16); ok &= gv[j][1]; }
          if (__all(ok != 0u)) break;
          __builtin_amdgcn_s_sleep(2);
        }
        float sm = 0.f, sq = 0.f;
#pragma unroll
        for (int j = 0; j < 8; ++j) { sm += __uint_as_float(gv[j][0]); sq += __uint_as_float(gv[j][1] & 0x7fffffffu); }
        const float mean = sm * (1.f / D_);
        const float var = fmaxf(sq * (1.f / D_) - mean * mean, 0.f);
        st[lane] = rb(mean);
        st[64 + lane] = 1.f / rb(sqrtf(rb(rb(var) + 1e-5f)));
        __builtin_amdgcn_s_setprio(0);
      }
      if (ks >= 10) {
#pragma unroll
        for (int j = 0; j < 2; ++j) {
          const int f = 2 * (ks - 10) + j;
          wf[f / 5][f % 5] = LD_W2(rs, l16, w2o + ((f % 5) * 54 + f / 5) * 1024);
        }
      }
      if (ks == 9) {
        sb4 = __builtin_bit_cast(f32x4, ld_sc1(rs, ((lane >> 3) * F_ + 4 * (lane & 7)) * 4, (u32)WS_SB + 32 * p0 * 4));
      }
      if (ks == 17) {
#pragma unroll
        for (int e = 0; e < 4; ++e) { sb4[e] += __shfl_xor(sb4[e], 8); sb4[e] += __shfl_xor(sb4[e], 16); }
        if ((lane & 24) == 0) *(f32x4*)(smem + MN_SB + wv * 256 + (lane >> 5) * 128 + (lane & 7) * 16) = sb4;
      }
      __builtin_amdgcn_sched_barrier(0);
    }
    TS(2, 1);
    TC(3, 1);
#pragma unroll
    for (int i = W2_MID; i < 7; ++i)
#pragma unroll
      for (int cb = 0; cb < 5; ++cb) wf[i][cb] = LD_W2(rs, l16, w2o + (cb * 54 + (i < 6 || nk == 7 ? i : 5)) * 1024);
    __builtin_amdgcn_sched_barrier(0);
    __syncthreads();
    TS(2, 2);
    if (active) {
      const float* sbp = (const float*)(smem + MN_SB) + wv * 64 + 4 * g;
      const u32 hoff = (u32)WS_HID + (u32)((tt * 4) * 54 + p0) * 1024;
#pragma unroll
      for (int tb = 0; tb < 4; ++tb) {
        const float mb = st[tb * 16 + r16], rsd = st[64 + tb * 16 + r16];
        u32x4 v;
#pragma unroll
        for (int hh = 0; hh < 2; ++hh) {
          const f32x4 sv = *(const f32x4*)(sbp + 16 * hh), bv = *(const f32x4*)(sbp + 32 + 16 * hh);
          const f32x4 a = (acc[hh][tb] - mb * sv) * rsd + bv;
          v[2 * hh] = cvtpk(fmaxf(a[0], 0.f), fmaxf(a[1], 0.f));
          v[2 * hh + 1] = cvtpk(fmaxf(a[2], 0.f), fmaxf(a[3], 0.f));
        }
        st_sc1(rs, l16, hoff + tb * 54 * 1024, v);
      }
    }
  }
  TS(2, 3);
  asm volatile("s_waitcnt vmcnt(0)" ::: "memory");
  __syncthreads();
  TS(2, 4);
  if (tid == 448) __hip_atomic_store(cnt + (16 + tt * 8 + sl) * 64, 1u, __ATOMIC_RELAXED, __HIP_MEMORY_SCOPE_AGENT);
  __builtin_amdgcn_sched_barrier(0);
  poll_words<1>(cnt + (16 + tt * 8 + wv) * 64, 1u, lane);
  TS(1, 6);

  {
    const u32 hoff = (u32)WS_HID + (u32)((tt * 4) * 54 + k0) * 1024;
    u32x4 hf[2][4];
#pragma unroll
    for (int i = 0; i < 2; ++i)
#pragma unroll
      for (int tb = 0; tb < 4; ++tb) hf[i][tb] = ld_sc1(rs, l16, hoff + (tb * 54 + i) * 1024);
    f32x4 acc[5][4];
#pragma unroll
    for (int cb = 0; cb < 5; ++cb)
#pragma unroll
      for (int tb = 0; tb < 4; ++tb) acc[cb][tb] = f32x4{0.f, 0.f, 0.f, 0.f};
#pragma unroll
    for (int i = 0; i < 7; ++i) {
      if (i < 6 || nk == 7) {
#pragma unroll
        for (int tb = 0; tb < 4; ++tb)
#pragma unroll
          for (int cb = 0; cb < 5; ++cb) acc[cb][tb] = mfma16(wf[i][cb], __builtin_bit_cast(bf16x8, hf[i & 1][tb]), acc[cb][tb]);
      }
      __builtin_amdgcn_sched_barrier(0);
      if (i + 2 < 7) {
        const int kk = (i + 2 < 6 || nk == 7) ? i + 2 : 5;
#pragma unroll
        for (int tb = 0; tb < 4; ++tb) hf[i & 1][tb] = ld_sc1(rs, l16, hoff + (tb * 54 + kk) * 1024);
        __builtin_amdgcn_sched_barrier(0);
      }
    }
    TS(2, 5);
#pragma unroll
    for (int tb = 0; tb < 4; ++tb)
#pragma unroll
      for (int cb = 0; cb < 5; ++cb) {
        const int owner = tb + (cb < 3 ? 0 : 4);
        if (owner != wv) {
          const int slot = wv < owner ? wv : wv - 1;
          *(f32x4*)(smem + M2_RED + (((tb * 5 + cb) * 7 + slot) * 64 + lane) * 16) = acc[cb][tb];
        }
      }
    f32x4 own[3];
#pragma unroll
    for (int o = 0; o < 8; ++o) {
      if (wv == o) {
        asm volatile("" ::: "memory");
#pragma unroll
        for (int j = 0; j < 3; ++j) own[j] = acc[o < 4 ? j : (j < 2 ? 3 + j : 4)][o & 3];
      }
    }
    __syncthreads();
    TS(2, 6);
    const int tok = 16 * w4 + r16, cb0 = wv < 4 ? 0 : 3;
    f32x4 pr[3][7], gb4[3];
    u32x2 xr[3];
#pragma unroll
    for (int j = 0; j < 3; ++j) {
      const int cb = (j < 2 || wv < 4) ? cb0 + j : 4;
#pragma unroll
      for (int s7 = 0; s7 < 7; ++s7) pr[j][s7] = *(const f32x4*)(smem + M2_RED + (((w4 * 5 + cb) * 7 + s7) * 64 + lane) * 16);
      gb4[j] = *(const f32x4*)(smem + MN_GB + (16 * cb + 4 * g) * 4);
      xr[j] = *(const u32x2*)(smem + M2_X + tok * M2_XS + 2 * (16 * cb + 4 * g));
    }
    __builtin_amdgcn_sched_barrier(0);
#pragma unroll
    for (int j = 0; j < 3; ++j) {
      const f32x4 y = ((own[j] + pr[j][0]) + (pr[j][1] + pr[j][2])) + ((pr[j][3] + pr[j][4]) + (pr[j][5] + pr[j][6]));
      const u32 y0 = cvtpk(y[0], y[1]), y1 = cvtpk(y[2], y[3]);
      const u32 v0 = cvtpk(lo2f(y0) * gb4[j][0], hi2f(y0) * gb4[j][1]), v1 = cvtpk(lo2f(y1) * gb4[j][2], hi2f(y1) * gb4[j][3]);
      u32x2 o;
      o[0] = cvtpk(lo2f(xr[j][0]) + lo2f(v0), hi2f(xr[j][0]) + hi2f(v0));
      o[1] = cvtpk(lo2f(xr[j][1]) + lo2f(v1), hi2f(xr[j][1]) + hi2f(v1));
      if (j < 2 || wv < 4) __builtin_amdgcn_raw_buffer_store_b64(o, __builtin_amdgcn_make_buffer_rsrc((void*)out, 0, 0x7fffffff, 0x00020000), ((tt * 64 + tok) * D_ + sl * 80 + 16 * (cb0 + j) + 4 * g) * 2, 0, AUX);
    }
  }
  TS(1, 7);
}

extern "C" void kernel_launch(void* const* d_in, const int* in_sizes, int n_in, void* d_out, int out_size, void* d_ws,
                              size_t ws_size, hipStream_t stream) {
  char* ws = (char*)d_ws;
  k_main<<<256, 512, 0, stream>>>(
      (const u16*)d_in[0], (const u16*)d_in[1], (const u16*)d_in[2], (const u16*)d_in[3], (const u16*)d_in[4],
      (const u16*)d_in[5], (const u16*)d_in[6], (const u16*)d_in[7], (const u16*)d_in[8], (const u16*)d_in[10],
      (const u16*)d_in[11], (const u16*)d_in[12], (const u16*)d_in[13], ws, (u16*)d_out);
}
```

```cpp
#define PIN
#define PUB_FIRST
#define GO_IT 5
#define W1_DELAY 16
#define CODE_WARM 7
#include <hip/hip_runtime.h>
#include <hip/hip_bf16.h>
#include <stdint.h>

extern "C" __device__ unsigned long long llvm_dispatch_id() __asm("llvm.amdgcn.dispatch.id");
typedef unsigned short u16;
typedef unsigned int u32;
typedef __attribute__((ext_vector_type(8))) short bf16x8;
typedef __attribute__((ext_vector_type(4))) short s16x4;
typedef __attribute__((ext_vector_type(4))) float f32x4;
typedef __attribute__((ext_vector_type(2))) float f32x2;
typedef __attribute__((ext_vector_type(4))) unsigned int u32x4;
typedef __attribute__((ext_vector_type(2))) unsigned int u32x2;
typedef __attribute__((ext_vector_type(3))) unsigned int u32x3;
typedef __bf16 bf16x2_t __attribute__((ext_vector_type(2)));
#define LDS_AS __attribute__((address_space(3)))
#define DI __device__ __forceinline__

constexpr int L_ = 2048, D_ = 640, N_ = 128, F_ = 1728;
constexpr int KSM = 4;

constexpr size_t WS_MODP = 0;
constexpr size_t WS_CNT = 126976;
constexpr size_t WS_FL = 196608;
constexpr size_t WS_NT = 262144;
constexpr size_t WS_XB = 40u << 20;
constexpr size_t WS_XT = WS_NT + (size_t)D_ * L_ * 2;
constexpr size_t WS_X1T = WS_XT + (size_t)D_ * L_ * 2;
constexpr size_t WS_X1S = WS_X1T + (size_t)D_ * L_ * 2;
constexpr int KTAB = 1296;
constexpr size_t WS_KT = WS_X1S + (size_t)D_ * L_ * 2;
constexpr size_t WS_SB = WS_KT + (size_t)D_ * KTAB;
constexpr size_t WS_W1F = WS_SB + 2 * 20 * 1728 * 4;
constexpr size_t WS_W2F = WS_W1F + (size_t)108 * 20 * 1024;
constexpr size_t WS_HID = WS_W2F + (size_t)40 * 54 * 1024;
constexpr size_t WS_ST = WS_HID + (size_t)128 * 54 * 1024;

#define TS(kern, slot) do {} while (0)
#define TW(slot) do {} while (0)
#define TC(kern, slot) do {} while (0)

DI void st_wt(void* p, u32x4 v) { asm volatile("global_store_dwordx4 %0, %1, off sc1\n\ts_nop 1" ::"v"(p), "v"(v) : "memory"); }
#define ST_PRE(p, v) (*(p) = (v))
#define ST_PREW(p, v) (*(p) = (v))
DI u32 cvtpk(float lo, float hi) {
  f32x2 v = {lo, hi};
  bf16x2_t r = __builtin_convertvector(v, bf16x2_t);
  return __builtin_bit_cast(u32, r);
}
typedef _Float16 f16x2_t __attribute__((ext_vector_type(2)));
typedef _Float16 f16x8_t __attribute__((ext_vector_type(8)));
DI u32 cvtph(float lo, float hi) {
  f32x2 v = {lo, hi};
  f16x2_t r = __builtin_convertvector(v, f16x2_t);
  return __builtin_bit_cast(u32, r);
}
DI float dot2bf(u32 a, u32 b, float c) {
  return __builtin_amdgcn_fdot2_f32_bf16(__builtin_bit_cast(bf16x2_t, a), __builtin_bit_cast(bf16x2_t, b), c, false);
}
DI float lo2f(u32 v) { return __uint_as_float(v << 16); }
DI float hi2f(u32 v) { return __uint_as_float(v & 0xffff0000u); }
DI float bf2f(u16 v) { return __uint_as_float(((u32)v) << 16); }
DI float rb(float x) { return lo2f(cvtpk(x, x)); }
DI f32x4 mfma16(bf16x8 a, bf16x8 b, f32x4 c) { return __builtin_amdgcn_mfma_f32_16x16x32_bf16(a, b, c, 0, 0, 0); }
DI s16x4 tr_read(const char* p) {
  return __builtin_amdgcn_ds_read_tr16_b64_v4i16((LDS_AS s16x4*)p);
}
DI bf16x8 cat8(s16x4 a, s16x4 b) { return __builtin_shufflevector(a, b, 0, 1, 2, 3, 4, 5, 6, 7); }
DI void wave_sync() {
  __builtin_amdgcn_fence(__ATOMIC_RELEASE, "wavefront");
  __builtin_amdgcn_wave_barrier();
  __builtin_amdgcn_fence(__ATOMIC_ACQUIRE, "wavefront");
}

DI f32x2 cmul2(f32x2 a, f32x2 b) {
  const f32x2 bj = {-b.y, b.x};
  return __builtin_elementwise_fma(a.yy, bj, a.xx * b);
}
struct c32 { float r, i; };
DI c32 cmul(c32 a, c32 b) { return {a.r * b.r - a.i * b.i, a.r * b.i + a.i * b.r}; }
DI c32 cexp_scaled(float ar, float ai, float e) {
  const float mag = __expf(ar * e);
  const float x = ai * e;
  const float C_HI = 0.15915494f, C_LO = 6.4206382e-9f;
  const float r = x * C_HI;
  const float rl = fmaf(x, C_HI, -r) + x * C_LO;
  const float fr = (r - floorf(r)) + rl;
  return {mag * __builtin_amdgcn_cosf(fr), mag * __builtin_amdgcn_sinf(fr)};
}
DI int tap_index(int m) {
  u32 bits = __float_as_uint((float)m);
  bits += 0x7fffu + ((bits >> 16) & 1u);
  const int id2 = (int)(bits >> 16) - (0x4300 - 128);
  return m < 128 ? m : id2;
}

DI int rl_swz(int row) { return (row & 3) | ((((row >> 2) ^ (row >> 3)) & 1) << 2); }

constexpr int CV_H0 = 0, CV_HS = 4160, CV_K = 12544, CV_T = 21760, CV_H3 = 32768 + 192, CV_CH = 37376;
static_assert(CV_CH % 256 == 0 && CV_H3 + 4160 <= CV_CH && CV_T + 4 * KTAB <= 32768, "conv lds");
constexpr int M1_A = 0, M1_ST = 81920;
constexpr int MN_SB = 3 * CV_CH;
constexpr int MN_R1 = MN_SB + 2048;
constexpr int M2_RED = 0, M2_X = 143360, M2_XS = 176;
constexpr int MN_GB = M2_X + 64 * M2_XS;
constexpr int MN_PC = MN_GB + 320;
constexpr int MN_SMEM = MN_PC + 32;
static_assert(MN_R1 + 6 * 4352 <= M2_X && M1_ST + 512 <= MN_SB, "mlp lds");

typedef __amdgpu_buffer_rsrc_t rsrc_t;
DI u32x4 ld_sc1(rsrc_t r, u32 voff, u32 soff) { return __builtin_amdgcn_raw_buffer_load_b128(r, voff, soff, 16); }
DI void st_sc1(rsrc_t r, u32 voff, u32 soff, u32x4 v) { __builtin_amdgcn_raw_buffer_store_b128(v, r, voff + soff, 0, 16); }
DI bf16x8 ld_frag1(rsrc_t r, u32 voff, u32 soff) { return __builtin_bit_cast(bf16x8, __builtin_amdgcn_raw_buffer_load_b128(r, voff, soff, 16)); }
DI bf16x8 ld_frag(rsrc_t r, u32 voff, u32 soff) { return __builtin_bit_cast(bf16x8, __builtin_amdgcn_raw_buffer_load_b128(r, voff, soff, 0)); }

DI int h2_swz(int k) { return ((k >> 1) & 1) | (((k >> 3) & 1) << 1); }

DI void wg_barrier() {
  __builtin_amdgcn_fence(__ATOMIC_RELEASE, "workgroup");
  __builtin_amdgcn_s_barrier();
  __builtin_amdgcn_fence(__ATOMIC_ACQUIRE, "workgroup");
}
DI void pair_sync(u32* c, u32 target) {
  __builtin_amdgcn_fence(__ATOMIC_RELEASE, "workgroup");
  if ((threadIdx.x & 63) == 0) __hip_atomic_fetch_add(c, 1u, __ATOMIC_RELAXED, __HIP_MEMORY_SCOPE_WORKGROUP);
#pragma unroll 1
  for (int spins = 0; spins < (1 << 22); ++spins) {
    if (__hip_atomic_load(c, __ATOMIC_RELAXED, __HIP_MEMORY_SCOPE_WORKGROUP) >= target) break;
    __builtin_amdgcn_s_sleep(1);
  }
  __builtin_amdgcn_fence(__ATOMIC_ACQUIRE, "workgroup");
}

template <class F0, class F1>
DI void kgen_pair(int d, int hi, int lane, const u16* __restrict__ Are, const u16* __restrict__ Aim, const u16* __restrict__ Bre,
                  const u16* __restrict__ Bim, const u16* __restrict__ Cre, const u16* __restrict__ Cim,
                  const u16* __restrict__ ldt, char* smem, char* scr, u32* pc, F0&& early, F1&& late) {
  const int r16 = lane & 15, g = lane >> 4, n = hi * 64 + lane;
  const int gi = d * N_ + n;
  const u16 r0 = ldt[d], r1 = Are[gi], r2 = Aim[gi], r3 = Bre[gi], r4 = Bim[gi], r5 = Cre[gi], r6 = Cim[gi];
  __builtin_amdgcn_sched_barrier(0);
  wg_barrier();
  const float dt = rb(expf(bf2f(r0)));
  const float ar = dt * bf2f(r1), ai = dt * bf2f(r2);
  const c32 dB = {dt * bf2f(r3), dt * bf2f(r4)};
  const c32 Cc = {bf2f(r5), bf2f(r6)};
  __builtin_amdgcn_sched_barrier(0);
  early();
  __builtin_amdgcn_sched_barrier(0);
  const c32 W = cmul(Cc, dB);
  const c32 E1 = cexp_scaled(ar, ai, 1.f), E8 = cexp_scaled(ar, ai, 8.f), E128 = cexp_scaled(ar, ai, 128.f);

  int ew = (int)((__float_as_uint(fmaxf(fabsf(W.r), fabsf(W.i))) >> 23) & 0xff);
  ew = max(ew, __builtin_amdgcn_update_dpp(0, ew, 0xB1, 0xf, 0xf, true));
  ew = max(ew, __builtin_amdgcn_update_dpp(0, ew, 0x4E, 0xf, 0xf, true));
  ew = max(ew, __builtin_amdgcn_update_dpp(0, ew, 0x141, 0xf, 0xf, true));
  ew = max(ew, __builtin_amdgcn_update_dpp(0, ew, 0x140, 0xf, 0xf, true));
  ew = max(max(__builtin_amdgcn_readlane(ew, 0), __builtin_amdgcn_readlane(ew, 16)), max(__builtin_amdgcn_readlane(ew, 32), __builtin_amdgcn_readlane(ew, 48)));
  ew = ew < 20 ? 20 : (ew > 240 ? 240 : ew);
  const float wsc = __uint_as_float((u32)(127 + 10 + 127 - ew - 1) << 23);
  const float unsc = __uint_as_float((u32)(ew + 1 - 20) << 23);

  char* buf = scr + hi * 4352;
  char* wr = buf + lane * 4;
  const char* rd = buf + r16 * 272 + g * 16;
  auto put = [&](int row, f32x2 v) { *(u32*)(wr + row * 272) = cvtph(v.x, v.y); };
  {
    const f32x2 e8 = {E8.r, E8.i};
    f32x2 p = {1024.f, 0.f};
#pragma unroll
    for (int jj = 0; jj < 16; ++jj) { put(jj, p); if (jj < 15) p = cmul2(p, e8); }
  }
  wave_sync();
  f16x8_t bh[4];
#pragma unroll
  for (int ks = 0; ks < 4; ++ks) bh[ks] = *(const f16x8_t*)(rd + ks * 64);
  wave_sync();
  const f32x2 e1 = {E1.r, -E1.i}, e128 = {E128.r, -E128.i};
  f32x2 sm[8];
  sm[1] = e1;
#pragma unroll
  for (int i = 2; i < 8; ++i) sm[i] = cmul2(sm[i - 1], e1);
  f32x2 WG[17];
  WG[0] = f32x2{W.r * wsc, -W.i * wsc};
#pragma unroll
  for (int i = 1; i < 17; ++i) WG[i] = cmul2(WG[i - 1], e128);
  f32x4 acc[3];
  auto tile_mma = [&](int t) {
    wave_sync();
    f32x4 a = {0.f, 0.f, 0.f, 0.f};
#pragma unroll
    for (int ks = 0; ks < 4; ++ks) a = __builtin_amdgcn_mfma_f32_16x16x32_f16(*(const f16x8_t*)(rd + ks * 64), bh[ks], a, 0, 0, 0);
    acc[t] = a * unsc;
    wave_sync();
  };
#pragma unroll
  for (int i = 0; i < 2; ++i)
#pragma unroll
    for (int rho = 0; rho < 8; ++rho) put(8 * i + rho, rho ? cmul2(sm[rho], WG[i]) : WG[i]);
  tile_mma(0);
#pragma unroll
  for (int i = 0; i < 2; ++i)
#pragma unroll
    for (int rho = 0; rho < 4; ++rho) put(4 * i + rho, rho ? cmul2(sm[2 * rho], WG[2 + i]) : WG[2 + i]);
#pragma unroll
  for (int i = 0; i < 4; ++i)
#pragma unroll
    for (int rho = 0; rho < 2; ++rho) put(8 + 2 * i + rho, rho ? cmul2(sm[4], WG[4 + i]) : WG[4 + i]);
  tile_mma(1);
#pragma unroll
  for (int i = 0; i < 9; ++i) put(i, WG[8 + i]);
  tile_mma(2);

  if (lane < 28) *(u32x4*)(smem + CV_K + hi * 4576 + (256 + lane) * 16) = u32x4{0u, 0u, 0u, 0u};
#pragma unroll
  for (int t = 0; t < 3; ++t) *(f32x4*)(buf + t * 1024 + lane * 16) = acc[t];
  pair_sync(pc, 2u);
  u16* T = (u16*)(smem + CV_T);
  {
    const char* ob = scr + (hi ^ 1) * 4352 + lane * 16;
    if (!hi) {
      const f32x4 a0 = acc[0] + *(const f32x4*)(ob), a2 = acc[2] + *(const f32x4*)(ob + 2048);
      u16* t0 = T + 128 * (g >> 1) + 4 * (g & 1) + 8 * r16;
      *(u32x2*)t0 = u32x2{cvtpk(a0[0], a0[1]), cvtpk(a0[2], a0[3])};
      if (g < 2) {
#pragma unroll
        for (int rg = 0; rg < 4; ++rg) T[512 + 16 * (4 * g + rg) + r16] = (u16)cvtpk(a2[rg], 0.f);
      } else if (lane == 32) {
        T[640] = (u16)cvtpk(a2[0], 0.f);
      } else if (lane >= 48 && lane < 55) {
        T[641 + lane - 48] = 0;
      }
    } else {
      const f32x4 a1 = acc[1] + *(const f32x4*)(ob + 1024);
      if (g < 2) {
        *(u32x2*)(T + 256 + 64 * g + 4 * r16) = u32x2{cvtpk(a1[0], a1[1]), cvtpk(a1[2], a1[3])};
      } else {
        *(u32*)(T + 384 + 64 * (g - 2) + 2 * r16) = cvtpk(a1[0], a1[1]);
        *(u32*)(T + 384 + 64 * (g - 2) + 32 + 2 * r16) = cvtpk(a1[2], a1[3]);
      }
    }
  }
  late();
  pair_sync(pc, 4u);
  char* k0 = smem + CV_K + n * 16;
  auto pk = [](u32 lo, u32 hi16) { return lo | (hi16 << 16); };
  {
    const u32 b = T[639 - n], a = T[640 - n], c = (n & 1) ? b : a;
    const u32 aa = pk(a, a), bb = pk(b, b);
    *(u32x4*)(k0 + 4576) = u32x4{aa, pk(a, c), bb, bb};
    *(u32x4*)(k0) = u32x4{aa, pk(c, b), bb, bb};
  }
  if (!hi) {
    const u32 c = T[510 - 2 * lane], b = T[511 - 2 * lane], a = T[512 - 2 * lane];
    const u32 bb = pk(b, b), cc = pk(c, c);
    *(u32x4*)(k0 + 2048 + 4576) = u32x4{pk(a, a), bb, pk(b, c), cc};
    *(u32x4*)(k0 + 2048) = u32x4{pk(a, b), bb, cc, cc};
  } else {
    const bool lo = lane >= 32;
    const int q8 = 504 - 8 * lane, base = lo ? q8 : 128 + (q8 >> 1);
    const u32x2 w01 = *(const u32x2*)(T + base), w23 = *(const u32x2*)(T + base + 4);
    const u32 y = T[lane == 63 ? 641 : (lane >= 31 ? q8 - 1 : base)];
    auto pm = [](u32 h, u32 l, u32 s) { return __builtin_amdgcn_perm(h, l, s); };
    const u32 x21 = pm(w01[1], w01[0], 0x03020504u), x43 = pm(w23[0], w01[1], 0x03020504u), x10 = pm(0u, w01[0], 0x01000302u);
    const u32 x32 = pm(0u, w01[1], 0x01000302u), x22 = pm(0u, w01[1], 0x01000100u), x0y = pm(y, w01[0], 0x05040100u);
    *(u32x4*)(k0 + 2048 + 4576) = lo ? u32x4{pm(0u, w23[1], 0x01000302u), pm(0u, w23[0], 0x01000302u), x32, x10}
                                     : u32x4{x43, x22, x21, pm(0u, w01[0], 0x01000100u)};
    *(u32x4*)(k0 + 2048) = lo ? u32x4{pm(w23[1], w23[0], 0x03020504u), x43, x21, x0y} : u32x4{x32, x22, x10, x0y};
  }
}

template <class F0>
DI void ln1_unit(int t0, int lane, const u16* __restrict__ x, rsrc_t rs, char* buf, F0&& mid) {
  const int u = lane >> 3, j = lane & 7;
  const u32x4* xa = (const u32x4*)(x + (size_t)(t0 + u) * D_);
  u32x4 A[10];
#pragma unroll
  for (int q = 0; q < 10; ++q) A[q] = xa[j + 8 * q];
  __builtin_amdgcn_sched_barrier(0);
  const u32 tag = mid();
#pragma unroll
  for (int q = 0; q < 10; ++q) *(u32x4*)(buf + u * 1344 + (j + 8 * q) * 16) = A[q];
  wave_sync();
  {
    const char* rd = buf + ((lane >> 2) & 3) * 1344 + (lane >> 4) * 32 + (lane & 3) * 8;
#pragma unroll
    for (int q = 0; q < 10; ++q)
      st_sc1(rs, lane * 16, (u32)WS_XB + t0 * (D_ * 2) + q * 1024, __builtin_bit_cast(u32x4, cat8(tr_read(rd + q * 128), tr_read(rd + 4 * 1344 + q * 128))));
  }
  float sa = 0.f, sb = 0.f, qa = 0.f, qb = 0.f;
#pragma unroll
  for (int q = 0; q < 5; ++q)
#pragma unroll
    for (int k = 0; k < 4; ++k) {
      sa = dot2bf(A[q][k], 0x3f803f80u, sa); qa = dot2bf(A[q][k], A[q][k], qa);
      sb = dot2bf(A[5 + q][k], 0x3f803f80u, sb); qb = dot2bf(A[5 + q][k], A[5 + q][k], qb);
    }
  sa += sb; qa += qb;
#pragma unroll
  for (int o = 1; o < 8; o <<= 1) { sa += __shfl_xor(sa, o, 8); qa += __shfl_xor(qa, o, 8); }
  const float ma = sa * (1.f / D_);
  const float va = fmaxf(qa - sa * ma, 0.f);
  const u32 s0 = cvtpk(ma, sqrtf(rb(rb(va * (1.f / D_)) + 1e-5f)));
  asm volatile("s_waitcnt vmcnt(0)" ::: "memory");
  if (j == 0) __builtin_amdgcn_raw_buffer_store_b64(u32x2{s0, tag}, rs, (u32)WS_NT + (t0 + u) * 8, 0, 16);
}

template <class F0>
DI void mod_unit(int un, int lane, const u16* __restrict__ c, const u16* __restrict__ wm, rsrc_t rs, F0&& mid) {
  const int cbk = un % 60, ks = un / 60, cj = lane & 7, rg = lane >> 3;
  const u32 cr0 = *(const u32*)(c + 160 * ks + 2 * lane), cr1 = *(const u32*)(c + 160 * ks + 128 + 2 * (lane & 15));
  u32x4 v[20];
#pragma unroll
  for (int i = 0; i < 20; ++i) v[i] = *(const u32x4*)(wm + (size_t)(160 * ks + 2 * rg + (i & 1) + 16 * (i >> 1)) * (6 * D_) + 64 * cbk + 8 * cj);
  __builtin_amdgcn_sched_barrier(0);
  const u32 tag = mid();
  auto silu = [](float cv) { return cv / (1.f + __expf(-cv)); };
  const u32 s0 = cvtpk(silu(lo2f(cr0)), silu(hi2f(cr0))), s1 = cvtpk(silu(lo2f(cr1)), silu(hi2f(cr1)));
  float a[8];
#pragma unroll
  for (int k = 0; k < 8; ++k) a[k] = 0.f;
#pragma unroll
  for (int i = 0; i < 10; ++i) {
    const u32 sv = __shfl(i < 8 ? s0 : s1, (rg + 8 * i) & 63);
#pragma unroll
    for (int k = 0; k < 4; ++k) {
      a[2 * k] = dot2bf(__builtin_amdgcn_perm(v[2 * i + 1][k], v[2 * i][k], 0x05040100u), sv, a[2 * k]);
      a[2 * k + 1] = dot2bf(__builtin_amdgcn_perm(v[2 * i + 1][k], v[2 * i][k], 0x07060302u), sv, a[2 * k + 1]);
    }
  }
#pragma unroll
  for (int k = 0; k < 8; ++k) { a[k] += __shfl_xor(a[k], 8); a[k] += __shfl_xor(a[k], 16); a[k] += __shfl_xor(a[k], 32); }
  const float e0 = (rg & 2) ? ((rg & 1) ? a[6] : a[4]) : ((rg & 1) ? a[2] : a[0]);
  const float e1 = (rg & 2) ? ((rg & 1) ? a[7] : a[5]) : ((rg & 1) ? a[3] : a[1]);
  const u32 col = ks * (6 * D_) + 64 * cbk + 8 * cj + 2 * (rg & 3);
  if (lane < 32) st_sc1(rs, col * 8, (u32)WS_XT, u32x4{__float_as_uint(e0), tag, __float_as_uint(e1), tag});
  else __builtin_amdgcn_raw_buffer_store_b64(u32x2{__float_as_uint(e0), __float_as_uint(e1)}, rs, (u32)WS_MODP + col * 4, 0, 16);
}

template <class F0>
DI void w1_tile(int un, int lane, const u16* __restrict__ w1, rsrc_t rs, char* buf, F0&& mid) {
  const bool rem = un >= 260;
  const int ks = rem ? 2 * (un - 260) : un / 13, nb = rem ? 13 : un % 13;
  const int g = lane >> 4, lc = lane & 15, q = lc >> 2, p = lane & 3;
  const u16* src = w1 + (size_t)(32 * ks + g + (rem ? 4 * (lc & 8) : 0)) * F_ + 128 * nb + 8 * (rem ? lc & 7 : lc);
  u32x4 v[8];
  mid();
  __builtin_amdgcn_s_sleep(W1_DELAY);
  __builtin_amdgcn_sched_barrier(0);
#pragma unroll
  for (int i = 0; i < 8; ++i) v[i] = *(const u32x4*)(src + (size_t)(4 * i) * F_);
  __builtin_amdgcn_sched_barrier(0);
#pragma unroll
  for (int i = 0; i < 8; ++i) {
    const int row = 4 * i + g;
    *(u32x4*)(buf + row * 256 + (((lc >> 1) ^ rl_swz(row)) << 5) + ((lc & 1) << 4)) = v[i];
  }
  wave_sync();
  const int row0 = 8 * g + q, row1 = row0 + 4;
#pragma unroll
  for (int cb = 0; cb < 8; ++cb) {
    const s16x4 lo = tr_read(buf + row0 * 256 + ((cb ^ rl_swz(row0)) << 5) + 8 * p);
    const s16x4 hi = tr_read(buf + row1 * 256 + ((cb ^ rl_swz(row1)) << 5) + 8 * p);
    const int blk = (8 * nb + cb) * 20 + ks - (rem && cb >= 4 ? 79 : 0);
    st_sc1(rs, lane * 16, (u32)WS_W1F + blk * 1024, __builtin_bit_cast(u32x4, cat8(lo, hi)));
  }
}

DI void conv_stage_h(char* smem, const u32 (&xp)[2], const float (&mn)[4], const float (&rs)[4], float ca1, float sa, int tid) {
  u32 h[2];
#pragma unroll
  for (int k = 0; k < 2; ++k) {
    const u32 c = cvtpk(lo2f(xp[k]) - mn[2 * k], hi2f(xp[k]) - mn[2 * k + 1]);
    const u32 n = cvtpk(lo2f(c) * rs[2 * k], hi2f(c) * rs[2 * k + 1]);
    const u32 p = cvtpk(lo2f(n) * ca1, hi2f(n) * ca1);
    h[k] = cvtpk(lo2f(p) + sa, hi2f(p) + sa);
  }
  char* yo = smem + tid * 8;
  *(u32*)(yo + CV_H0 + 60) = h[0]; *(u32*)(yo + CV_H0 + 64) = h[1];
  *(u32x2*)(yo + CV_H0 + CV_HS + 56) = u32x2{h[0], h[1]};
  *(u32*)(yo + CV_H0 + 2 * CV_HS + 52) = h[0]; *(u32*)(yo + CV_H0 + 2 * CV_HS + 56) = h[1];
  *(u32x2*)(yo + CV_H3 + 48) = u32x2{h[0], h[1]};
  if (tid < 64) {
    const int cc = tid >> 4, yc = tid & 15;
    if (yc < 15 - cc) *(u32*)(smem + (cc == 3 ? CV_H3 : CV_H0 + cc * CV_HS) + yc * 4) = 0u;
  }
}

template <class CB>
DI void conv_mma(const char* smem, int w, int lane, f32x4 (&acc)[8], CB&& side) {
  const int r16 = lane & 15, g = lane >> 4, cpy = r16 & 3;
  const char* hb = smem + (cpy == 3 ? CV_H3 : CV_H0 + cpy * CV_HS) + 16 * ((r16 >> 2) + 4 * w + g);
  const char* kb = smem + CV_K + (r16 & 1) * 4576 + 16 * (252 + 4 * w - 4 * (r16 >> 1) + g) - 256 * 14;
#pragma unroll
  for (int i = 0; i < 8; ++i) acc[i] = f32x4{0.f, 0.f, 0.f, 0.f};
#pragma unroll
  for (int par = 1; par >= 0; --par) {
    bf16x8 Bf[8];
#pragma unroll
    for (int t = 0; t < 8; ++t) Bf[t] = *(const bf16x8*)(hb + 256 * (2 * t + par));
#pragma unroll
    for (int sg = -par; sg <= 14; sg += 2) {
      const bf16x8 Af = *(const bf16x8*)(kb + 256 * (14 - sg));
#pragma unroll
      for (int i = 0; i < 8; ++i) {
        const int tau = 2 * i - sg;
        if (tau >= 0 && tau <= 15) acc[i] = mfma16(Af, Bf[tau >> 1], acc[i]);
      }
      side((1 - par) * 8 + ((sg + par) >> 1));
    }
  }
}

DI void conv_put(char* smem, int w, int lane, const f32x4 (&acc)[8]) {
  char* pb = smem + w * 8192 + (64 * (lane >> 4) + 2 * (lane & 15)) * 4;
#pragma unroll
  for (int i = 0; i < 8; ++i) {
    *(f32x2*)(pb + i * 1024) = f32x2{acc[i][0], acc[i][1]};
    *(f32x2*)(pb + i * 1024 + 128) = f32x2{acc[i][2], acc[i][3]};
  }
}

DI u32x2 conv_fin(const char* smem, int t, const u32 (&xv)[2], float dsk, float ga, float c1, u32x2& os) {
  f32x4 y = *(const f32x4*)(smem + t * 16);
#pragma unroll
  for (int w = 1; w < 4; ++w) y += *(const f32x4*)(smem + w * 8192 + t * 16);
  const u32x2 hv = *(const u32x2*)(smem + CV_H3 + 48 + t * 8);
  u32x2 o;
#pragma unroll
  for (int k = 0; k < 2; ++k) {
    const u32 yr = cvtpk(y[2 * k], y[2 * k + 1]);
    const u32 hd = cvtpk(lo2f(hv[k]) * dsk, hi2f(hv[k]) * dsk);
    const u32 v = cvtpk(lo2f(yr) + lo2f(hd), hi2f(yr) + hi2f(hd));
    const u32 gv = cvtpk(lo2f(v) * ga, hi2f(v) * ga);
    o[k] = cvtpk(lo2f(xv[k]) + lo2f(gv), hi2f(xv[k]) + hi2f(gv));
    os[k] = cvtpk(lo2f(o[k]) * c1, hi2f(o[k]) * c1);
  }
  return o;
}

template <int NW>
DI void poll_words(const u32* cnt, u32 target, int lane) {
#pragma unroll 1
  for (int spins = 0; spins < (1 << 20); ++spins) {
    u32 v = target;
    if (lane < NW) v = __hip_atomic_load(cnt + lane * 64, __ATOMIC_RELAXED, __HIP_MEMORY_SCOPE_AGENT);
    if (__all(v >= target)) break;
    __builtin_amdgcn_s_sleep(1);
  }
}

#define LD_W2 ld_frag1
constexpr int W1_RING = 10;
#define XB_AUX 0
#define W1B_PRE 5
#define BB_RING 6
#define W2_IN 35
#define W2_POST 0
#define W2_LAG 2
#define W1_RES 20
#define POLL_SLEEP 2
#define GO_WV 0
#define GO_CHK 4
constexpr int W2_MID = 4;

__global__ __launch_bounds__(512, 2) void k_main(const u16* __restrict__ x, const u16* __restrict__ Are, const u16* __restrict__ Aim,
                                                    const u16* __restrict__ Bre, const u16* __restrict__ Bim, const u16* __restrict__ Cre,
                                                    const u16* __restrict__ Cim, const u16* __restrict__ ldt, const u16* __restrict__ Dsk,
                                                    const u16* __restrict__ w1, const u16* __restrict__ w2, const u16* __restrict__ cvec,
                                                    const u16* __restrict__ wm, char* __restrict__ ws, u16* __restrict__ out) {
  __shared__ __attribute__((aligned(256))) char smem[MN_SMEM];
  asm volatile("" ::"s"(x), "s"(Are), "s"(Aim), "s"(Bre), "s"(Bim), "s"(Cre), "s"(Cim), "s"(ldt));
  asm volatile("" ::"s"(Dsk), "s"(w1), "s"(w2), "s"(cvec), "s"(wm), "s"(ws), "s"(out));
  const int tid = threadIdx.x, wv = __builtin_amdgcn_readfirstlane(tid >> 6), lane = tid & 63;
  const int r16 = lane & 15, g = lane >> 4, q = r16 >> 2, p = lane & 3;
  const int b = blockIdx.x, hw = wv >> 2, w4 = wv & 3, t256 = tid & 255;
  const rsrc_t rs = __builtin_amdgcn_make_buffer_rsrc((void*)ws, 0, 0x7fffffff, 0x00020000);
  u32* cnt = (u32*)(ws + WS_CNT);
  const int idx = b >> 3, tt = ((b & 7) >> 1) * 8 + (idx & 7), sl = (b & 1) * 4 + (idx >> 3);
  const int pbase = 7 * sl - (sl >> 2), pcnt = (sl & 3) == 3 ? 6 : 7;
  const bool active = wv < pcnt;
  const int p0 = pbase + (active ? wv : 0);
  TS(1, 0);

  const bool three = idx < 16;
  const int d0 = 80 * (b & 7) + (three ? 3 * idx : 16 + 2 * idx);
  const u32 l16 = lane * 16;
  u32* pcs = (u32*)(smem + MN_PC);
  u32* fl = (u32*)(ws + WS_FL);
  u32 tag;
  bf16x8 af[20][2];
  const u32 w1o = (u32)WS_W1F + (u32)(2 * p0) * 20 * 1024;
  {
    if (tid < 8 && tid != 4) pcs[tid] = 0u;
    u32x4 cw0, cw1;
    if (wv == CODE_WARM) {
      unsigned long long pc; u32 len;
      asm volatile("s_getpc_b64 %0\n.Lcw_here:\n\ts_mov_b32 %1, .Lcw_end-.Lcw_here" : "=s"(pc), "=s"(len));
      const u32 o = idx * 1280 + lane * 16;
      cw0 = *(const __attribute__((address_space(1))) u32x4*)(pc + min(o, len - 16u));
      cw1 = *(const __attribute__((address_space(1))) u32x4*)(pc + min(o + 256u, len - 16u));
    }
    u32x4 sg[2];
    u32x2 mg;
    const int mj = (lane & 15) >> 2, mch = min(lane >> 4, three ? 2 : 1);
    auto poll = [&]() {
      sg[0] = ld_sc1(rs, tid * 32, (u32)WS_NT);
      sg[1] = ld_sc1(rs, tid * 32 + 16, (u32)WS_NT);
      mg = __builtin_amdgcn_raw_buffer_load_b64(rs, ((lane & 3) * (6 * D_) + (mj < 3 ? mj : 4) * D_ + d0 + mch) * 8, (u32)WS_XT, 16);
    };
    const int nkg = three ? 6 : 4;
    if (wv < nkg) {
      kgen_pair(d0 + (wv >> 1), wv & 1, lane, Are, Aim, Bre, Bim, Cre, Cim, ldt, smem + (wv >> 1) * CV_CH, smem + MN_R1 + (wv >> 1) * 8704,
                pcs + (wv >> 1), []() {},
                poll);
      tag = pcs[4];
      TW(1);
    } else {
      __builtin_amdgcn_s_setprio(3);
      auto mid = [&]() { wg_barrier(); tag = pcs[4]; return tag; };
      if (wv == 7) {
        const u32 ep = __hip_atomic_load(fl + 8192, __ATOMIC_RELAXED, __HIP_MEMORY_SCOPE_AGENT);
        ln1_unit(8 * b, lane, x, rs, smem + M2_X, [&]() {
          const unsigned long long h = (llvm_dispatch_id() * 0x9E3779B97F4A7C15ull) ^ ((unsigned long long)__builtin_amdgcn_dispatch_ptr() * 0xC2B2AE3D27D4EB4Full);
          tag = ep + 1u + ((u32)(h >> 33) & 0x3fffffffu);
          if (lane == 0) pcs[4] = tag;
          wg_barrier();
          return tag;
        });
        __builtin_amdgcn_raw_buffer_store_b64(u32x2{0u, 0u}, rs, lane * 8, (u32)WS_ST + (tt * 8 + sl) * 512, 16);
        if (lane == 0) __hip_atomic_store(cnt + (16 + tt * 8 + sl) * 64, 0u, __ATOMIC_RELAXED, __HIP_MEMORY_SCOPE_AGENT);
        if (b < 8 && lane == 0) __hip_atomic_store(cnt + b * 64, 0u, __ATOMIC_RELAXED, __HIP_MEMORY_SCOPE_AGENT);
      } else if (wv == 6 && b < 240) {
        mod_unit(b, lane, cvec, wm, rs, mid);
      } else {
        const int un = wv == 6 ? b + 16 : (b - 128) * 2 + wv - 4;
        w1_tile(un < 270 ? un : 269, lane, w1, rs, smem + 2 * CV_CH + (wv - 4) * 8192, mid);
      }
      TW(1);
      asm volatile("s_waitcnt vmcnt(0)" ::: "memory");
      TW(2);
      u32 o = 0;
      if (lane == 0) o = __hip_atomic_fetch_add(pcs + 3, 1u, __ATOMIC_RELAXED, __HIP_MEMORY_SCOPE_WORKGROUP);
      if (lane == 0 && o == (three ? 1u : 3u)) __hip_atomic_store(fl + b, tag, __ATOMIC_RELAXED, __HIP_MEMORY_SCOPE_AGENT);
      __builtin_amdgcn_s_setprio(0);
      poll();
    }
    if (wv == CODE_WARM) asm volatile("" ::"v"(cw0), "v"(cw1));
#pragma unroll 1
    for (int spins = 0; spins < (1 << 20); ++spins) {
      if (__all(sg[0][1] == tag && sg[0][3] == tag && sg[1][1] == tag && sg[1][3] == tag && mg[1] == tag)) break;
      __builtin_amdgcn_s_sleep(POLL_SLEEP);
      poll();
    }
    TW(3);

    u32 xp[3][2];
#pragma unroll
    for (int c = 0; c < 3; ++c) {
      const u32x2 v = __builtin_amdgcn_raw_buffer_load_b64(rs, (tid >> 1) * (D_ * 16) + (tid & 1) * 8 + (d0 + (c < 2 || three ? c : 0)) * 16, (u32)WS_XB, XB_AUX);
      xp[c][0] = v[0]; xp[c][1] = v[1];
    }
    auto ldf = [&](u32 off) { return __uint_as_float(__builtin_amdgcn_raw_buffer_load_b32(rs, off, (u32)WS_MODP, 16)); };
    u32x4 af0;
    const u16 dskr = Dsk[d0 + mch];
    f32x4 accA[8], accC[8];
    const int t2 = (b - 128) * 3 + wv - 4;
    const bool do2 = !three && wv >= 4 && wv < 7 && t2 < 270;
    if (do2) {
#pragma unroll
      for (int i = 0; i < 8; ++i) accC[i] = *(const f32x4*)(w2 + (size_t)(32 * (t2 / 5) + 4 * i + g) * D_ + 128 * (t2 % 5) + 8 * r16);
    }
    char* smA = smem + hw * CV_CH;
    char* smC = smem + 2 * CV_CH;
    float mp = __uint_as_float(mg[0]);
    mp += __shfl_xor(mp, 1); mp += __shfl_xor(mp, 2);
    mp = rb(mp);
    auto rl = [&](int l) { return __uint_as_float(__builtin_amdgcn_readlane(__float_as_uint(mp), l)); };
    float mn[4], rsd[4];
#pragma unroll
    for (int i = 0; i < 4; ++i) { const u32 s = sg[i >> 1][2 * (i & 1)]; mn[i] = lo2f(s); rsd[i] = __builtin_amdgcn_rcpf(hi2f(s)); }
    conv_stage_h(smem, xp[0], mn, rsd, rb(1.f + rl(4)), rl(0), tid);
    conv_stage_h(smem + CV_CH, xp[1], mn, rsd, rb(1.f + rl(20)), rl(16), tid);
    if (three) conv_stage_h(smC, xp[2], mn, rsd, rb(1.f + rl(36)), rl(32), tid);
    TW(4);
    if (wv == GO_WV) af0 = ld_sc1(rs, l16, (u32)WS_FL);
    const int t1 = (b - 128) * 4 + wv;
    const bool do1 = !three && wv < 4 && t1 < 432;
    u32x4 rv[5];
    f32x4 gbp;
    __syncthreads();
    TS(1, 1);
    conv_mma(smA, w4, lane, accA, [&](int it) {
      if (it == GO_CHK) {
        if (wv == GO_WV) {
#pragma unroll 1
          for (int spins = 0; spins < (1 << 20); ++spins) {
            if (__all(af0[0] == tag && af0[1] == tag && af0[2] == tag && af0[3] == tag)) break;
            __builtin_amdgcn_s_sleep(1);
            af0 = ld_sc1(rs, l16, (u32)WS_FL);
          }
          if (lane == 0) __hip_atomic_store(pcs + 5, 1u, __ATOMIC_RELAXED, __HIP_MEMORY_SCOPE_WORKGROUP);
          if (b == 0 && lane == 0) __hip_atomic_store(fl + 8192, tag, __ATOMIC_RELAXED, __HIP_MEMORY_SCOPE_AGENT);
        }
      }
      if (it == GO_IT) {
        if (wv != GO_WV) {
#pragma unroll 1
          for (int spins = 0; spins < (1 << 22); ++spins) {
            if (__hip_atomic_load(pcs + 5, __ATOMIC_RELAXED, __HIP_MEMORY_SCOPE_WORKGROUP) != 0u) break;
            __builtin_amdgcn_s_sleep(1);
          }
        }
        asm volatile("" ::: "memory");
        TW(5);
        if (do1) {
#pragma unroll
          for (int i = 0; i < 5; ++i) rv[i] = ld_sc1(rs, l16, (u32)WS_W1F + (t1 * 5 + i) * 1024);
          const u32 mq = (160 * (t1 & 3) + 4 * (lane < 40 ? lane : 0)) * 4;
#pragma unroll
          for (int k = 0; k < KSM; ++k) {
            accC[k] = __builtin_bit_cast(f32x4, ld_sc1(rs, mq, (u32)WS_MODP + (k * (6 * D_) + 4 * D_) * 4));
            accC[4 + k] = __builtin_bit_cast(f32x4, ld_sc1(rs, mq, (u32)WS_MODP + (k * (6 * D_) + 3 * D_) * 4));
          }
        }
#pragma unroll
        for (int k = 0; k < KSM; ++k) gbp[k] = ldf((k * (6 * D_) + 5 * D_ + sl * 80 + (tid < 80 ? tid : 0)) * 4);
      }
      if (it > GO_IT && it <= GO_IT + W1_RING) {
        const int ks = it - GO_IT - 1;
#pragma unroll
        for (int fb = 0; fb < 2; ++fb) af[ks][fb] = ld_frag1(rs, l16, w1o + (fb * 20 + ks) * 1024);
      }
    });
    if (three && hw == 0) conv_mma(smC, w4, lane, accC, [](int) {});
    if (tid < 80) ((float*)(smem + MN_GB))[tid] = rb((gbp[0] + gbp[1]) + (gbp[2] + gbp[3]));
    if (do2) {
      char* buf = smC + 4096 + (wv - 4) * 8192;
#pragma unroll
      for (int i = 0; i < 8; ++i) {
        const int row = 4 * i + g;
        *(f32x4*)(buf + row * 256 + (((r16 >> 1) ^ rl_swz(row)) << 5) + ((r16 & 1) << 4)) = accC[i];
      }
      wave_sync();
      const int row0 = 4 * g + q, row1 = row0 + 16;
#pragma unroll
      for (int cb = 0; cb < 8; ++cb) {
        const s16x4 lo = tr_read(buf + row0 * 256 + ((cb ^ rl_swz(row0)) << 5) + 8 * p);
        const s16x4 hi = tr_read(buf + row1 * 256 + ((cb ^ rl_swz(row1)) << 5) + 8 * p);
        st_sc1(rs, l16, (u32)WS_W2F + ((8 * (t2 % 5) + cb) * 54 + t2 / 5) * 1024, __builtin_bit_cast(u32x4, cat8(lo, hi)));
      }
    }
    if (do1) {
      char* buf = smC + wv * 1024;
      const f32x4 mc = (accC[0] + accC[1]) + (accC[2] + accC[3]), ms = (accC[4] + accC[5]) + (accC[6] + accC[7]);
      if (lane < 40) {
        *(u32x2*)(buf + lane * 8) = u32x2{cvtpk(1.f + rb(mc[0]), 1.f + rb(mc[1])), cvtpk(1.f + rb(mc[2]), 1.f + rb(mc[3]))};
        *(u32x2*)(buf + 512 + lane * 8) = u32x2{cvtpk(ms[0], ms[1]), cvtpk(ms[2], ms[3])};
      }
      wave_sync();
      float S = 0.f, B = 0.f;
#pragma unroll
      for (int i = 0; i < 5; ++i) {
        const u32x4 c1p = *(const u32x4*)(buf + i * 64 + g * 16), sbp = *(const u32x4*)(buf + 512 + i * 64 + g * 16);
#pragma unroll
        for (int k = 0; k < 4; ++k) { S = dot2bf(rv[i][k], c1p[k], S); B = dot2bf(rv[i][k], sbp[k], B); }
      }
      S += __shfl_xor(S, 16); S += __shfl_xor(S, 32);
      B += __shfl_xor(B, 16); B += __shfl_xor(B, 32);
      if (lane < 32)
        __builtin_amdgcn_raw_buffer_store_b32(__float_as_uint(lane < 16 ? S : B), rs, (g * 4 * F_ + r16) * 4, (u32)WS_SB + ((t1 & 3) * F_ + 16 * (t1 >> 2)) * 4, 16);
    }
    __syncthreads();
    TS(1, 2);
    conv_put(smA, w4, lane, accA);
    if (three && hw == 0) conv_put(smC, w4, lane, accC);
    __syncthreads();
    const float dsk = bf2f(dskr);
#pragma unroll
    for (int c = 0; c < 3; ++c) {
      if (c < 2 || three) {
        u32x2 os;
        const u32x2 o = conv_fin(smem + c * CV_CH, tid, xp[c], __uint_as_float(__builtin_amdgcn_readlane(__float_as_uint(dsk), 16 * c)), rl(16 * c + 8),
                                 rb(1.f + rl(16 * c + 12)), os);
        __builtin_amdgcn_raw_buffer_store_b64(o, rs, tid * 8 + (u32)WS_X1T + (d0 + c) * (L_ * 2), 0, 16);
        __builtin_amdgcn_raw_buffer_store_b64(os, rs, tid * 8 + (u32)WS_X1S + (d0 + c) * (L_ * 2), 0, 16);
      }
    }
  }
  TS(1, 3);

  const int k0 = 7 * wv - (wv >> 2), nk = (wv & 3) == 3 ? 6 : 7;
  const u32 w2o = (u32)WS_W2F + (u32)((sl * 5) * 54 + k0) * 1024;
  bf16x8 wf[7][5];
  auto ldw2 = [&](int f) { wf[f / 5][f % 5] = LD_W2(rs, l16, w2o + ((f % 5) * 54 + (f < 30 || nk == 7 ? f / 5 : 5)) * 1024); };
  {
    const int l8 = lane & 7, crow = 80 * wv + (lane >> 3);
    u32x4 xv[10];
    auto tile_ld = [&]() {
#pragma unroll
      for (int i = 0; i < 10; ++i) xv[i] = ld_sc1(rs, crow * (L_ * 2) + l8 * 16, (u32)WS_X1S + i * (8 * L_ * 2) + tt * 128);
    };
    auto tile_st = [&]() {
#pragma unroll
      for (int i = 0; i < 10; ++i) {
        const int c = crow + 8 * i;
        *(u32x4*)(smem + M1_A + c * 128 + (((l8 >> 1) ^ h2_swz(c)) << 5) + ((l8 & 1) << 4)) = xv[i];
      }
      wg_barrier();
    };
    f32x4 acc[2][4];
    auto zero_acc = [&]() {
#pragma unroll
      for (int fb = 0; fb < 2; ++fb)
#pragma unroll
        for (int tb = 0; tb < 4; ++tb) acc[fb][tb] = f32x4{0.f, 0.f, 0.f, 0.f};
    };
    float* st = (float*)(smem + M1_ST);
    if (wv < 7) {
      asm volatile("s_waitcnt vmcnt(0)" ::: "memory");
      wg_barrier();
      __builtin_amdgcn_sched_barrier(0);
#pragma unroll
      for (int ks = W1_RING; ks < W1_RING + W1B_PRE; ++ks)
#pragma unroll
        for (int fb = 0; fb < 2; ++fb) af[ks][fb] = ld_frag1(rs, l16, w1o + (fb * 20 + ks) * 1024);
      __builtin_amdgcn_sched_barrier(0);
      wg_barrier();
      TS(1, 4);
      tile_ld();
      __builtin_amdgcn_sched_barrier(0);
#pragma unroll
      for (int ks = W1_RING + W1B_PRE; ks < W1_RES; ++ks)
#pragma unroll
        for (int fb = 0; fb < 2; ++fb) af[ks][fb] = ld_frag1(rs, l16, w1o + (fb * 20 + ks) * 1024);
      __builtin_amdgcn_sched_barrier(0);
      tile_st();
      TS(1, 5);
      TC(3, 0);
      const int row0 = 8 * g + q, sw = h2_swz(row0);
      const char* rb0 = smem + M1_A + row0 * 128 + 8 * p;
      auto ldb = [&](int s) {
        const s16x4 lo = tr_read(rb0 + (s >> 2) * 4096 + (((s & 3) ^ sw) << 5));
        const s16x4 hi = tr_read(rb0 + (s >> 2) * 4096 + 512 + (((s & 3) ^ sw) << 5));
        return cat8(lo, hi);
      };
      f32x4 sb4;
      bf16x8 bb[BB_RING];
#pragma unroll
      for (int s = 0; s < BB_RING; ++s) bb[s] = ldb(s);
      zero_acc();
#pragma unroll
      for (int ks = 0; ks < 20; ++ks) {
#pragma unroll
        for (int tb = 0; tb < 4; ++tb) {
          const int s = 4 * ks + tb;
#pragma unroll
          for (int fb = 0; fb < 2; ++fb) acc[fb][tb] = mfma16(af[ks][fb], bb[s % BB_RING], acc[fb][tb]);
          if (s + BB_RING < 80) bb[s % BB_RING] = ldb(s + BB_RING);
          __builtin_amdgcn_sched_barrier(0);
        }
#pragma unroll
        for (int f = (ks * W2_IN) / 20; f < ((ks + 1) * W2_IN) / 20; ++f) ldw2(f);
        if (ks == 2) {
#pragma unroll
          for (int e = 0; e < 4; ++e)
            sb4[e] = __uint_as_float(__builtin_amdgcn_raw_buffer_load_b32(rs, ((lane >> 5) * 4 * F_ + (lane & 31)) * 4, (u32)WS_SB + (e * F_ + 32 * p0) * 4, 16));
        }
        if (ks == 17) {
          *(float*)(smem + MN_SB + wv * 256 + lane * 4) = (sb4[0] + sb4[1]) + (sb4[2] + sb4[3]);
        }
        __builtin_amdgcn_sched_barrier(0);
      }
    } else {
      __builtin_amdgcn_s_setprio(3);
      asm volatile("s_waitcnt vmcnt(0)" ::: "memory");
      wg_barrier();
      u32 o = 0;
      if (lane == 0) o = __hip_atomic_fetch_add(cnt + (b & 7) * 64, 1u, __ATOMIC_RELAXED, __HIP_MEMORY_SCOPE_AGENT);
      o = __builtin_amdgcn_readfirstlane(o);
      if (o == 31u && lane < 8) __hip_atomic_store(cnt + (8 + lane) * 64 + (b & 7), tag, __ATOMIC_RELAXED, __HIP_MEMORY_SCOPE_AGENT);
      for (int spins = 0; spins < (1 << 20); ++spins) {
        u32 v = tag;
        if (lane < 8) v = __hip_atomic_load(cnt + (8 + (b & 7)) * 64 + lane, __ATOMIC_RELAXED, __HIP_MEMORY_SCOPE_AGENT);
        if (__all(v == tag)) break;
        __builtin_amdgcn_s_sleep(1);
      }
      wg_barrier();
      tile_ld();
      u32x4 xr[10];
#pragma unroll
      for (int i = 0; i < 10; ++i) xr[i] = ld_sc1(rs, (lane >> 3) * (L_ * 2) + (lane & 7) * 16, (u32)WS_X1T + (sl * 80 + 8 * i) * (L_ * 2) + tt * 128);
      __builtin_amdgcn_sched_barrier(0);
      tile_st();
      TC(3, 0);
      zero_acc();
#pragma unroll
      for (int i = 0; i < 10; ++i) {
        const u32x4 v = xr[i];
        const f32x4 a0 = {lo2f(v[0]), hi2f(v[0]), lo2f(v[1]), hi2f(v[1])}, a1 = {lo2f(v[2]), hi2f(v[2]), lo2f(v[3]), hi2f(v[3])};
        acc[0][0] += a0; acc[0][1] += a1; acc[0][2] += a0 * a0; acc[0][3] += a1 * a1;
      }
      const bool b5 = lane & 32, b4 = lane & 16, b3 = lane & 8;
      float v4[2][4], v2[2][2], v1[2];
#pragma unroll
      for (int h = 0; h < 2; ++h) {
#pragma unroll
        for (int j = 0; j < 4; ++j) {
          const float lo = acc[0][2 * h][j], hi = acc[0][2 * h + 1][j];
          v4[h][j] = (b5 ? hi : lo) + __shfl_xor(b5 ? lo : hi, 32);
        }
#pragma unroll
        for (int j = 0; j < 2; ++j) v2[h][j] = (b4 ? v4[h][2 + j] : v4[h][j]) + __shfl_xor(b4 ? v4[h][j] : v4[h][2 + j], 16);
        v1[h] = (b3 ? v2[h][1] : v2[h][0]) + __shfl_xor(b3 ? v2[h][0] : v2[h][1], 8);
      }
      const u32 toff = (8 * (lane & 7) + (lane >> 3)) * 8;
      __builtin_amdgcn_raw_buffer_store_b64(u32x2{__float_as_uint(v1[0]), __float_as_uint(v1[1]) | 0x80000000u}, rs, toff,
                                            (u32)WS_ST + (tt * 8 + sl) * 512, 16);
#pragma unroll
      for (int i = 0; i < 10; ++i) {
        const int c = 8 * i + (lane >> 3);
        *(u32x4*)(smem + M2_X + c * 128 + ((((lane & 7) >> 1) ^ ((c >> 1) & 3)) << 5) + ((lane & 1) << 4)) = xr[i];
      }
      u32x2 gv[8];
#pragma unroll 1
      for (int spins = 0; spins < (1 << 20); ++spins) {
        u32 ok = 0x80000000u;
#pragma unroll
        for (int j = 0; j < 8; ++j) { gv[j] = __builtin_amdgcn_raw_buffer_load_b64(rs, lane * 8, (u32)WS_ST + (tt * 8 + j) * 512, 16); ok &= gv[j][1]; }
        if (__all(ok != 0u)) break;
        __builtin_amdgcn_s_sleep(2);
      }
      float sm = 0.f, sq = 0.f;
#pragma unroll
      for (int j = 0; j < 8; ++j) { sm += __uint_as_float(gv[j][0]); sq += __uint_as_float(gv[j][1] & 0x7fffffffu); }
      const float mean = sm * (1.f / D_);
      const float var = fmaxf(sq * (1.f / D_) - mean * mean, 0.f);
      st[lane] = rb(mean);
      st[64 + lane] = 1.f / rb(sqrtf(rb(rb(var) + 1e-5f)));
      __builtin_amdgcn_s_setprio(0);
    }
    asm volatile("" ::: "memory");
    if (wv == 7) {
#pragma unroll
      for (int f = 0; f < W2_IN; ++f) ldw2(f);
    }
    TS(2, 1);
    TC(3, 1);
    __builtin_amdgcn_sched_barrier(0);
    __syncthreads();
    TS(2, 2);
    if (active) {
      const float* sbp = (const float*)(smem + MN_SB) + wv * 64 + 4 * g;
      const u32 hoff = (u32)WS_HID + (u32)((tt * 4) * 54 + p0) * 1024;
#pragma unroll
      for (int tb = 0; tb < 4; ++tb) {
        const float mb = st[tb * 16 + r16], rsd = st[64 + tb * 16 + r16];
        u32x4 v;
#pragma unroll
        for (int hh = 0; hh < 2; ++hh) {
          const f32x4 sv = *(const f32x4*)(sbp + 16 * hh), bv = *(const f32x4*)(sbp + 32 + 16 * hh);
          const f32x4 a = (acc[hh][tb] - mb * sv) * rsd + bv;
          v[2 * hh] = cvtpk(fmaxf(a[0], 0.f), fmaxf(a[1], 0.f));
          v[2 * hh + 1] = cvtpk(fmaxf(a[2], 0.f), fmaxf(a[3], 0.f));
        }
        st_sc1(rs, l16, hoff + tb * 54 * 1024, v);
      }
    }
  }
  TS(2, 3);
  asm volatile("s_waitcnt vmcnt(0)" ::: "memory");
  __syncthreads();
  TS(2, 4);
  if (tid == 448) __hip_atomic_store(cnt + (16 + tt * 8 + sl) * 64, 1u, __ATOMIC_RELAXED, __HIP_MEMORY_SCOPE_AGENT);
  __builtin_amdgcn_sched_barrier(0);
#pragma unroll
  for (int f = W2_IN; f < W2_IN + W2_POST; ++f) ldw2(f);
  __builtin_amdgcn_sched_barrier(0);
  poll_words<1>(cnt + (16 + tt * 8 + wv) * 64, 1u, lane);
  TS(1, 6);

  {
    const u32 hoff = (u32)WS_HID + (u32)((tt * 4) * 54 + k0) * 1024;
    u32x4 hf[2][4];
#pragma unroll
    for (int i = 0; i < 2; ++i)
#pragma unroll
      for (int tb = 0; tb < 4; ++tb) hf[i][tb] = ld_sc1(rs, l16, hoff + (tb * 54 + i) * 1024);
    __builtin_amdgcn_sched_barrier(0);
#pragma unroll
    for (int f = W2_IN + W2_POST; f < 35; ++f) ldw2(f);
    __builtin_amdgcn_sched_barrier(0);
    f32x4 acc[5][4];
#pragma unroll
    for (int cb = 0; cb < 5; ++cb)
#pragma unroll
      for (int tb = 0; tb < 4; ++tb) acc[cb][tb] = f32x4{0.f, 0.f, 0.f, 0.f};
#pragma unroll
    for (int i = 0; i < 7; ++i) {
      if (i < 6 || nk == 7) {
#pragma unroll
        for (int tb = 0; tb < 4; ++tb)
#pragma unroll
          for (int cb = 0; cb < 5; ++cb) acc[cb][tb] = mfma16(wf[i][cb], __builtin_bit_cast(bf16x8, hf[i & 1][tb]), acc[cb][tb]);
      }
      __builtin_amdgcn_sched_barrier(0);
      if (i + 2 < 7) {
        const int kk = (i + 2 < 6 || nk == 7) ? i + 2 : 5;
#pragma unroll
        for (int tb = 0; tb < 4; ++tb) hf[i & 1][tb] = ld_sc1(rs, l16, hoff + (tb * 54 + kk) * 1024);
        __builtin_amdgcn_sched_barrier(0);
      }
    }
    TS(2, 5);
#pragma unroll
    for (int tb = 0; tb < 4; ++tb)
#pragma unroll
      for (int cb = 0; cb < 5; ++cb) {
        const int owner = tb + (cb < 3 ? 0 : 4);
        if (owner != wv) {
          const int slot = wv < owner ? wv : wv - 1;
          *(f32x4*)(smem + M2_RED + (((tb * 5 + cb) * 7 + slot) * 64 + lane) * 16) = acc[cb][tb];
        }
      }
    f32x4 own[3];
#pragma unroll
    for (int o = 0; o < 8; ++o) {
      if (wv == o) {
        asm volatile("" ::: "memory");
#pragma unroll
        for (int j = 0; j < 3; ++j) own[j] = acc[o < 4 ? j : (j < 2 ? 3 + j : 4)][o & 3];
      }
    }
    __syncthreads();
    TS(2, 6);
    const int tok = 16 * w4 + r16, cb0 = wv < 4 ? 0 : 3;
    f32x4 pr[3][7], gb4[3];
    u32x2 xr[3];
#pragma unroll
    for (int j = 0; j < 3; ++j) {
      const int cb = (j < 2 || wv < 4) ? cb0 + j : 4;
#pragma unroll
      for (int s7 = 0; s7 < 7; ++s7) pr[j][s7] = *(const f32x4*)(smem + M2_RED + (((w4 * 5 + cb) * 7 + s7) * 64 + lane) * 16);
      gb4[j] = *(const f32x4*)(smem + MN_GB + (16 * cb + 4 * g) * 4);
      xr[j] = __builtin_bit_cast(u32x2, tr_read(smem + M2_X + (16 * cb + 4 * g + q) * 128 + ((w4 ^ ((2 * g + (q >> 1)) & 3)) << 5) + 8 * p));
    }
    __builtin_amdgcn_sched_barrier(0);
#pragma unroll
    for (int j = 0; j < 3; ++j) {
      const f32x4 y = ((own[j] + pr[j][0]) + (pr[j][1] + pr[j][2])) + ((pr[j][3] + pr[j][4]) + (pr[j][5] + pr[j][6]));
      const u32 y0 = cvtpk(y[0], y[1]), y1 = cvtpk(y[2], y[3]);
      const u32 v0 = cvtpk(lo2f(y0) * gb4[j][0], hi2f(y0) * gb4[j][1]), v1 = cvtpk(lo2f(y1) * gb4[j][2], hi2f(y1) * gb4[j][3]);
      u32x2 o;
      o[0] = cvtpk(lo2f(xr[j][0]) + lo2f(v0), hi2f(xr[j][0]) + hi2f(v0));
      o[1] = cvtpk(lo2f(xr[j][1]) + lo2f(v1), hi2f(xr[j][1]) + hi2f(v1));
      if (j < 2 || wv < 4) *(u32x2*)(out + (size_t)(tt * 64 + tok) * D_ + sl * 80 + 16 * (cb0 + j) + 4 * g) = o;
    }
  }
  asm volatile(".Lcw_end:");
  TS(1, 7);
}

extern "C" void kernel_launch(void* const* d_in, const int* in_sizes, int n_in, void* d_out, int out_size, void* d_ws,
                              size_t ws_size, hipStream_t stream) {
  char* ws = (char*)d_ws;
  k_main<<<256, 512, 0, stream>>>(
      (const u16*)d_in[0], (const u16*)d_in[1], (const u16*)d_in[2], (const u16*)d_in[3], (const u16*)d_in[4],
      (const u16*)d_in[5], (const u16*)d_in[6], (const u16*)d_in[7], (const u16*)d_in[8], (const u16*)d_in[10],
      (const u16*)d_in[11], (const u16*)d_in[12], (const u16*)d_in[13], ws, (u16*)d_out);
}
```

```cpp
#include <hip/hip_runtime.h>
#include <hip/hip_bf16.h>
#include <stdint.h>

extern "C" __device__ unsigned long long llvm_dispatch_id() __asm("llvm.amdgcn.dispatch.id");
typedef unsigned short u16;
typedef unsigned int u32;
typedef __attribute__((ext_vector_type(8))) short bf16x8;
typedef __attribute__((ext_vector_type(4))) short s16x4;
typedef __attribute__((ext_vector_type(4))) float f32x4;
typedef __attribute__((ext_vector_type(2))) float f32x2;
typedef __attribute__((ext_vector_type(4))) unsigned int u32x4;
typedef __attribute__((ext_vector_type(2))) unsigned int u32x2;
typedef __bf16 bf16x2_t __attribute__((ext_vector_type(2)));
#define LDS_AS __attribute__((address_space(3)))
#define DI __device__ __forceinline__

constexpr int L_ = 2048, D_ = 640, N_ = 128, F_ = 1728;
constexpr int KSM = 4;

constexpr size_t WS_MODP = 0;
constexpr size_t WS_CNT = 126976;
constexpr size_t WS_FL = 196608;
constexpr size_t WS_NT = 262144;
constexpr size_t WS_XT = 327680;
constexpr size_t WS_SB = 458752;
constexpr size_t WS_ST = 524288;
constexpr size_t WS_XB = 1572864;
constexpr size_t WS_X1S = WS_XB + (size_t)D_ * L_ * 2;
constexpr size_t WS_X1T = WS_X1S + (size_t)D_ * L_ * 2;
constexpr size_t WS_W1F = WS_X1T + (size_t)D_ * L_ * 2;
constexpr size_t WS_W2F = WS_W1F + (size_t)108 * 20 * 1024;
constexpr size_t WS_HID = WS_W2F + (size_t)40 * 54 * 1024;
constexpr int KTAB = 1296;


DI u32 cvtpk(float lo, float hi) {
  f32x2 v = {lo, hi};
  bf16x2_t r = __builtin_convertvector(v, bf16x2_t);
  return __builtin_bit_cast(u32, r);
}
typedef _Float16 f16x2_t __attribute__((ext_vector_type(2)));
typedef _Float16 f16x8_t __attribute__((ext_vector_type(8)));
DI u32 cvtph(float lo, float hi) {
  f32x2 v = {lo, hi};
  f16x2_t r = __builtin_convertvector(v, f16x2_t);
  return __builtin_bit_cast(u32, r);
}
DI float dot2bf(u32 a, u32 b, float c) {
  return __builtin_amdgcn_fdot2_f32_bf16(__builtin_bit_cast(bf16x2_t, a), __builtin_bit_cast(bf16x2_t, b), c, false);
}
DI float lo2f(u32 v) { return __uint_as_float(v << 16); }
DI float hi2f(u32 v) { return __uint_as_float(v & 0xffff0000u); }
DI float bf2f(u16 v) { return __uint_as_float(((u32)v) << 16); }
DI float rb(float x) { return lo2f(cvtpk(x, x)); }
DI f32x4 mfma16(bf16x8 a, bf16x8 b, f32x4 c) { return __builtin_amdgcn_mfma_f32_16x16x32_bf16(a, b, c, 0, 0, 0); }
DI s16x4 tr_read(const char* p) {
  return __builtin_amdgcn_ds_read_tr16_b64_v4i16((LDS_AS s16x4*)p);
}
DI bf16x8 cat8(s16x4 a, s16x4 b) { return __builtin_shufflevector(a, b, 0, 1, 2, 3, 4, 5, 6, 7); }
DI void wave_sync() {
  __builtin_amdgcn_fence(__ATOMIC_RELEASE, "wavefront");
  __builtin_amdgcn_wave_barrier();
  __builtin_amdgcn_fence(__ATOMIC_ACQUIRE, "wavefront");
}

DI f32x2 cmul2(f32x2 a, f32x2 b) {
  const f32x2 bj = {-b.y, b.x};
  return __builtin_elementwise_fma(a.yy, bj, a.xx * b);
}
struct c32 { float r, i; };
DI c32 cmul(c32 a, c32 b) { return {a.r * b.r - a.i * b.i, a.r * b.i + a.i * b.r}; }
DI c32 cexp_scaled(float ar, float ai, float e) {
  const float mag = __expf(ar * e);
  const float x = ai * e;
  const float C_HI = 0.15915494f, C_LO = 6.4206382e-9f;
  const float r = x * C_HI;
  const float rl = fmaf(x, C_HI, -r) + x * C_LO;
  const float fr = (r - floorf(r)) + rl;
  return {mag * __builtin_amdgcn_cosf(fr), mag * __builtin_amdgcn_sinf(fr)};
}
DI int rl_swz(int row) { return (row & 3) | ((((row >> 2) ^ (row >> 3)) & 1) << 2); }

constexpr int CV_H0 = 0, CV_HS = 4160, CV_K = 12544, CV_T = 21760, CV_H3 = 32768 + 192, CV_CH = 37376;
static_assert(CV_CH % 256 == 0 && CV_H3 + 4160 <= CV_CH && CV_T + 4 * KTAB <= 32768, "conv lds");
constexpr int M1_A = 0, M1_ST = 81920;
constexpr int MN_SB = 3 * CV_CH;
constexpr int MN_R1 = MN_SB + 2048;
constexpr int M2_RED = 0, M2_X = 143360;
constexpr int MN_GB = M2_X + 11264;
constexpr int MN_PC = MN_GB + 320;
constexpr int MN_SMEM = MN_PC + 32;
static_assert(MN_R1 + 32768 <= MN_GB && MN_R1 + 6 * 4352 <= M2_X && M1_ST + 512 <= MN_SB, "mlp lds");

typedef __amdgpu_buffer_rsrc_t rsrc_t;
DI u32x4 ld_sc1(rsrc_t r, u32 voff, u32 soff) { return __builtin_amdgcn_raw_buffer_load_b128(r, voff, soff, 16); }
DI void st_sc1(rsrc_t r, u32 voff, u32 soff, u32x4 v) { __builtin_amdgcn_raw_buffer_store_b128(v, r, voff + soff, 0, 16); }
DI bf16x8 ld_frag1(rsrc_t r, u32 voff, u32 soff) { return __builtin_bit_cast(bf16x8, __builtin_amdgcn_raw_buffer_load_b128(r, voff, soff, 16)); }

DI int h2_swz(int k) { return ((k >> 1) & 1) | (((k >> 3) & 1) << 1); }

DI void wg_barrier() {
  __builtin_amdgcn_fence(__ATOMIC_RELEASE, "workgroup");
  __builtin_amdgcn_s_barrier();
  __builtin_amdgcn_fence(__ATOMIC_ACQUIRE, "workgroup");
}
DI void pair_sync(u32* c, u32 target) {
  __builtin_amdgcn_fence(__ATOMIC_RELEASE, "workgroup");
  if ((threadIdx.x & 63) == 0) __hip_atomic_fetch_add(c, 1u, __ATOMIC_RELAXED, __HIP_MEMORY_SCOPE_WORKGROUP);
#pragma unroll 1
  for (int spins = 0; spins < (1 << 22); ++spins) {
    if (__hip_atomic_load(c, __ATOMIC_RELAXED, __HIP_MEMORY_SCOPE_WORKGROUP) >= target) break;
    __builtin_amdgcn_s_sleep(1);
  }
  __builtin_amdgcn_fence(__ATOMIC_ACQUIRE, "workgroup");
}

template <class F0, class F1>
DI void kgen_pair(int d, int hi, int lane, const u16* __restrict__ Are, const u16* __restrict__ Aim, const u16* __restrict__ Bre,
                  const u16* __restrict__ Bim, const u16* __restrict__ Cre, const u16* __restrict__ Cim,
                  const u16* __restrict__ ldt, char* smem, char* scr, u32* pc, F0&& early, F1&& late) {
  const int r16 = lane & 15, g = lane >> 4, n = hi * 64 + lane;
  const int gi = d * N_ + n;
  const u16 r0 = ldt[d], r1 = Are[gi], r2 = Aim[gi], r3 = Bre[gi], r4 = Bim[gi], r5 = Cre[gi], r6 = Cim[gi];
  __builtin_amdgcn_sched_barrier(0);
  wg_barrier();
  const float dt = rb(expf(bf2f(r0)));
  const float ar = dt * bf2f(r1), ai = dt * bf2f(r2);
  const c32 dB = {dt * bf2f(r3), dt * bf2f(r4)};
  const c32 Cc = {bf2f(r5), bf2f(r6)};
  __builtin_amdgcn_sched_barrier(0);
  early();
  __builtin_amdgcn_sched_barrier(0);
  const c32 W = cmul(Cc, dB);
  const c32 E1 = cexp_scaled(ar, ai, 1.f), E8 = cexp_scaled(ar, ai, 8.f), E128 = cexp_scaled(ar, ai, 128.f);

  int ew = (int)((__float_as_uint(fmaxf(fabsf(W.r), fabsf(W.i))) >> 23) & 0xff);
  ew = max(ew, __builtin_amdgcn_update_dpp(0, ew, 0xB1, 0xf, 0xf, true));
  ew = max(ew, __builtin_amdgcn_update_dpp(0, ew, 0x4E, 0xf, 0xf, true));
  ew = max(ew, __builtin_amdgcn_update_dpp(0, ew, 0x141, 0xf, 0xf, true));
  ew = max(ew, __builtin_amdgcn_update_dpp(0, ew, 0x140, 0xf, 0xf, true));
  ew = max(max(__builtin_amdgcn_readlane(ew, 0), __builtin_amdgcn_readlane(ew, 16)), max(__builtin_amdgcn_readlane(ew, 32), __builtin_amdgcn_readlane(ew, 48)));
  ew = ew < 20 ? 20 : (ew > 240 ? 240 : ew);
  const float wsc = __uint_as_float((u32)(127 + 10 + 127 - ew - 1) << 23);
  const float unsc = __uint_as_float((u32)(ew + 1 - 20) << 23);

  char* buf = scr + hi * 4352;
  char* wr = buf + lane * 4;
  const char* rd = buf + r16 * 272 + g * 16;
  auto put = [&](int row, f32x2 v) { *(u32*)(wr + row * 272) = cvtph(v.x, v.y); };
  {
    const f32x2 e8 = {E8.r, E8.i};
    f32x2 p = {1024.f, 0.f};
#pragma unroll
    for (int jj = 0; jj < 16; ++jj) { put(jj, p); if (jj < 15) p = cmul2(p, e8); }
  }
  wave_sync();
  f16x8_t bh[4];
#pragma unroll
  for (int ks = 0; ks < 4; ++ks) bh[ks] = *(const f16x8_t*)(rd + ks * 64);
  wave_sync();
  const f32x2 e1 = {E1.r, -E1.i}, e128 = {E128.r, -E128.i};
  f32x2 sm[8];
  sm[1] = e1;
#pragma unroll
  for (int i = 2; i < 8; ++i) sm[i] = cmul2(sm[i - 1], e1);
  f32x2 WG[17];
  WG[0] = f32x2{W.r * wsc, -W.i * wsc};
#pragma unroll
  for (int i = 1; i < 17; ++i) WG[i] = cmul2(WG[i - 1], e128);
  f32x4 acc[3];
  auto tile_mma = [&](int t) {
    wave_sync();
    f32x4 a = {0.f, 0.f, 0.f, 0.f};
#pragma unroll
    for (int ks = 0; ks < 4; ++ks) a = __builtin_amdgcn_mfma_f32_16x16x32_f16(*(const f16x8_t*)(rd + ks * 64), bh[ks], a, 0, 0, 0);
    acc[t] = a * unsc;
    wave_sync();
  };
#pragma unroll
  for (int i = 0; i < 2; ++i)
#pragma unroll
    for (int rho = 0; rho < 8; ++rho) put(8 * i + rho, rho ? cmul2(sm[rho], WG[i]) : WG[i]);
  tile_mma(0);
#pragma unroll
  for (int i = 0; i < 2; ++i)
#pragma unroll
    for (int rho = 0; rho < 4; ++rho) put(4 * i + rho, rho ? cmul2(sm[2 * rho], WG[2 + i]) : WG[2 + i]);
#pragma unroll
  for (int i = 0; i < 4; ++i)
#pragma unroll
    for (int rho = 0; rho < 2; ++rho) put(8 + 2 * i + rho, rho ? cmul2(sm[4], WG[4 + i]) : WG[4 + i]);
  tile_mma(1);
#pragma unroll
  for (int i = 0; i < 9; ++i) put(i, WG[8 + i]);
  tile_mma(2);

  if (lane < 28) *(u32x4*)(smem + CV_K + hi * 4576 + (256 + lane) * 16) = u32x4{0u, 0u, 0u, 0u};
#pragma unroll
  for (int t = 0; t < 3; ++t) *(f32x4*)(buf + t * 1024 + lane * 16) = acc[t];
  pair_sync(pc, 2u);
  u16* T = (u16*)(smem + CV_T);
  {
    const char* ob = scr + (hi ^ 1) * 4352 + lane * 16;
    if (!hi) {
      const f32x4 a0 = acc[0] + *(const f32x4*)(ob), a2 = acc[2] + *(const f32x4*)(ob + 2048);
      u16* t0 = T + 128 * (g >> 1) + 4 * (g & 1) + 8 * r16;
      *(u32x2*)t0 = u32x2{cvtpk(a0[0], a0[1]), cvtpk(a0[2], a0[3])};
      if (g < 2) {
#pragma unroll
        for (int rg = 0; rg < 4; ++rg) T[512 + 16 * (4 * g + rg) + r16] = (u16)cvtpk(a2[rg], 0.f);
      } else if (lane == 32) {
        T[640] = (u16)cvtpk(a2[0], 0.f);
      } else if (lane >= 48 && lane < 55) {
        T[641 + lane - 48] = 0;
      }
    } else {
      const f32x4 a1 = acc[1] + *(const f32x4*)(ob + 1024);
      if (g < 2) {
        *(u32x2*)(T + 256 + 64 * g + 4 * r16) = u32x2{cvtpk(a1[0], a1[1]), cvtpk(a1[2], a1[3])};
      } else {
        *(u32*)(T + 384 + 64 * (g - 2) + 2 * r16) = cvtpk(a1[0], a1[1]);
        *(u32*)(T + 384 + 64 * (g - 2) + 32 + 2 * r16) = cvtpk(a1[2], a1[3]);
      }
    }
  }
  late();
  pair_sync(pc, 4u);
  char* k0 = smem + CV_K + n * 16;
  auto pk = [](u32 lo, u32 hi16) { return lo | (hi16 << 16); };
  {
    const u32 b = T[639 - n], a = T[640 - n], c = (n & 1) ? b : a;
    const u32 aa = pk(a, a), bb = pk(b, b);
    *(u32x4*)(k0 + 4576) = u32x4{aa, pk(a, c), bb, bb};
    *(u32x4*)(k0) = u32x4{aa, pk(c, b), bb, bb};
  }
  if (!hi) {
    const u32 c = T[510 - 2 * lane], b = T[511 - 2 * lane], a = T[512 - 2 * lane];
    const u32 bb = pk(b, b), cc = pk(c, c);
    *(u32x4*)(k0 + 2048 + 4576) = u32x4{pk(a, a), bb, pk(b, c), cc};
    *(u32x4*)(k0 + 2048) = u32x4{pk(a, b), bb, cc, cc};
  } else {
    const bool lo = lane >= 32;
    const int q8 = 504 - 8 * lane, base = lo ? q8 : 128 + (q8 >> 1);
    const u32x2 w01 = *(const u32x2*)(T + base), w23 = *(const u32x2*)(T + base + 4);
    const u32 y = T[lane == 63 ? 641 : (lane >= 31 ? q8 - 1 : base)];
    auto pm = [](u32 h, u32 l, u32 s) { return __builtin_amdgcn_perm(h, l, s); };
    const u32 x21 = pm(w01[1], w01[0], 0x03020504u), x43 = pm(w23[0], w01[1], 0x03020504u), x10 = pm(0u, w01[0], 0x01000302u);
    const u32 x32 = pm(0u, w01[1], 0x01000302u), x22 = pm(0u, w01[1], 0x01000100u), x0y = pm(y, w01[0], 0x05040100u);
    *(u32x4*)(k0 + 2048 + 4576) = lo ? u32x4{pm(0u, w23[1], 0x01000302u), pm(0u, w23[0], 0x01000302u), x32, x10}
                                     : u32x4{x43, x22, x21, pm(0u, w01[0], 0x01000100u)};
    *(u32x4*)(k0 + 2048) = lo ? u32x4{pm(w23[1], w23[0], 0x03020504u), x43, x21, x0y} : u32x4{x32, x22, x10, x0y};
  }
}

template <class F0>
DI void ln1_unit(int t0, int lane, const u16* __restrict__ x, rsrc_t rs, char* buf, F0&& mid) {
  const int u = lane >> 3, j = lane & 7;
  const u32x4* xa = (const u32x4*)(x + (size_t)(t0 + u) * D_);
  u32x4 A[10];
#pragma unroll
  for (int q = 0; q < 10; ++q) A[q] = xa[j + 8 * q];
  __builtin_amdgcn_sched_barrier(0);
  const u32 tag = mid();
#pragma unroll
  for (int q = 0; q < 10; ++q) *(u32x4*)(buf + u * 1344 + (j + 8 * q) * 16) = A[q];
  wave_sync();
  {
    const char* rd = buf + ((lane >> 2) & 3) * 1344 + (lane >> 4) * 32 + (lane & 3) * 8;
#pragma unroll
    for (int q = 0; q < 10; ++q)
      st_sc1(rs, lane * 16, (u32)WS_XB + t0 * (D_ * 2) + q * 1024, __builtin_bit_cast(u32x4, cat8(tr_read(rd + q * 128), tr_read(rd + 4 * 1344 + q * 128))));
  }
  float sa = 0.f, sb = 0.f, qa = 0.f, qb = 0.f;
#pragma unroll
  for (int q = 0; q < 5; ++q)
#pragma unroll
    for (int k = 0; k < 4; ++k) {
      sa = dot2bf(A[q][k], 0x3f803f80u, sa); qa = dot2bf(A[q][k], A[q][k], qa);
      sb = dot2bf(A[5 + q][k], 0x3f803f80u, sb); qb = dot2bf(A[5 + q][k], A[5 + q][k], qb);
    }
  sa += sb; qa += qb;
#pragma unroll
  for (int o = 1; o < 8; o <<= 1) { sa += __shfl_xor(sa, o, 8); qa += __shfl_xor(qa, o, 8); }
  const float ma = sa * (1.f / D_);
  const float va = fmaxf(qa - sa * ma, 0.f);
  const u32 s0 = cvtpk(ma, sqrtf(rb(rb(va * (1.f / D_)) + 1e-5f)));
  asm volatile("s_waitcnt vmcnt(0)" ::: "memory");
  if (j == 0) __builtin_amdgcn_raw_buffer_store_b64(u32x2{s0, tag}, rs, (u32)WS_NT + (t0 + u) * 8, 0, 16);
}

template <class F0>
DI void mod_unit(int un, int lane, const u16* __restrict__ c, const u16* __restrict__ wm, rsrc_t rs, F0&& mid) {
  const int cbk = un % 60, ks = un / 60, cj = lane & 7, rg = lane >> 3;
  const u32 cr0 = *(const u32*)(c + 160 * ks + 2 * lane), cr1 = *(const u32*)(c + 160 * ks + 128 + 2 * (lane & 15));
  u32x4 v[20];
#pragma unroll
  for (int i = 0; i < 20; ++i) v[i] = *(const u32x4*)(wm + (size_t)(160 * ks + 2 * rg + (i & 1) + 16 * (i >> 1)) * (6 * D_) + 64 * cbk + 8 * cj);
  __builtin_amdgcn_sched_barrier(0);
  const u32 tag = mid();
  auto silu = [](float cv) { return cv / (1.f + __expf(-cv)); };
  const u32 s0 = cvtpk(silu(lo2f(cr0)), silu(hi2f(cr0))), s1 = cvtpk(silu(lo2f(cr1)), silu(hi2f(cr1)));
  float a[8];
#pragma unroll
  for (int k = 0; k < 8; ++k) a[k] = 0.f;
#pragma unroll
  for (int i = 0; i < 10; ++i) {
    const u32 sv = __shfl(i < 8 ? s0 : s1, (rg + 8 * i) & 63);
#pragma unroll
    for (int k = 0; k < 4; ++k) {
      a[2 * k] = dot2bf(__builtin_amdgcn_perm(v[2 * i + 1][k], v[2 * i][k], 0x05040100u), sv, a[2 * k]);
      a[2 * k + 1] = dot2bf(__builtin_amdgcn_perm(v[2 * i + 1][k], v[2 * i][k], 0x07060302u), sv, a[2 * k + 1]);
    }
  }
#pragma unroll
  for (int k = 0; k < 8; ++k) { a[k] += __shfl_xor(a[k], 8); a[k] += __shfl_xor(a[k], 16); a[k] += __shfl_xor(a[k], 32); }
  const float e0 = (rg & 2) ? ((rg & 1) ? a[6] : a[4]) : ((rg & 1) ? a[2] : a[0]);
  const float e1 = (rg & 2) ? ((rg & 1) ? a[7] : a[5]) : ((rg & 1) ? a[3] : a[1]);
  const u32 col = ks * (6 * D_) + 64 * cbk + 8 * cj + 2 * (rg & 3);
  if (lane < 32) st_sc1(rs, col * 8, (u32)WS_XT, u32x4{__float_as_uint(e0), tag, __float_as_uint(e1), tag});
  else __builtin_amdgcn_raw_buffer_store_b64(u32x2{__float_as_uint(e0), __float_as_uint(e1)}, rs, (u32)WS_MODP + col * 4, 0, 16);
}

template <class F0>
DI void w1_tile(int un, int lane, const u16* __restrict__ w1, rsrc_t rs, char* buf, F0&& mid) {
  const bool rem = un >= 260;
  const int ks = rem ? 2 * (un - 260) : un / 13, nb = rem ? 13 : un % 13;
  const int g = lane >> 4, lc = lane & 15, q = lc >> 2, p = lane & 3;
  const u16* src = w1 + (size_t)(32 * ks + g + (rem ? 4 * (lc & 8) : 0)) * F_ + 128 * nb + 8 * (rem ? lc & 7 : lc);
  u32x4 v[8];
  mid();
  __builtin_amdgcn_s_sleep(16);
  __builtin_amdgcn_sched_barrier(0);
#pragma unroll
  for (int i = 0; i < 8; ++i) v[i] = *(const u32x4*)(src + (size_t)(4 * i) * F_);
  __builtin_amdgcn_sched_barrier(0);
#pragma unroll
  for (int i = 0; i < 8; ++i) {
    const int row = 4 * i + g;
    *(u32x4*)(buf + row * 256 + (((lc >> 1) ^ rl_swz(row)) << 5) + ((lc & 1) << 4)) = v[i];
  }
  wave_sync();
  const int row0 = 8 * g + q, row1 = row0 + 4;
#pragma unroll
  for (int cb = 0; cb < 8; ++cb) {
    const s16x4 lo = tr_read(buf + row0 * 256 + ((cb ^ rl_swz(row0)) << 5) + 8 * p);
    const s16x4 hi = tr_read(buf + row1 * 256 + ((cb ^ rl_swz(row1)) << 5) + 8 * p);
    const int blk = (8 * nb + cb) * 20 + ks - (rem && cb >= 4 ? 79 : 0);
    st_sc1(rs, lane * 16, (u32)WS_W1F + blk * 1024, __builtin_bit_cast(u32x4, cat8(lo, hi)));
  }
}

DI void conv_stage_h(char* smem, const u32 (&xp)[2], const float (&mn)[4], const float (&rs)[4], float ca1, float sa, int tid) {
  u32 h[2];
#pragma unroll
  for (int k = 0; k < 2; ++k) {
    const u32 c = cvtpk(lo2f(xp[k]) - mn[2 * k], hi2f(xp[k]) - mn[2 * k + 1]);
    const u32 n = cvtpk(lo2f(c) * rs[2 * k], hi2f(c) * rs[2 * k + 1]);
    const u32 p = cvtpk(lo2f(n) * ca1, hi2f(n) * ca1);
    h[k] = cvtpk(lo2f(p) + sa, hi2f(p) + sa);
  }
  char* yo = smem + tid * 8;
  *(u32*)(yo + CV_H0 + 60) = h[0]; *(u32*)(yo + CV_H0 + 64) = h[1];
  *(u32x2*)(yo + CV_H0 + CV_HS + 56) = u32x2{h[0], h[1]};
  *(u32*)(yo + CV_H0 + 2 * CV_HS + 52) = h[0]; *(u32*)(yo + CV_H0 + 2 * CV_HS + 56) = h[1];
  *(u32x2*)(yo + CV_H3 + 48) = u32x2{h[0], h[1]};
  if (tid < 64) {
    const int cc = tid >> 4, yc = tid & 15;
    if (yc < 15 - cc) *(u32*)(smem + (cc == 3 ? CV_H3 : CV_H0 + cc * CV_HS) + yc * 4) = 0u;
  }
}

template <class CB>
DI void conv_mma(const char* smem, int w, int lane, f32x4 (&acc)[8], CB&& side) {
  const int r16 = lane & 15, g = lane >> 4, cpy = r16 & 3;
  const char* hb = smem + (cpy == 3 ? CV_H3 : CV_H0 + cpy * CV_HS) + 16 * ((r16 >> 2) + 4 * w + g);
  const char* kb = smem + CV_K + (r16 & 1) * 4576 + 16 * (252 + 4 * w - 4 * (r16 >> 1) + g) - 256 * 14;
#pragma unroll
  for (int i = 0; i < 8; ++i) acc[i] = f32x4{0.f, 0.f, 0.f, 0.f};
#pragma unroll
  for (int par = 1; par >= 0; --par) {
    bf16x8 Bf[8];
#pragma unroll
    for (int t = 0; t < 8; ++t) Bf[t] = *(const bf16x8*)(hb + 256 * (2 * t + par));
#pragma unroll
    for (int sg = -par; sg <= 14; sg += 2) {
      const bf16x8 Af = *(const bf16x8*)(kb + 256 * (14 - sg));
#pragma unroll
      for (int i = 0; i < 8; ++i) {
        const int tau = 2 * i - sg;
        if (tau >= 0 && tau <= 15) acc[i] = mfma16(Af, Bf[tau >> 1], acc[i]);
      }
      side((1 - par) * 8 + ((sg + par) >> 1));
    }
  }
}

DI void conv_mma_half(const char* smem, int w, int par, int lane, f32x4 (&acc)[8]) {
  const int r16 = lane & 15, g = lane >> 4, cpy = r16 & 3;
  const char* hb = smem + (cpy == 3 ? CV_H3 : CV_H0 + cpy * CV_HS) + 16 * ((r16 >> 2) + 4 * w + g) + 256 * par;
  const char* kb = smem + CV_K + (r16 & 1) * 4576 + 16 * (252 + 4 * w - 4 * (r16 >> 1) + g) - 256 * 14 + 256 * par;
  bf16x8 Bf[8];
#pragma unroll
  for (int t = 0; t < 8; ++t) { Bf[t] = *(const bf16x8*)(hb + 512 * t); acc[t] = f32x4{0.f, 0.f, 0.f, 0.f}; }
#pragma unroll
  for (int s = 0; s < 8; ++s) {
    const bf16x8 Af = *(const bf16x8*)(kb + 256 * (14 - 2 * s));
#pragma unroll
    for (int i = s; i < 8; ++i) acc[i] = mfma16(Af, Bf[i - s], acc[i]);
  }
}

DI void conv_put(char* smem, int w, int lane, const f32x4 (&acc)[8]) {
  char* pb = smem + w * 8192 + (64 * (lane >> 4) + 2 * (lane & 15)) * 4;
#pragma unroll
  for (int i = 0; i < 8; ++i) {
    *(f32x2*)(pb + i * 1024) = f32x2{acc[i][0], acc[i][1]};
    *(f32x2*)(pb + i * 1024 + 128) = f32x2{acc[i][2], acc[i][3]};
  }
}

DI u32x2 conv_fin(const char* smem, const char* more, int t, const u32 (&xv)[2], float dsk, float ga, float c1, u32x2& os) {
  f32x4 y = *(const f32x4*)(smem + t * 16);
#pragma unroll
  for (int w = 1; w < 4; ++w) y += *(const f32x4*)(smem + w * 8192 + t * 16);
  if (more) {
#pragma unroll
    for (int w = 0; w < 4; ++w) y += *(const f32x4*)(more + w * 8192 + t * 16);
  }
  const u32x2 hv = *(const u32x2*)(smem + CV_H3 + 48 + t * 8);
  u32x2 o;
#pragma unroll
  for (int k = 0; k < 2; ++k) {
    const u32 yr = cvtpk(y[2 * k], y[2 * k + 1]);
    const u32 hd = cvtpk(lo2f(hv[k]) * dsk, hi2f(hv[k]) * dsk);
    const u32 v = cvtpk(lo2f(yr) + lo2f(hd), hi2f(yr) + hi2f(hd));
    const u32 gv = cvtpk(lo2f(v) * ga, hi2f(v) * ga);
    o[k] = cvtpk(lo2f(xv[k]) + lo2f(gv), hi2f(xv[k]) + hi2f(gv));
    os[k] = cvtpk(lo2f(o[k]) * c1, hi2f(o[k]) * c1);
  }
  return o;
}

template <int NW>
DI void poll_words(const u32* cnt, u32 target, int lane) {
#pragma unroll 1
  for (int spins = 0; spins < (1 << 20); ++spins) {
    u32 v = target;
    if (lane < NW) v = __hip_atomic_load(cnt + lane * 64, __ATOMIC_RELAXED, __HIP_MEMORY_SCOPE_AGENT);
    if (__all(v >= target)) break;
    __builtin_amdgcn_s_sleep(1);
  }
  asm volatile("" ::: "memory");
}

constexpr int W1_RING = 8;
constexpr int BB_RING = 6;
constexpr int GO_CHK = 4, GO_IT = 5;

__global__ __launch_bounds__(512, 2) void k_main(const u16* __restrict__ x, const u16* __restrict__ Are, const u16* __restrict__ Aim,
                                                    const u16* __restrict__ Bre, const u16* __restrict__ Bim, const u16* __restrict__ Cre,
                                                    const u16* __restrict__ Cim, const u16* __restrict__ ldt, const u16* __restrict__ Dsk,
                                                    const u16* __restrict__ w1, const u16* __restrict__ w2, const u16* __restrict__ cvec,
                                                    const u16* __restrict__ wm, char* __restrict__ ws, u16* __restrict__ out) {
  __shared__ __attribute__((aligned(256))) char smem[MN_SMEM];
  asm volatile("" ::"s"(x), "s"(Are), "s"(Aim), "s"(Bre), "s"(Bim), "s"(Cre), "s"(Cim), "s"(ldt));
  asm volatile("" ::"s"(Dsk), "s"(w1), "s"(w2), "s"(cvec), "s"(wm), "s"(ws), "s"(out));
  const int tid = threadIdx.x, wv = __builtin_amdgcn_readfirstlane(tid >> 6), lane = tid & 63;
  const int r16 = lane & 15, g = lane >> 4, q = r16 >> 2, p = lane & 3;
  const int b = blockIdx.x, hw = wv >> 2, w4 = wv & 3;
  const rsrc_t rs = __builtin_amdgcn_make_buffer_rsrc((void*)ws, 0, 0x7fffffff, 0x00020000);
  u32* cnt = (u32*)(ws + WS_CNT);
  const int idx = b >> 3, tt = ((b & 7) >> 1) * 8 + (idx & 7), sl = (b & 1) * 4 + (idx >> 3);
  const int pbase = 7 * sl - (sl >> 2), pcnt = (sl & 3) == 3 ? 6 : 7;
  const bool active = wv < pcnt;
  const int p0 = pbase + (active ? wv : 0);

  const bool three = idx < 16;
  const int d0 = 80 * (b & 7) + (three ? 3 * idx : 16 + 2 * idx);
  const u32 l16 = lane * 16;
  u32* pcs = (u32*)(smem + MN_PC);
  u32* fl = (u32*)(ws + WS_FL);
  u32 tag;
  bf16x8 af[20][2];
  const rsrc_t rw = __builtin_amdgcn_make_buffer_rsrc((void*)ws, 0, active ? 0x7fffffff : 0, 0x00020000);
  const u32 w1o = (u32)WS_W1F + (u32)(2 * p0) * 20 * 1024;
  {
    if (tid < 8 && tid != 4) pcs[tid] = 0u;
    u32x4 cw0, cw1;
    if (wv == 7) {
      unsigned long long pc; u32 len;
      asm volatile("s_getpc_b64 %0\n.Lcw_here:\n\ts_mov_b32 %1, .Lcw_end-.Lcw_here" : "=s"(pc), "=s"(len));
      const u32 o = idx * 1280 + lane * 16;
      cw0 = *(const __attribute__((address_space(1))) u32x4*)(pc + min(o, len - 16u));
      cw1 = *(const __attribute__((address_space(1))) u32x4*)(pc + min(o + 256u, len - 16u));
    }
    u32x4 sg[2];
    u32x2 mg;
    const int mj = (lane & 15) >> 2, mch = min(lane >> 4, three ? 2 : 1);
    const u16 dskr = Dsk[d0 + mch];
    auto poll = [&]() {
      sg[0] = ld_sc1(rs, tid * 32, (u32)WS_NT);
      sg[1] = ld_sc1(rs, tid * 32 + 16, (u32)WS_NT);
      mg = __builtin_amdgcn_raw_buffer_load_b64(rs, ((lane & 3) * (6 * D_) + (mj < 3 ? mj : 4) * D_ + d0 + mch) * 8, (u32)WS_XT, 16);
    };
    const int nkg = three ? 6 : 4;
    if (wv < nkg) {
      kgen_pair(d0 + (wv >> 1), wv & 1, lane, Are, Aim, Bre, Bim, Cre, Cim, ldt, smem + (wv >> 1) * CV_CH, smem + MN_R1 + (wv >> 1) * 8704,
                pcs + (wv >> 1), []() {}, poll);
      tag = pcs[4];
    } else {
      __builtin_amdgcn_s_setprio(3);
      auto mid = [&]() { wg_barrier(); tag = pcs[4]; return tag; };
      auto arrive = [&]() {
        u32 o = 0;
        if (lane == 0) o = __hip_atomic_fetch_add(pcs + 3, 1u, __ATOMIC_RELAXED, __HIP_MEMORY_SCOPE_WORKGROUP);
        if (lane == 0 && o == (three ? 1u : 3u)) __hip_atomic_store(fl + b, tag, __ATOMIC_RELAXED, __HIP_MEMORY_SCOPE_AGENT);
      };
      if (wv == 7) {
        const u32 ep = __hip_atomic_load(fl + 8192, __ATOMIC_RELAXED, __HIP_MEMORY_SCOPE_AGENT);
        ln1_unit(8 * b, lane, x, rs, smem + M2_X, [&]() {
          const unsigned long long h = (llvm_dispatch_id() * 0x9E3779B97F4A7C15ull) ^ ((unsigned long long)__builtin_amdgcn_dispatch_ptr() * 0xC2B2AE3D27D4EB4Full);
          tag = ep + 1u + ((u32)(h >> 33) & 0x3fffffffu);
          if (lane == 0) pcs[4] = tag;
          wg_barrier();
          return tag;
        });
        __builtin_amdgcn_raw_buffer_store_b64(u32x2{0u, 0u}, rs, lane * 8, (u32)WS_ST + (tt * 8 + sl) * 512, 16);
        if (lane == 0) __hip_atomic_store(cnt + (16 + tt * 8 + sl) * 64, 0u, __ATOMIC_RELAXED, __HIP_MEMORY_SCOPE_AGENT);
        if (b < 8 && lane == 0) __hip_atomic_store(cnt + b * 64, 0u, __ATOMIC_RELAXED, __HIP_MEMORY_SCOPE_AGENT);
        asm volatile("s_waitcnt vmcnt(0)" ::: "memory");
        arrive();
      } else if (wv == 6 && b < 240) {
        mod_unit(b, lane, cvec, wm, rs, mid);
        asm volatile("s_waitcnt vmcnt(0)" ::: "memory");
        arrive();
      } else {
        const int un = wv == 6 ? b + 16 : (b - 128) * 2 + wv - 4;
        const int t2 = un < 270 ? un : 269;
        char* buf = smem + 2 * CV_CH + (wv - 4) * 8192;
        w1_tile(t2, lane, w1, rs, buf, mid);
        asm volatile("s_waitcnt vmcnt(0)" ::: "memory");
        arrive();
      }
      __builtin_amdgcn_s_setprio(0);
      poll();
    }
    if (wv == 7) asm volatile("" ::"v"(cw0), "v"(cw1));
#pragma unroll 1
    for (int spins = 0; spins < (1 << 20); ++spins) {
      if (__all(sg[0][1] == tag && sg[0][3] == tag && sg[1][1] == tag && sg[1][3] == tag && mg[1] == tag)) break;
      __builtin_amdgcn_s_sleep(2);
      poll();
    }
    asm volatile("" ::: "memory");

    u32 xp[3][2];
    {
      const int xc = min(lane & 3, three ? 2 : 1);
      char* xs = smem + (wv < 7 ? MN_R1 + wv * 4352 : M2_X);
      u32x4 xb[2];
#pragma unroll
      for (int i = 0; i < 2; ++i) xb[i] = ld_sc1(rs, (lane >> 2) * (D_ * 16) + (d0 + xc) * 16, (u32)WS_XB + (32 * wv + 16 * i) * (D_ * 16));
#pragma unroll
      for (int i = 0; i < 2; ++i) *(u32x4*)(xs + xc * 512 + (16 * i + (lane >> 2)) * 16) = xb[i];
      wave_sync();
#pragma unroll
      for (int c = 0; c < 3; ++c) { const u32x2 v = *(const u32x2*)(xs + c * 512 + lane * 8); xp[c][0] = v[0]; xp[c][1] = v[1]; }
    }
    auto ldf = [&](u32 off) { return __uint_as_float(__builtin_amdgcn_raw_buffer_load_b32(rs, off, (u32)WS_MODP, 16)); };
    u32x4 af0;
    f32x4 accA[8], accC[8];
    char* smA = smem + hw * CV_CH;
    char* smC = smem + 2 * CV_CH;
    float mp = __uint_as_float(mg[0]);
    mp += __shfl_xor(mp, 1); mp += __shfl_xor(mp, 2);
    mp = rb(mp);
    auto rl = [&](int l) { return __uint_as_float(__builtin_amdgcn_readlane(__float_as_uint(mp), l)); };
    float mn[4], rsd[4];
#pragma unroll
    for (int i = 0; i < 4; ++i) { const u32 s = sg[i >> 1][2 * (i & 1)]; mn[i] = lo2f(s); rsd[i] = __builtin_amdgcn_rcpf(hi2f(s)); }
    conv_stage_h(smem, xp[0], mn, rsd, rb(1.f + rl(4)), rl(0), tid);
    conv_stage_h(smem + CV_CH, xp[1], mn, rsd, rb(1.f + rl(20)), rl(16), tid);
    if (three) conv_stage_h(smC, xp[2], mn, rsd, rb(1.f + rl(36)), rl(32), tid);
    if (wv == 0) af0 = ld_sc1(rs, l16, (u32)WS_FL);
    const int t2 = (b - 128) * 3 + wv - 4;
    const bool do2 = !three && wv >= 4 && wv < 7 && t2 < 270;
    __builtin_amdgcn_sched_barrier(0);
    if (do2) {
#pragma unroll
      for (int i = 0; i < 8; ++i) accC[i] = *(const f32x4*)(w2 + (size_t)(32 * (t2 / 5) + 4 * i + g) * D_ + 128 * (t2 % 5) + 8 * r16);
    }
    const int t1 = (b - 128) * 4 + wv;
    const bool do1 = !three && wv < 4 && t1 < 432;
    u32x4 rv[5];
    f32x4 gbp;
    __syncthreads();
    conv_mma(smA, w4, lane, accA, [&](int it) {
      if (it == GO_CHK) {
        if (wv == 0) {
#pragma unroll 1
          for (int spins = 0; spins < (1 << 20); ++spins) {
            if (__all(af0[0] == tag && af0[1] == tag && af0[2] == tag && af0[3] == tag)) break;
            __builtin_amdgcn_s_sleep(1);
            af0 = ld_sc1(rs, l16, (u32)WS_FL);
          }
          if (lane == 0) __hip_atomic_store(pcs + 5, 1u, __ATOMIC_RELAXED, __HIP_MEMORY_SCOPE_WORKGROUP);
          if (b == 0 && lane == 0) __hip_atomic_store(fl + 8192, tag, __ATOMIC_RELAXED, __HIP_MEMORY_SCOPE_AGENT);
        }
      }
      if (it == GO_IT) {
        if (wv != 0) {
#pragma unroll 1
          for (int spins = 0; spins < (1 << 22); ++spins) {
            if (__hip_atomic_load(pcs + 5, __ATOMIC_RELAXED, __HIP_MEMORY_SCOPE_WORKGROUP) != 0u) break;
            __builtin_amdgcn_s_sleep(1);
          }
        }
        asm volatile("" ::: "memory");
        if (do1) {
#pragma unroll
          for (int i = 0; i < 5; ++i) rv[i] = ld_sc1(rs, l16, (u32)WS_W1F + (t1 * 5 + i) * 1024);
          const u32 mq = (160 * (t1 & 3) + 4 * (lane < 40 ? lane : 0)) * 4;
#pragma unroll
          for (int k = 0; k < KSM; ++k) {
            accC[k] = __builtin_bit_cast(f32x4, ld_sc1(rs, mq, (u32)WS_MODP + (k * (6 * D_) + 4 * D_) * 4));
            accC[4 + k] = __builtin_bit_cast(f32x4, ld_sc1(rs, mq, (u32)WS_MODP + (k * (6 * D_) + 3 * D_) * 4));
          }
        }
#pragma unroll
        for (int k = 0; k < KSM; ++k) gbp[k] = ldf((k * (6 * D_) + 5 * D_ + sl * 80 + (tid < 80 ? tid : 0)) * 4);
      }
      if (it > GO_IT && it <= GO_IT + W1_RING) {
        const int ks = it - GO_IT - 1;
#pragma unroll
        for (int fb = 0; fb < 2; ++fb) af[ks][fb] = ld_frag1(rw, l16, w1o + (fb * 20 + ks) * 1024);
      }
    });
    if (three) conv_mma_half(smC, w4, hw, lane, accC);
    if (tid < 80) ((float*)(smem + MN_GB))[tid] = rb((gbp[0] + gbp[1]) + (gbp[2] + gbp[3]));
    __syncthreads();
    conv_put(smA, w4, lane, accA);
    if (three) conv_put(hw ? smem + MN_R1 : smC, w4, lane, accC);
    __syncthreads();
    const float dsk = bf2f(dskr);
#pragma unroll
    for (int c = 0; c < 3; ++c) {
      if (c < 2 || three) {
        u32x2 os;
        const u32x2 o = conv_fin(smem + c * CV_CH, c == 2 ? smem + MN_R1 : nullptr, tid, xp[c], __uint_as_float(__builtin_amdgcn_readlane(__float_as_uint(dsk), 16 * c)), rl(16 * c + 8),
                                 rb(1.f + rl(16 * c + 12)), os);
        __builtin_amdgcn_raw_buffer_store_b64(o, rs, tid * 8 + (u32)WS_X1T + (d0 + c) * (L_ * 2), 0, 16);
        __builtin_amdgcn_raw_buffer_store_b64(os, rs, tid * 8 + (u32)WS_X1S + (d0 + c) * (L_ * 2), 0, 16);
      }
    }
    if (do2) {
      char* buf = smC + 4096 + (wv - 4) * 8192;
#pragma unroll
      for (int i = 0; i < 8; ++i) {
        const int row = 4 * i + g;
        *(f32x4*)(buf + row * 256 + (((r16 >> 1) ^ rl_swz(row)) << 5) + ((r16 & 1) << 4)) = accC[i];
      }
      wave_sync();
      const int row0 = 4 * g + q, row1 = row0 + 16;
#pragma unroll
      for (int cb = 0; cb < 8; ++cb) {
        const s16x4 lo = tr_read(buf + row0 * 256 + ((cb ^ rl_swz(row0)) << 5) + 8 * p);
        const s16x4 hi = tr_read(buf + row1 * 256 + ((cb ^ rl_swz(row1)) << 5) + 8 * p);
        st_sc1(rs, l16, (u32)WS_W2F + ((8 * (t2 % 5) + cb) * 54 + t2 / 5) * 1024, __builtin_bit_cast(u32x4, cat8(lo, hi)));
      }
    }
    if (do1) {
      char* buf = smC + wv * 1024;
      const f32x4 mc = (accC[0] + accC[1]) + (accC[2] + accC[3]), ms = (accC[4] + accC[5]) + (accC[6] + accC[7]);
      if (lane < 40) {
        *(u32x2*)(buf + lane * 8) = u32x2{cvtpk(1.f + rb(mc[0]), 1.f + rb(mc[1])), cvtpk(1.f + rb(mc[2]), 1.f + rb(mc[3]))};
        *(u32x2*)(buf + 512 + lane * 8) = u32x2{cvtpk(ms[0], ms[1]), cvtpk(ms[2], ms[3])};
      }
      wave_sync();
      float S = 0.f, B = 0.f;
#pragma unroll
      for (int i = 0; i < 5; ++i) {
        const u32x4 c1p = *(const u32x4*)(buf + i * 64 + g * 16), sbp = *(const u32x4*)(buf + 512 + i * 64 + g * 16);
#pragma unroll
        for (int k = 0; k < 4; ++k) { S = dot2bf(rv[i][k], c1p[k], S); B = dot2bf(rv[i][k], sbp[k], B); }
      }
      S += __shfl_xor(S, 16); S += __shfl_xor(S, 32);
      B += __shfl_xor(B, 16); B += __shfl_xor(B, 32);
      if (lane < 32)
        __builtin_amdgcn_raw_buffer_store_b32(__float_as_uint(lane < 16 ? S : B), rs, (g * 4 * F_ + r16) * 4, (u32)WS_SB + ((t1 & 3) * F_ + 16 * (t1 >> 2)) * 4, 16);
    }
  }

  const int k0 = 7 * wv - (wv >> 2), nk = (wv & 3) == 3 ? 6 : 7;
  const u32 w2o = (u32)WS_W2F + (u32)((sl * 5) * 54 + k0) * 1024;
  bf16x8 wf[7][5];
  auto cbof = [&](int c) { return hw ? (c + 3) % 5 : c; };
  auto ldw2 = [&](int f) { wf[f / 5][f % 5] = ld_frag1(rs, l16, w2o + (cbof(f % 5) * 54 + (f < 30 || nk == 7 ? f / 5 : 5)) * 1024); };
  {
    const int l8 = lane & 7, crow = 80 * wv + (lane >> 3);
    u32x4 xv[10];
    auto tile_ld = [&]() {
#pragma unroll
      for (int i = 0; i < 10; ++i) xv[i] = ld_sc1(rs, crow * (L_ * 2) + l8 * 16, (u32)WS_X1S + i * (8 * L_ * 2) + tt * 128);
    };
    auto tile_st = [&]() {
#pragma unroll
      for (int i = 0; i < 10; ++i) {
        const int c = crow + 8 * i;
        *(u32x4*)(smem + M1_A + c * 128 + (((l8 >> 1) ^ h2_swz(c)) << 5) + ((l8 & 1) << 4)) = xv[i];
      }
      wg_barrier();
    };
    f32x4 acc[2][4];
    auto zero_acc = [&]() {
#pragma unroll
      for (int fb = 0; fb < 2; ++fb)
#pragma unroll
        for (int tb = 0; tb < 4; ++tb) acc[fb][tb] = f32x4{0.f, 0.f, 0.f, 0.f};
    };
    float* st = (float*)(smem + M1_ST);
    if (wv < 7) {
      asm volatile("s_waitcnt vmcnt(0)" ::: "memory");
      wg_barrier();
      __builtin_amdgcn_sched_barrier(0);
#pragma unroll
      for (int ks = W1_RING; ks < 20; ++ks)
#pragma unroll
        for (int fb = 0; fb < 2; ++fb) af[ks][fb] = ld_frag1(rw, l16, w1o + (fb * 20 + ks) * 1024);
      __builtin_amdgcn_sched_barrier(0);
      wg_barrier();
      tile_ld();
      __builtin_amdgcn_sched_barrier(0);
      tile_st();
      const int row0 = 8 * g + q, sw = h2_swz(row0);
      const char* rb0 = smem + M1_A + row0 * 128 + 8 * p;
      auto ldb = [&](int s) {
        const s16x4 lo = tr_read(rb0 + (s >> 2) * 4096 + (((s & 3) ^ sw) << 5));
        const s16x4 hi = tr_read(rb0 + (s >> 2) * 4096 + 512 + (((s & 3) ^ sw) << 5));
        return cat8(lo, hi);
      };
      f32x4 sb4;
      bf16x8 bb[BB_RING];
#pragma unroll
      for (int s = 0; s < BB_RING; ++s) bb[s] = ldb(s);
      zero_acc();
#pragma unroll
      for (int ks = 0; ks < 20; ++ks) {
#pragma unroll
        for (int tb = 0; tb < 4; ++tb) {
          const int s = 4 * ks + tb;
#pragma unroll
          for (int fb = 0; fb < 2; ++fb) acc[fb][tb] = mfma16(af[ks][fb], bb[s % BB_RING], acc[fb][tb]);
          if (s + BB_RING < 80) bb[s % BB_RING] = ldb(s + BB_RING);
          __builtin_amdgcn_sched_barrier(0);
        }
#pragma unroll
        for (int f = (ks * 35) / 20; f < ((ks + 1) * 35) / 20; ++f) ldw2(f);
        if (ks == 2) {
#pragma unroll
          for (int e = 0; e < 4; ++e)
            sb4[e] = __uint_as_float(__builtin_amdgcn_raw_buffer_load_b32(rs, ((lane >> 5) * 4 * F_ + (lane & 31)) * 4, (u32)WS_SB + (e * F_ + 32 * p0) * 4, 16));
        }
        if (ks == 17) {
          *(float*)(smem + MN_SB + wv * 256 + lane * 4) = (sb4[0] + sb4[1]) + (sb4[2] + sb4[3]);
        }
        __builtin_amdgcn_sched_barrier(0);
      }
    } else {
      __builtin_amdgcn_s_setprio(3);
      asm volatile("s_waitcnt vmcnt(0)" ::: "memory");
      wg_barrier();
      u32 o = 0;
      if (lane == 0) o = __hip_atomic_fetch_add(cnt + (b & 7) * 64, 1u, __ATOMIC_RELAXED, __HIP_MEMORY_SCOPE_AGENT);
      o = __builtin_amdgcn_readfirstlane(o);
      if (o == 31u && lane < 8) __hip_atomic_store(cnt + (8 + lane) * 64 + (b & 7), tag, __ATOMIC_RELAXED, __HIP_MEMORY_SCOPE_AGENT);
      for (int spins = 0; spins < (1 << 20); ++spins) {
        u32 v = tag;
        if (lane < 8) v = __hip_atomic_load(cnt + (8 + (b & 7)) * 64 + lane, __ATOMIC_RELAXED, __HIP_MEMORY_SCOPE_AGENT);
        if (__all(v == tag)) break;
        __builtin_amdgcn_s_sleep(1);
      }
      wg_barrier();
      tile_ld();
      u32x4 xr[10];
#pragma unroll
      for (int i = 0; i < 10; ++i) xr[i] = ld_sc1(rs, (lane >> 3) * (L_ * 2) + (lane & 7) * 16, (u32)WS_X1T + (sl * 80 + 8 * i) * (L_ * 2) + tt * 128);
      __builtin_amdgcn_sched_barrier(0);
      tile_st();
      zero_acc();
#pragma unroll
      for (int i = 0; i < 10; ++i) {
        const u32x4 v = xr[i];
        const f32x4 a0 = {lo2f(v[0]), hi2f(v[0]), lo2f(v[1]), hi2f(v[1])}, a1 = {lo2f(v[2]), hi2f(v[2]), lo2f(v[3]), hi2f(v[3])};
        acc[0][0] += a0; acc[0][1] += a1; acc[0][2] += a0 * a0; acc[0][3] += a1 * a1;
      }
      const bool b5 = lane & 32, b4 = lane & 16, b3 = lane & 8;
      float v4[2][4], v2[2][2], v1[2];
#pragma unroll
      for (int h = 0; h < 2; ++h) {
#pragma unroll
        for (int j = 0; j < 4; ++j) {
          const float lo = acc[0][2 * h][j], hi = acc[0][2 * h + 1][j];
          v4[h][j] = (b5 ? hi : lo) + __shfl_xor(b5 ? lo : hi, 32);
        }
#pragma unroll
        for (int j = 0; j < 2; ++j) v2[h][j] = (b4 ? v4[h][2 + j] : v4[h][j]) + __shfl_xor(b4 ? v4[h][j] : v4[h][2 + j], 16);
        v1[h] = (b3 ? v2[h][1] : v2[h][0]) + __shfl_xor(b3 ? v2[h][0] : v2[h][1], 8);
      }
      const u32 toff = (8 * (lane & 7) + (lane >> 3)) * 8;
      __builtin_amdgcn_raw_buffer_store_b64(u32x2{__float_as_uint(v1[0]), __float_as_uint(v1[1]) | 0x80000000u}, rs, toff,
                                            (u32)WS_ST + (tt * 8 + sl) * 512, 16);
#pragma unroll
      for (int i = 0; i < 10; ++i) {
        const int c = 8 * i + (lane >> 3);
        *(u32x4*)(smem + M2_X + c * 128 + ((((lane & 7) >> 1) ^ ((c >> 1) & 3)) << 5) + ((lane & 1) << 4)) = xr[i];
      }
      u32x2 gv[8];
#pragma unroll 1
      for (int spins = 0; spins < (1 << 20); ++spins) {
        u32 ok = 0x80000000u;
#pragma unroll
        for (int j = 0; j < 8; ++j) { gv[j] = __builtin_amdgcn_raw_buffer_load_b64(rs, lane * 8, (u32)WS_ST + (tt * 8 + j) * 512, 16); ok &= gv[j][1]; }
        if (__all(ok != 0u)) break;
        __builtin_amdgcn_s_sleep(2);
      }
      float sm = 0.f, sq = 0.f;
#pragma unroll
      for (int j = 0; j < 8; ++j) { sm += __uint_as_float(gv[j][0]); sq += __uint_as_float(gv[j][1] & 0x7fffffffu); }
      const float mean = sm * (1.f / D_);
      const float var = fmaxf(sq * (1.f / D_) - mean * mean, 0.f);
      st[lane] = rb(mean);
      st[64 + lane] = 1.f / rb(sqrtf(rb(rb(var) + 1e-5f)));
      __builtin_amdgcn_fence(__ATOMIC_RELEASE, "workgroup");
      if (lane == 0) __hip_atomic_store(pcs + 6, 1u, __ATOMIC_RELAXED, __HIP_MEMORY_SCOPE_WORKGROUP);
      __builtin_amdgcn_s_setprio(0);
    }
    asm volatile("" ::: "memory");
    if (wv == 7) {
#pragma unroll
      for (int f = 0; f < 35; ++f) ldw2(f);
    }
    __builtin_amdgcn_sched_barrier(0);
    if (active) {
#pragma unroll 1
      for (int spins = 0; spins < (1 << 22); ++spins) {
        if (__hip_atomic_load(pcs + 6, __ATOMIC_RELAXED, __HIP_MEMORY_SCOPE_WORKGROUP) != 0u) break;
        __builtin_amdgcn_s_sleep(1);
      }
      __builtin_amdgcn_fence(__ATOMIC_ACQUIRE, "workgroup");
      const float* sbp = (const float*)(smem + MN_SB) + wv * 64 + 4 * g;
      const u32 hoff = (u32)WS_HID + (u32)((tt * 4) * 54 + p0) * 1024;
#pragma unroll
      for (int tb = 0; tb < 4; ++tb) {
        const float mb = st[tb * 16 + r16], rsd = st[64 + tb * 16 + r16];
        u32x4 v;
#pragma unroll
        for (int hh = 0; hh < 2; ++hh) {
          const f32x4 sv = *(const f32x4*)(sbp + 16 * hh), bv = *(const f32x4*)(sbp + 32 + 16 * hh);
          const f32x4 a = (acc[hh][tb] - mb * sv) * rsd + bv;
          v[2 * hh] = cvtpk(fmaxf(a[0], 0.f), fmaxf(a[1], 0.f));
          v[2 * hh + 1] = cvtpk(fmaxf(a[2], 0.f), fmaxf(a[3], 0.f));
        }
        st_sc1(rs, l16, hoff + tb * 54 * 1024, v);
      }
      asm volatile("s_waitcnt vmcnt(0)" ::: "memory");
      u32 o = 0;
      if (lane == 0) o = __hip_atomic_fetch_add(pcs + 7, 1u, __ATOMIC_RELAXED, __HIP_MEMORY_SCOPE_WORKGROUP);
      if (lane == 0 && o == (u32)pcnt - 1u) __hip_atomic_store(cnt + (16 + tt * 8 + sl) * 64, 1u, __ATOMIC_RELAXED, __HIP_MEMORY_SCOPE_AGENT);
    }
  }
  __syncthreads();
  __builtin_amdgcn_sched_barrier(0);
  poll_words<1>(cnt + (16 + tt * 8 + wv) * 64, 1u, lane);

  {
    const u32 hoff = (u32)WS_HID + (u32)((tt * 4) * 54 + k0) * 1024;
    u32x4 hf[2][4];
#pragma unroll
    for (int i = 0; i < 2; ++i)
#pragma unroll
      for (int tb = 0; tb < 4; ++tb) hf[i][tb] = ld_sc1(rs, l16, hoff + ((tb ^ w4) * 54 + i) * 1024);
    __builtin_amdgcn_sched_barrier(0);
    f32x4 acc[5][4];
#pragma unroll
    for (int cb = 0; cb < 5; ++cb)
#pragma unroll
      for (int tb = 0; tb < 4; ++tb) acc[cb][tb] = f32x4{0.f, 0.f, 0.f, 0.f};
#pragma unroll
    for (int i = 0; i < 7; ++i) {
      if (i < 6 || nk == 7) {
#pragma unroll
        for (int tb = 0; tb < 4; ++tb)
#pragma unroll
          for (int cb = 0; cb < 5; ++cb) acc[cb][tb] = mfma16(wf[i][cb], __builtin_bit_cast(bf16x8, hf[i & 1][tb]), acc[cb][tb]);
      }
      __builtin_amdgcn_sched_barrier(0);
      if (i + 2 < 7) {
        const int kk = (i + 2 < 6 || nk == 7) ? i + 2 : 5;
#pragma unroll
        for (int tb = 0; tb < 4; ++tb) hf[i & 1][tb] = ld_sc1(rs, l16, hoff + ((tb ^ w4) * 54 + kk) * 1024);
        __builtin_amdgcn_sched_barrier(0);
      }
    }
#pragma unroll
    for (int t = 0; t < 4; ++t)
#pragma unroll
      for (int c = 0; c < 5; ++c) {
        const int tb = t ^ w4, cb = cbof(c), owner = tb + (cb < 3 ? 0 : 4), slot = wv < owner ? wv : wv - 1;
        if (t > 0 || c > 2 || (c == 2 && hw)) *(f32x4*)(smem + M2_RED + ((tb * 5 + cb) * 7 + slot) * 1024 + l16) = acc[c][t];
      }
    const f32x4 own[3] = {acc[0][0], acc[1][0], acc[2][0]};
    __syncthreads();
    const int tok = 16 * w4 + r16, cb0 = wv < 4 ? 0 : 3;
    f32x4 pr[3][7], gb4[3];
    u32x2 xr[3];
#pragma unroll
    for (int j = 0; j < 3; ++j) {
      const int cb = (j < 2 || wv < 4) ? cb0 + j : 4;
#pragma unroll
      for (int s7 = 0; s7 < 7; ++s7) pr[j][s7] = *(const f32x4*)(smem + M2_RED + (((w4 * 5 + cb) * 7 + s7) * 64 + lane) * 16);
      gb4[j] = *(const f32x4*)(smem + MN_GB + (16 * cb + 4 * g) * 4);
      xr[j] = __builtin_bit_cast(u32x2, tr_read(smem + M2_X + (16 * cb + 4 * g + q) * 128 + ((w4 ^ ((2 * g + (q >> 1)) & 3)) << 5) + 8 * p));
    }
    __builtin_amdgcn_sched_barrier(0);
#pragma unroll
    for (int j = 0; j < 3; ++j) {
      const f32x4 y = ((own[j] + pr[j][0]) + (pr[j][1] + pr[j][2])) + ((pr[j][3] + pr[j][4]) + (pr[j][5] + pr[j][6]));
      const u32 y0 = cvtpk(y[0], y[1]), y1 = cvtpk(y[2], y[3]);
      const u32 v0 = cvtpk(lo2f(y0) * gb4[j][0], hi2f(y0) * gb4[j][1]), v1 = cvtpk(lo2f(y1) * gb4[j][2], hi2f(y1) * gb4[j][3]);
      u32x2 o;
      o[0] = cvtpk(lo2f(xr[j][0]) + lo2f(v0), hi2f(xr[j][0]) + hi2f(v0));
      o[1] = cvtpk(lo2f(xr[j][1]) + lo2f(v1), hi2f(xr[j][1]) + hi2f(v1));
      if (j < 2 || wv < 4) *(u32x2*)(out + (size_t)(tt * 64 + tok) * D_ + sl * 80 + 16 * (cb0 + j) + 4 * g) = o;
    }
  }
  asm volatile(".Lcw_end:");
}

extern "C" void kernel_launch(void* const* d_in, const int* in_sizes, int n_in, void* d_out, int out_size, void* d_ws,
                              size_t ws_size, hipStream_t stream) {
  char* ws = (char*)d_ws;
  k_main<<<256, 512, 0, stream>>>(
      (const u16*)d_in[0], (const u16*)d_in[1], (const u16*)d_in[2], (const u16*)d_in[3], (const u16*)d_in[4],
      (const u16*)d_in[5], (const u16*)d_in[6], (const u16*)d_in[7], (const u16*)d_in[8], (const u16*)d_in[10],
      (const u16*)d_in[11], (const u16*)d_in[12], (const u16*)d_in[13], ws, (u16*)d_out);
}
```
